```python
import torch

def _softmax(x, axis=-1):
    return torch.softmax(x, dim=axis)
def _gelu(x):
    return torch.nn.functional.gelu(x, approximate="tanh")
def _silu(x):
    return torch.nn.functional.silu(x)
def _sigmoid(x):
    return torch.sigmoid(x)
def _layernorm(x, axis=-1, eps=1e-5):
    m = x.mean(dim=axis, keepdim=True)
    v = x.var(dim=axis, keepdim=True, unbiased=False)
    return (x - m) / torch.sqrt(v + eps)
def _scatter_add(tgt, idx, src, axis=0):
    return tgt.clone().index_add_(axis, idx, src)
def _segment_sum(src, seg, n):
    out = torch.zeros((n,) + src.shape[1:], dtype=src.dtype)
    return out.index_add_(0, seg, src)
def _pad(x, pw):
    flat = tuple(v for pair in reversed(pw) for v in pair)
    return torch.nn.functional.pad(x, flat)

kernel_name = 'sequential_preLN__sliding_block_cached__glu_gated_swiglu__decode'

def setup_inputs():

    _w = (lambda a: (lambda t: t.to(torch.bfloat16) if t.is_floating_point() else t)(torch.as_tensor(a)))
    def _A_setup():
        B, Sq, D = 2, 1, 576
        Hq, Hkv, Sk, Dh = 32, 16, 8192, 128
        assert Hq % Hkv == 0, (Hq, Hkv)
        return {
            "x": _w((lambda *s: torch.randn(*s, dtype=torch.bfloat16) * 0.05)(B, Sq, D)),
            "k_cache": _w((lambda *s: torch.randn(*s, dtype=torch.bfloat16) * 1.6)(B, Hkv, Sk, Dh)),
            "v_cache": _w((lambda *s: torch.randn(*s, dtype=torch.bfloat16) * 0.8)(B, Hkv, Sk, Dh)),
            "wq": _w((lambda *s: torch.randn(*s, dtype=torch.bfloat16) * 1.6)(D, Hq * Dh) * (D ** -0.5)),
            "wo": _w((lambda *s: torch.randn(*s, dtype=torch.bfloat16) * 0.8)(Hq * Dh, D) * ((Hq * Dh) ** -0.5)),
        }
    _A_in = _A_setup()

    def _B_setup():
        B, S, D, F = 2, 1, 576, 4608
        return {
            "x": _w((lambda *s: torch.randn(*s, dtype=torch.bfloat16) * 0.05)(B, S, D)),
            "w1": _w((lambda *s: torch.randn(*s, dtype=torch.bfloat16) * 0.4)(D, F) * (2.0 / D) ** 0.5),
            "w3": _w((lambda *s: torch.randn(*s, dtype=torch.bfloat16) * 0.4)(D, F) * (2.0 / D) ** 0.5),
            "w2": _w((lambda *s: torch.randn(*s, dtype=torch.bfloat16) * 0.4)(F, D) * (2.0 / F) ** 0.5),
        }
    _B_in = _B_setup()

    return {
        "A_x": _A_in["x"],
        "A_k_cache": _A_in["k_cache"],
        "A_v_cache": _A_in["v_cache"],
        "A_wq": _A_in["wq"],
        "A_wo": _A_in["wo"],
        "B_x": _B_in["x"],
        "B_w1": _B_in["w1"],
        "B_w3": _B_in["w3"],
        "B_w2": _B_in["w2"],
    }

def reference(A_x, A_k_cache, A_v_cache, A_wq, A_wo, B_x, B_w1, B_w3, B_w2):
    def _A_ref(x, k_cache, v_cache, wq, wo):
        B, Sq, D = x.shape
        Hq, Dh = 32, 128
        Hkv, Sk = k_cache.shape[1], k_cache.shape[2]
        W = min(4096, max(256, Sk // 4))
        rep = Hq // Hkv
        q = (lambda x, *d: x.permute(*d))((x @ wq).reshape(B, Sq, Hq, Dh), 0, 2, 1, 3)
        k = (lambda x, n, axis: torch.repeat_interleave(x, n, dim=axis))(k_cache[:, :, Sk - W:, :], rep, axis=1)
        v = (lambda x, n, axis: torch.repeat_interleave(x, n, dim=axis))(v_cache[:, :, Sk - W:, :], rep, axis=1)
        scores = torch.einsum("bhqd,bhkd->bhqk", q, k) * (Dh ** -0.5)
        p = _softmax(scores, axis=-1)
        o = torch.einsum("bhqk,bhkd->bhqd", p, v)
        return (lambda x, *d: x.permute(*d))(o, 0, 2, 1, 3).reshape(B, Sq, Hq * Dh) @ wo

    def _B_ref(x, w1, w3, w2):
        gate = torch.einsum("bsd,df->bsf", x, w1)
        up = torch.einsum("bsd,df->bsf", x, w3)
        h = _silu(gate) * up
        out = torch.einsum("bsf,fd->bsd", h, w2)
        return out

    x = A_x
    h = x + _A_ref(_layernorm(x, axis=-1), A_k_cache, A_v_cache, A_wq, A_wo)
    h2 = h + _B_ref(_layernorm(h, axis=-1), B_w1, B_w3, B_w2)
    return h2
```

```cpp
#include <hip/hip_runtime.h>
#include <hip/hip_bf16.h>
#include <stdint.h>

typedef unsigned v4u __attribute__((ext_vector_type(4)));
typedef unsigned short u16;

constexpr int B = 2, D = 576, HQ = 32, HKV = 16, SK = 8192, DH = 128, W = 2048, F = 4608;
constexpr int QD = HQ * DH;

__device__ __forceinline__ float bflo(unsigned u) { return __uint_as_float(u << 16); }
__device__ __forceinline__ float bfhi(unsigned u) { return __uint_as_float(u & 0xffff0000u); }
__device__ __forceinline__ float bf2f(u16 u) { return __uint_as_float((unsigned)u << 16); }
__device__ __forceinline__ float rbf(float f) { return __bfloat162float(__float2bfloat16(f)); }

__device__ __forceinline__ float wave_sum(float v) {
  for (int m = 32; m >= 1; m >>= 1) v += __shfl_xor(v, m);
  return v;
}
__device__ __forceinline__ float wave_max(float v) {
  for (int m = 32; m >= 1; m >>= 1) v = fmaxf(v, __shfl_xor(v, m));
  return v;
}

__device__ void ln_rows(float* xs  , float* xn) {
  int tid = threadIdx.x, w = tid >> 6, lane = tid & 63;
  if (w < 2) {
    float s = 0;
    for (int i = lane; i < D; i += 64) s += xs[w * D + i];
    s = wave_sum(s) / D;
    float v = 0;
    for (int i = lane; i < D; i += 64) { float d = xs[w * D + i] - s; v += d * d; }
    v = wave_sum(v) / D;
    float r = rsqrtf(v + 1e-5f);
    for (int i = lane; i < D; i += 64) xn[w * D + i] = (xs[w * D + i] - s) * r;
  }
}

__global__ void __launch_bounds__(256) k_q(const u16* x, const u16* wq, float* q) {
  __shared__ float xs[2 * D], xn[2 * D], red[32][2][64];
  int tid = threadIdx.x;
  for (int i = tid; i < 2 * D; i += 256) xs[i] = bf2f(x[i]);
  __syncthreads();
  ln_rows(xs, xn);
  __syncthreads();
  int c = tid & 7, rg = tid >> 3;
  float a[2][8] = {};
  for (int r = rg; r < D; r += 32) {
    v4u wv = *(const v4u*)(wq + (size_t)r * QD + blockIdx.x * 64 + c * 8);
    float x0 = xn[r], x1 = xn[D + r];
    for (int k = 0; k < 4; ++k) {
      float lo = bflo(wv[k]), hi = bfhi(wv[k]);
      a[0][2 * k] += x0 * lo; a[0][2 * k + 1] += x0 * hi;
      a[1][2 * k] += x1 * lo; a[1][2 * k + 1] += x1 * hi;
    }
  }
  for (int b = 0; b < 2; ++b) for (int k = 0; k < 8; ++k) red[rg][b][c * 8 + k] = a[b][k];
  __syncthreads();
  if (tid < 128) {
    int b = tid >> 6, col = tid & 63; float s = 0;
    for (int g = 0; g < 32; ++g) s += red[g][b][col];
    q[b * QD + blockIdx.x * 64 + col] = s;
  }
}

__global__ void __launch_bounds__(256) k_attn(const u16* kc, const u16* vc, const float* q, float* po, float* pml) {
  __shared__ float sc[2][256], red[16][2][128], ml[2][2];
  int bid = blockIdx.x, s = bid & 7, kvh = (bid >> 3) & 15, b = bid >> 7;
  int tid = threadIdx.x, c = tid & 15, rg = tid >> 4;
  const u16* kb = kc + ((size_t)(b * HKV + kvh) * SK + (SK - W) + s * 256) * DH;
  const u16* vb = vc + ((size_t)(b * HKV + kvh) * SK + (SK - W) + s * 256) * DH;
  float qv[2][8];
  for (int h = 0; h < 2; ++h) for (int k = 0; k < 8; ++k) qv[h][k] = q[b * QD + (kvh * 2 + h) * DH + c * 8 + k];
  v4u kr[16], vr[16];
  for (int i = 0; i < 16; ++i) kr[i] = *(const v4u*)(kb + (size_t)(rg + 16 * i) * DH + c * 8);
  for (int i = 0; i < 16; ++i) vr[i] = *(const v4u*)(vb + (size_t)(rg + 16 * i) * DH + c * 8);
  for (int i = 0; i < 16; ++i) {
    float d0 = 0, d1 = 0;
    for (int k = 0; k < 4; ++k) {
      float lo = bflo(kr[i][k]), hi = bfhi(kr[i][k]);
      d0 += qv[0][2 * k] * lo + qv[0][2 * k + 1] * hi;
      d1 += qv[1][2 * k] * lo + qv[1][2 * k + 1] * hi;
    }
    for (int m = 8; m >= 1; m >>= 1) { d0 += __shfl_xor(d0, m); d1 += __shfl_xor(d1, m); }
    if (c == 0) { sc[0][rg + 16 * i] = d0 * 0.08838834764831845f; sc[1][rg + 16 * i] = d1 * 0.08838834764831845f; }
  }
  __syncthreads();
  int w = tid >> 6, lane = tid & 63;
  if (w < 2) {
    float m = -INFINITY;
    for (int i = lane; i < 256; i += 64) m = fmaxf(m, sc[w][i]);
    m = wave_max(m);
    float l = 0;
    for (int i = lane; i < 256; i += 64) { float p = __expf(sc[w][i] - m); sc[w][i] = p; l += p; }
    l = wave_sum(l);
    if (lane == 0) { ml[w][0] = m; ml[w][1] = l; }
  }
  __syncthreads();
  float a[2][8] = {};
  for (int i = 0; i < 16; ++i) {
    float p0 = sc[0][rg + 16 * i], p1 = sc[1][rg + 16 * i];
    for (int k = 0; k < 4; ++k) {
      float lo = bflo(vr[i][k]), hi = bfhi(vr[i][k]);
      a[0][2 * k] += p0 * lo; a[0][2 * k + 1] += p0 * hi;
      a[1][2 * k] += p1 * lo; a[1][2 * k + 1] += p1 * hi;
    }
  }
  for (int h = 0; h < 2; ++h) for (int k = 0; k < 8; ++k) red[rg][h][c * 8 + k] = a[h][k];
  __syncthreads();
  { int h = tid >> 7, d = tid & 127; float s2 = 0;
    for (int g = 0; g < 16; ++g) s2 += red[g][h][d];
    po[((size_t)(b * HQ + kvh * 2 + h) * 8 + s) * DH + d] = s2;
    if (d < 2) pml[((b * HQ + kvh * 2 + h) * 8 + s) * 2 + d] = ml[h][d];
  }
}

__global__ void __launch_bounds__(320) k_wo(const float* po, const float* pml, const u16* wo, float* pa) {
  __shared__ float o[2][32], red[4][2][576];
  int j = blockIdx.x, tid = threadIdx.x;
  int head = j >> 2, d0 = (j & 3) * 32;
  if (tid < 64) {
    int b = tid >> 5, d = tid & 31;
    const float* m = pml + (b * HQ + head) * 16;
    float M = -INFINITY;
    for (int s = 0; s < 8; ++s) M = fmaxf(M, m[2 * s]);
    float L = 0, acc = 0;
    for (int s = 0; s < 8; ++s) { float w = __expf(m[2 * s] - M); L += w * m[2 * s + 1]; acc += w * po[((size_t)(b * HQ + head) * 8 + s) * DH + d0 + d]; }
    o[b][d] = acc / L;
  }
  __syncthreads();
  if (tid < 288) {
    int c = tid % 72, rg = tid / 72;
    float a[2][8] = {};
    for (int r = rg; r < 32; r += 4) {
      v4u wv = *(const v4u*)(wo + (size_t)(j * 32 + r) * D + c * 8);
      float x0 = o[0][r], x1 = o[1][r];
      for (int k = 0; k < 4; ++k) {
        float lo = bflo(wv[k]), hi = bfhi(wv[k]);
        a[0][2 * k] += x0 * lo; a[0][2 * k + 1] += x0 * hi;
        a[1][2 * k] += x1 * lo; a[1][2 * k + 1] += x1 * hi;
      }
    }
    for (int b = 0; b < 2; ++b) for (int k = 0; k < 8; ++k) red[rg][b][c * 8 + k] = a[b][k];
  }
  __syncthreads();
  for (int i = tid; i < 2 * D; i += 320) {
    int b = i / D, n = i % D;
    pa[(size_t)j * 2 * D + i] = red[0][b][n] + red[1][b][n] + red[2][b][n] + red[3][b][n];
  }
}

__global__ void __launch_bounds__(256) k_red(const float* pa, int np, const u16* x, const float* hin, float* hout, __hip_bfloat16* out) {
  __shared__ float red[16][16];
  int c = threadIdx.x & 15, g = threadIdx.x >> 4, i = blockIdx.x * 16 + c;
  float s = 0;
  for (int p = g; p < np; p += 16) s += pa[(size_t)p * 2 * D + i];
  red[g][c] = s;
  __syncthreads();
  if (threadIdx.x < 16) {
    float t = 0;
    for (int gg = 0; gg < 16; ++gg) t += red[gg][c];
    if (x) hout[i] = bf2f(x[i]) + t;
    else out[i] = __float2bfloat16(hin[i] + t);
  }
}

__global__ void __launch_bounds__(256) k_mlp(const float* h, const u16* w1, const u16* w3, const u16* w2, float* pm) {
  __shared__ float xs[2 * D], xn[2 * D], red[32][2][2][32], hid[2][32], red2[3][2][576];
  int tid = threadIdx.x, j = blockIdx.x;
  for (int i = tid; i < 2 * D; i += 256) xs[i] = h[i];
  __syncthreads();
  ln_rows(xs, xn);
  __syncthreads();
  { int c = tid & 3, m = (tid >> 2) & 1, rg = tid >> 3;
    const u16* wp = m ? w3 : w1;
    float a[2][8] = {};
    for (int r = rg; r < D; r += 32) {
      v4u wv = *(const v4u*)(wp + (size_t)r * F + j * 32 + c * 8);
      float x0 = xn[r], x1 = xn[D + r];
      for (int k = 0; k < 4; ++k) {
        float lo = bflo(wv[k]), hi = bfhi(wv[k]);
        a[0][2 * k] += x0 * lo; a[0][2 * k + 1] += x0 * hi;
        a[1][2 * k] += x1 * lo; a[1][2 * k + 1] += x1 * hi;
      }
    }
    for (int b = 0; b < 2; ++b) for (int k = 0; k < 8; ++k) red[rg][m][b][c * 8 + k] = a[b][k];
  }
  __syncthreads();
  if (tid < 64) {
    int b = tid >> 5, u = tid & 31; float g = 0, up = 0;
    for (int r = 0; r < 32; ++r) { g += red[r][0][b][u]; up += red[r][1][b][u]; }
    hid[b][u] = g / (1.f + __expf(-g)) * up;
  }
  __syncthreads();
  if (tid < 216) {
    int c = tid % 72, rg = tid / 72;
    float a[2][8] = {};
    for (int r = rg; r < 32; r += 3) {
      v4u wv = *(const v4u*)(w2 + (size_t)(j * 32 + r) * D + c * 8);
      float x0 = hid[0][r], x1 = hid[1][r];
      for (int k = 0; k < 4; ++k) {
        float lo = bflo(wv[k]), hi = bfhi(wv[k]);
        a[0][2 * k] += x0 * lo; a[0][2 * k + 1] += x0 * hi;
        a[1][2 * k] += x1 * lo; a[1][2 * k + 1] += x1 * hi;
      }
    }
    for (int b = 0; b < 2; ++b) for (int k = 0; k < 8; ++k) red2[rg][b][c * 8 + k] = a[b][k];
  }
  __syncthreads();
  for (int i = tid; i < 2 * D; i += 256) {
    int b = i / D, n = i % D;
    pm[(size_t)j * 2 * D + i] = red2[0][b][n] + red2[1][b][n] + red2[2][b][n];
  }
}

extern "C" void kernel_launch(void* const* d_in, const int* in_sizes, int n_in,
                              void* d_out, int out_size, void* d_ws, size_t ws_size,
                              hipStream_t stream) {
  const u16* x = (const u16*)d_in[0];
  const u16* kc = (const u16*)d_in[1];
  const u16* vc = (const u16*)d_in[2];
  const u16* wq = (const u16*)d_in[3];
  const u16* wo = (const u16*)d_in[4];
  const u16* w1 = (const u16*)d_in[6];
  const u16* w3 = (const u16*)d_in[7];
  const u16* w2 = (const u16*)d_in[8];
  float* ws = (float*)d_ws;
  float* q = ws;
  float* po = q + 8192;
  float* pml = po + 65536;
  float* pa = pml + 1024;
  float* h = pa + 128 * 1152;
  float* pm = h + 1152;
  k_q<<<64, 256, 0, stream>>>(x, wq, q);
  k_attn<<<256, 256, 0, stream>>>(kc, vc, q, po, pml);
  k_wo<<<128, 320, 0, stream>>>(po, pml, wo, pa);
  k_red<<<72, 256, 0, stream>>>(pa, 128, x, nullptr, h, nullptr);
  k_mlp<<<144, 256, 0, stream>>>(h, w1, w3, w2, pm);
  k_red<<<72, 256, 0, stream>>>(pm, 144, nullptr, h, nullptr, (__hip_bfloat16*)d_out);
}
```

```cpp
#include <hip/hip_runtime.h>
#include <hip/hip_bf16.h>
#include <stdint.h>

#define GAS __attribute__((address_space(1)))
#define LAS __attribute__((address_space(3)))
typedef unsigned u32;
typedef unsigned long long u64;
typedef unsigned short u16;
typedef u32 v4u __attribute__((ext_vector_type(4)));
typedef u32 v2u __attribute__((ext_vector_type(2)));
typedef float f4 __attribute__((ext_vector_type(4)));
typedef float f2 __attribute__((ext_vector_type(2)));
typedef __bf16 bf8 __attribute__((ext_vector_type(8)));
typedef __bf16 bf2 __attribute__((ext_vector_type(2)));
typedef __amdgpu_buffer_rsrc_t rsrc_t;

constexpr int D = 576, SK = 8192, WIN = 2048, FF = 4608, QD = 4096;
constexpr int WS_EPOCH = 0, WS_FLAGS = 4096, WS_QG = 8192, WS_ML = 73728, WS_OP = 77824, WS_AP = 208896, WS_MP = 245760,
              WS_END = 909312;
constexpr int WS_STAMP = 1 << 20;
constexpr u32 SENT = 0xFFFFFFFFu;
constexpr int AUX_NT = 2, AUX_SC1 = 16;
constexpr int SPIN_LDS = 1 << 18, SPIN_GLB = 1 << 15;
constexpr int L_FLAG = 0;
constexpr int L_Q = 128;
constexpr int L_P = 640;
constexpr int L_MX = 1664;
constexpr int L_ST = 1792;
constexpr int L_QP = 2048;
constexpr int L_O = 5120;
constexpr int L_R = 9216;
constexpr int L_END = 9216 + 9216;
enum { F_QCNT = 0, F_QRDY, F_MXCNT, F_PCNT, F_OCNT, F_VISS, F_CLR, F_INOK, F_RCNT };

__device__ __forceinline__ rsrc_t mkrs(const void* p, int bytes) { return __builtin_amdgcn_make_buffer_rsrc((void*)p, 0, bytes, 0x00020000); }
__device__ __forceinline__ float asf(u32 u) { return __uint_as_float(u); }
__device__ __forceinline__ u32 asu(float f) { return __float_as_uint(f); }
__device__ __forceinline__ u32 cvtpk(float lo, float hi) { return __builtin_bit_cast(u32, __builtin_convertvector(f2{lo, hi}, bf2)); }
__device__ __forceinline__ float rbf(float f) { return asf(cvtpk(f, f) & 0xffff0000u); }
__device__ __forceinline__ float blo(u32 u) { return asf(u << 16); }
__device__ __forceinline__ float bhi(u32 u) { return asf(u & 0xffff0000u); }
template <int C> __device__ __forceinline__ u32 dppu(u32 x) { return (u32)__builtin_amdgcn_mov_dpp((int)x, C, 0xf, 0xf, true); }
template <int C> __device__ __forceinline__ float dpp(float x) { return asf(dppu<C>(asu(x))); }
constexpr int XOR1 = 0xB1, XOR2 = 0x4E, HMIR = 0x141, ROR4 = 0x124, ROR8 = 0x128, SHL1 = 0x101;
__device__ __forceinline__ float x16_sum(float x) { auto s = __builtin_amdgcn_permlane16_swap(asu(x), asu(x), false, false); return asf(s[0]) + asf(s[1]); }
__device__ __forceinline__ float x32_sum(float x) { auto s = __builtin_amdgcn_permlane32_swap(asu(x), asu(x), false, false); return asf(s[0]) + asf(s[1]); }
__device__ __forceinline__ float x16_max(float x) { auto s = __builtin_amdgcn_permlane16_swap(asu(x), asu(x), false, false); return fmaxf(asf(s[0]), asf(s[1])); }
__device__ __forceinline__ float x32_max(float x) { auto s = __builtin_amdgcn_permlane32_swap(asu(x), asu(x), false, false); return fmaxf(asf(s[0]), asf(s[1])); }
__device__ __forceinline__ f4 mfma(v4u a, v4u b, f4 c) { return __builtin_amdgcn_mfma_f32_16x16x32_bf16(__builtin_bit_cast(bf8, a), __builtin_bit_cast(bf8, b), c, 0, 0, 0); }
__device__ __forceinline__ u32 plo(u32 a, u32 b) { return __builtin_amdgcn_perm(b, a, 0x05040100u); }
__device__ __forceinline__ u32 phi(u32 a, u32 b) { return __builtin_amdgcn_perm(b, a, 0x07060302u); }
__device__ __forceinline__ void tr8(const v4u* r, v4u* f) {
#pragma unroll
  for (int e = 0; e < 4; ++e) {
    f[2 * e] = v4u{plo(r[0][e], r[1][e]), plo(r[2][e], r[3][e]), plo(r[4][e], r[5][e]), plo(r[6][e], r[7][e])};
    f[2 * e + 1] = v4u{phi(r[0][e], r[1][e]), phi(r[2][e], r[3][e]), phi(r[4][e], r[5][e]), phi(r[6][e], r[7][e])};
  }
}
__device__ __forceinline__ void tr4(const v2u* r, v4u* f) {
#pragma unroll
  for (int e = 0; e < 2; ++e) {
    f[2 * e] = v4u{plo(r[0][e], r[1][e]), plo(r[2][e], r[3][e]), plo(r[4][e], r[5][e]), plo(r[6][e], r[7][e])};
    f[2 * e + 1] = v4u{phi(r[0][e], r[1][e]), phi(r[2][e], r[3][e]), phi(r[4][e], r[5][e]), phi(r[6][e], r[7][e])};
  }
}
__device__ __forceinline__ u32 nosent(u32 x) { return x < 0xFFFFFFFEu ? x : 0xFFFFFFFEu; }
__device__ __forceinline__ v4u nosent4(f4 v) { return v4u{nosent(asu(v[0])), nosent(asu(v[1])), nosent(asu(v[2])), nosent(asu(v[3]))}; }
__device__ __forceinline__ bool ok4(v4u v) { return v[0] != SENT && v[1] != SENT && v[2] != SENT && v[3] != SENT; }
#define PIN4(f) asm volatile("" : "+v"(f[0]), "+v"(f[1]), "+v"(f[2]), "+v"(f[3]))
#define PIN8(f) asm volatile("" : "+v"(f[0]), "+v"(f[1]), "+v"(f[2]), "+v"(f[3]), "+v"(f[4]), "+v"(f[5]), "+v"(f[6]), "+v"(f[7]))
#define CBAR() asm volatile("" ::: "memory")
#define SCHED() __builtin_amdgcn_sched_barrier(0)

__device__ __forceinline__ u32 lds_ld(LAS u32* p) { return __hip_atomic_load(p, __ATOMIC_RELAXED, __HIP_MEMORY_SCOPE_WORKGROUP); }
__device__ __forceinline__ void lds_wait(LAS u32* p, u32 want) {
  CBAR();
#pragma nounroll
  for (int n = 0; n < SPIN_LDS; ++n) {
    if ((u32)__builtin_amdgcn_readfirstlane((int)lds_ld(p)) >= want) break;
    __builtin_amdgcn_s_sleep(1);
  }
  CBAR();
}
__device__ __forceinline__ u32 lds_arrive(LAS u32* p, int lane) {
  asm volatile("s_waitcnt lgkmcnt(0)" ::: "memory");
  u32 old = 0;
  if (lane == 0) old = __hip_atomic_fetch_add(p, 1u, __ATOMIC_RELAXED, __HIP_MEMORY_SCOPE_WORKGROUP);
  old = (u32)__builtin_amdgcn_readfirstlane((int)old);
  CBAR();
  return old;
}

#define STAMP(k) do {} while (0)

__global__ void __launch_bounds__(1024) fused_decode(const u16* __restrict__ x, const u16* __restrict__ kc, const u16* __restrict__ vc,
                                                     const u16* __restrict__ wq, const u16* __restrict__ wo, const u16* __restrict__ w1,
                                                     const u16* __restrict__ w3, const u16* __restrict__ w2, u16* __restrict__ out,
                                                     char* ws) {
  __shared__ __attribute__((aligned(16))) char smem[L_END];
  LAS char* L = (LAS char*)smem;
  LAS u32* FL = (LAS u32*)(L + L_FLAG);
  const int bid = blockIdx.x, lane = threadIdx.x & 63, w = __builtin_amdgcn_readfirstlane(threadIdx.x >> 6);
  const int xcd = bid & 7, ii = bid >> 3;
  const int kvh = 2 * xcd + (ii >> 4), jj = ii & 15, bb = jj >> 3;
  const bool isY = ii < 18, isX = ii >= 18 && ii < 27;
  const int yi = xcd * 18 + ii, xc = ii - 18;
  const int g4 = lane >> 4, r16 = lane & 15;
  const rsrc_t rws = mkrs(ws, WS_END);
  STAMP(0);

  int yt = -1;
  int v2 = -1;
  if (w >= 4 && w < 12) {
    const int kw = w & 3;
    const bool isK = w < 8;
    const size_t base = ((size_t)(bb * 16 + kvh) * SK + (SK - WIN) + (jj & 7) * 256 + kw * 64) * 256;
    const rsrc_t rk = mkrs((const char*)(isK ? kc : vc) + base, 64 * 256);
    const int voff = isK ? ((r16 >> 2) * 256 + (g4 * 4 + (lane & 3)) * 16) : lane * 16;
    const int rot = bid * 5;
    v4u t[16];
    if (isK) {
#pragma unroll
      for (int i = 0; i < 4; ++i) t[i] = __builtin_amdgcn_raw_buffer_load_b128(rk, voff, ((i + rot) & 15) * 1024, AUX_NT);
      SCHED();
      __builtin_amdgcn_s_barrier();
#pragma unroll
      for (int i = 4; i < 16; ++i) t[i] = __builtin_amdgcn_raw_buffer_load_b128(rk, voff, ((i + rot) & 15) * 1024, AUX_NT);
      SCHED();
      STAMP(1);
      lds_wait(FL + F_QRDY, 1);
      STAMP(2);
      v4u qf = *(LAS v4u*)(L + L_Q + ((r16 >> 2) & 1) * 256 + (g4 * 4 + (lane & 3)) * 16);
      const v4u z4 = {0, 0, 0, 0};
      const v4u q0 = r16 < 8 ? qf : z4, q1 = r16 < 8 ? z4 : qf;
      float sc[8];
#pragma unroll
      for (int m = 0; m < 8; ++m) {
        f4 d = mfma(t[m], q0, f4{0, 0, 0, 0});
        d = mfma(t[m + 8], q1, d);
        sc[m] = (dpp<0x00>(d[0]) + dpp<0x55>(d[1])) + (dpp<0xAA>(d[2]) + dpp<0xFF>(d[3]));
      }
      const bool b0 = lane & 1, b1 = lane & 2;
      float s0 = b1 ? (b0 ? sc[6] : sc[4]) : (b0 ? sc[2] : sc[0]);
      float s1 = b1 ? (b0 ? sc[7] : sc[5]) : (b0 ? sc[3] : sc[1]);
      u32 pk = cvtpk(s0, s1);
      pk = cvtpk(blo(pk) * 0.08838834764831845f, bhi(pk) * 0.08838834764831845f);
      s0 = blo(pk) * 1.4426950408889634f; s1 = bhi(pk) * 1.4426950408889634f;
      float mx = fmaxf(s0, s1);
      mx = fmaxf(mx, dpp<XOR1>(mx)); mx = fmaxf(mx, dpp<XOR2>(mx)); mx = fmaxf(mx, dpp<ROR8>(mx));
      mx = x32_max(x16_max(mx));
      const int hd = (r16 >> 2) & 1;
      LAS float* MX = (LAS float*)(L + L_MX);
      if ((lane & 0x3B) == 0) MX[hd * 4 + kw] = mx;
      lds_arrive(FL + F_MXCNT, lane);
      lds_wait(FL + F_MXCNT, 4);
      f4 m4 = *(LAS f4*)(MX + hd * 4);
      mx = fmaxf(fmaxf(m4[0], m4[1]), fmaxf(m4[2], m4[3]));
      const float p0 = __builtin_amdgcn_exp2f(s0 - mx), p1 = __builtin_amdgcn_exp2f(s1 - mx);
      *(LAS u32*)(L + L_P + kw * 256 + hd * 128 + (r16 >> 3) * 64 + g4 * 16 + (lane & 3) * 4) = cvtpk(p0, p1);
      float ls = p0 + p1;
      ls += dpp<XOR1>(ls); ls += dpp<XOR2>(ls); ls += dpp<ROR8>(ls);
      ls = x32_sum(x16_sum(ls));
      if ((lane & 0x3B) == 0) MX[8 + hd * 4 + kw] = ls;
      if (lds_arrive(FL + F_PCNT, lane) == 3) {
        f4 l4 = *(LAS f4*)(MX + 8 + hd * 4);
        lds_wait(FL + F_CLR, 1);
        if ((lane & 0x3B) == 0)
          __builtin_amdgcn_raw_buffer_store_b64(v2u{nosent(asu(mx)), nosent(asu((l4[0] + l4[1]) + (l4[2] + l4[3])))}, rws,
                                                WS_ML + ((2 * kvh + hd) * 16 + jj) * 8, 0, AUX_SC1);
      }
      STAMP(3);
      if (isY) { if (kw == 0) yt = 8; if (kw == 1) v2 = 4; }
    } else {
      __builtin_amdgcn_s_barrier();
#pragma unroll
      for (int i = 0; i < 16; ++i) t[i] = __builtin_amdgcn_raw_buffer_load_b128(rk, voff, ((i + rot) & 15) * 1024, AUX_NT);
      SCHED();
      lds_arrive(FL + F_VISS, lane);
      STAMP(1);
      lds_wait(FL + F_PCNT, 4);
      STAMP(2);
      const v4u pa = *(LAS v4u*)(L + L_P + kw * 256 + ((r16 >> 2) & 1) * 128 + g4 * 16);
      const v4u pb = *(LAS v4u*)(L + L_P + kw * 256 + ((r16 >> 2) & 1) * 128 + 64 + g4 * 16);
      f4 acc[8];
      v4u f[8];
      tr8(t, f);
#pragma unroll
      for (int j = 0; j < 8; ++j) acc[j] = mfma(pa, f[j], f4{0, 0, 0, 0});
      SCHED();
      tr8(t + 8, f);
#pragma unroll
      for (int j = 0; j < 8; ++j) acc[j] = mfma(pb, f[j], acc[j]);
      STAMP(3);
      {
        const bool hi = g4 >= 2;
        f4 o = {hi ? acc[4][0] : acc[0][0], hi ? acc[5][0] : acc[1][0], hi ? acc[6][0] : acc[2][0], hi ? acc[7][0] : acc[3][0]};
        *(LAS f4*)(L + L_O + kw * 1024 + (g4 & 1) * 512 + r16 * 32 + (g4 >> 1) * 16) = o;
      }
      if (lds_arrive(FL + F_OCNT, lane) == 3) {
        f4 o = (*(LAS f4*)(L + L_O + lane * 16) + *(LAS f4*)(L + L_O + 1024 + lane * 16)) +
               (*(LAS f4*)(L + L_O + 2048 + lane * 16) + *(LAS f4*)(L + L_O + 3072 + lane * 16));
        lds_wait(FL + F_CLR, 1);
        __builtin_amdgcn_raw_buffer_store_b64(v2u{nosent(cvtpk(o[0], o[1])), nosent(cvtpk(o[2], o[3]))}, rws,
                                              WS_OP + (2 * kvh * 8 + (lane >> 2)) * 512 + jj * 32 + (lane & 3) * 8, 0, AUX_SC1);
      }
      STAMP(4);
      if (isY) v2 = kw;
    }
  } else {
    const u64 tag = *(const __attribute__((address_space(4))) u64*)(ws + WS_EPOCH) + 1;
    if (w < 3) {
      const rsrc_t rq = mkrs(wq, D * QD * 2);
      const rsrc_t rx = mkrs(x, 2 * D * 2);
      const int kr = r16 >> 1, k0 = w * 256 + kr * 32 + g4 * 8;
      v4u t[8];
      const int voff = k0 * (QD * 2) + (kvh * 256 + jj * 16 + (lane & 1) * 8) * 2;
      const int hb = lane >> 5, l5 = lane & 31;
      v4u xs0 = __builtin_amdgcn_raw_buffer_load_b128(rx, hb * (D * 2) + l5 * 16, 0, 0);
      v4u xs1 = __builtin_amdgcn_raw_buffer_load_b128(rx, hb * (D * 2) + l5 * 16 + 512, 0, 0);
      v4u xs2 = __builtin_amdgcn_raw_buffer_load_b128(rx, l5 < 8 ? hb * (D * 2) + l5 * 16 + 1024 : 1 << 20, 0, 0);
      v4u xf = __builtin_amdgcn_raw_buffer_load_b128(rx, k0 < D ? (lane & 1) * (D * 2) + k0 * 2 : 1 << 20, 0, 0);
#pragma unroll
      for (int i = 0; i < 8; ++i) t[i] = __builtin_amdgcn_raw_buffer_load_b128(rq, voff, i * (QD * 2), AUX_NT);
      SCHED();
      __builtin_amdgcn_s_barrier();
      SCHED();
      float s = 0;
#pragma unroll
      for (int e = 0; e < 4; ++e) s += (blo(xs0[e]) + bhi(xs0[e])) + (blo(xs1[e]) + bhi(xs1[e])) + (blo(xs2[e]) + bhi(xs2[e]));
      s += dpp<XOR1>(s); s += dpp<XOR2>(s); s += dpp<HMIR>(s); s += dpp<ROR8>(s); s = x16_sum(s);
      const float mean = s * (1.f / D);
      float v = 0, v2s = 0;
#pragma unroll
      for (int e = 0; e < 4; ++e) {
        float a = blo(xs0[e]) - mean, b = bhi(xs0[e]) - mean, c = blo(xs1[e]) - mean, d = bhi(xs1[e]) - mean;
        float e0 = blo(xs2[e]) - mean, e1 = bhi(xs2[e]) - mean;
        v += (a * a + b * b) + (c * c + d * d); v2s += e0 * e0 + e1 * e1;
      }
      v += l5 < 8 ? v2s : 0.f;
      v += dpp<XOR1>(v); v += dpp<XOR2>(v); v += dpp<HMIR>(v); v += dpp<ROR8>(v); v = x16_sum(v);
      const float mb = rbf(mean);
      const float rd = 1.f / rbf(__builtin_sqrtf(rbf(rbf(v * (1.f / D)) + 1e-5f)));
      const float m0 = asf(__builtin_amdgcn_readlane(asu(mb), 0)), m1 = asf(__builtin_amdgcn_readlane(asu(mb), 32));
      const float r0 = asf(__builtin_amdgcn_readlane(asu(rd), 0)), r1 = asf(__builtin_amdgcn_readlane(asu(rd), 32));
      const float mm = (lane & 1) ? m1 : m0, rr = (lane & 1) ? r1 : r0;
      v4u a;
#pragma unroll
      for (int e = 0; e < 4; ++e) { u32 c = cvtpk(blo(xf[e]) - mm, bhi(xf[e]) - mm); a[e] = cvtpk(blo(c) * rr, bhi(c) * rr); }
      v4u f[8];
      tr8(t, f);
      f4 acc[8];
#pragma unroll
      for (int j = 0; j < 8; ++j) acc[j] = mfma(a, f[j], f4{0, 0, 0, 0});
      STAMP(1);
      if ((r16 >> 2) == g4) {
        const bool od = lane & 2;
        LAS char* qp = L + L_QP + (w * 8 + kr) * 128 + (lane & 1) * 32;
#pragma unroll
        for (int b = 0; b < 2; ++b) {
          *(LAS f4*)(qp + b * 64) = f4{od ? acc[0][2 + b] : acc[0][b], od ? acc[1][2 + b] : acc[1][b], od ? acc[2][2 + b] : acc[2][b], od ? acc[3][2 + b] : acc[3][b]};
          *(LAS f4*)(qp + b * 64 + 16) = f4{od ? acc[4][2 + b] : acc[4][b], od ? acc[5][2 + b] : acc[5][b], od ? acc[6][2 + b] : acc[6][b], od ? acc[7][2 + b] : acc[7][b]};
        }
      }
      if (lds_arrive(FL + F_QCNT, lane) == 2) {
        float q = 0;
#pragma unroll
        for (int k = 0; k < 9; ++k) q += *(LAS float*)(L + L_QP + (hb * 9 + k) * 128 + l5 * 4);
        q = x32_sum(q);
        if (lane < 32)
          __builtin_amdgcn_raw_buffer_store_b64(v2u{(cvtpk(q, q) & 0xffffu) | ((u32)tag << 16), (u32)(tag >> 16)}, rws,
                                                WS_QG + ((lane >> 4) * QD + kvh * 256 + jj * 16 + r16) * 8, 0, AUX_SC1);
        STAMP(2);
        const int go = WS_QG + (bb * QD + kvh * 256) * 8 + lane * 16;
        const u32 th = (u32)(tag >> 16), tl = (u32)tag & 0xffffu;
        v4u g0, g1;
        __builtin_amdgcn_s_sleep(16);
        _Pragma("nounroll") for (int n = 0; n < SPIN_GLB; ++n) {
          g0 = __builtin_amdgcn_raw_buffer_load_b128(rws, go, 0, AUX_SC1);
          g1 = __builtin_amdgcn_raw_buffer_load_b128(rws, go + 1024, 0, AUX_SC1);
          CBAR();
          const bool ok = g0[1] == th && g0[3] == th && g1[1] == th && g1[3] == th && (g0[0] >> 16) == tl && (g0[2] >> 16) == tl &&
                          (g1[0] >> 16) == tl && (g1[2] >> 16) == tl;
          if (__all(ok)) break;
        }
        *(LAS u32*)(L + L_Q + lane * 4) = plo(g0[0], g0[2]);
        *(LAS u32*)(L + L_Q + 256 + lane * 4) = plo(g1[0], g1[2]);
        lds_arrive(FL + F_QRDY, lane);
        STAMP(3);
      }
    } else if (w == 3) {
      if (lane < 32) FL[lane] = 0;
      asm volatile("s_waitcnt lgkmcnt(0)" ::: "memory");
      const v4u s4 = {SENT, SENT, SENT, SENT};
      __builtin_amdgcn_raw_buffer_store_b64(v2u{SENT, SENT}, rws, WS_OP + (2 * kvh * 8 + (lane >> 2)) * 512 + jj * 32 + (lane & 3) * 8, 0, AUX_SC1);
      if (lane < 2) __builtin_amdgcn_raw_buffer_store_b64(v2u{SENT, SENT}, rws, WS_ML + ((2 * kvh + lane) * 16 + jj) * 8, 0, AUX_SC1);
      if (isX && lane < 32) __builtin_amdgcn_raw_buffer_store_b128(s4, rws, WS_AP + (xc * 8 + xcd) * 512 + lane * 16, 0, AUX_SC1);
      if (isY) {
#pragma unroll
        for (int n = 0; n < 5; ++n)
          if (n < 4 || lane < 32) __builtin_amdgcn_raw_buffer_store_b128(s4, rws, WS_MP + (n * 16 + (lane >> 2)) * 9216 + yi * 64 + (lane & 3) * 16, 0, AUX_SC1);
      }
      __builtin_amdgcn_s_barrier();
      asm volatile("s_waitcnt vmcnt(0)" ::: "memory");
      if (lane == 0) __builtin_amdgcn_raw_buffer_store_b64(v2u{(u32)tag, (u32)(tag >> 32)}, rws, WS_FLAGS + bid * 8, 0, AUX_SC1);
      lds_arrive(FL + F_CLR, lane);
      STAMP(1);
      if (isX || isY) {
        __builtin_amdgcn_s_sleep(32);
        _Pragma("nounroll") for (int n = 0; n < SPIN_GLB; ++n) {
          v4u g0 = __builtin_amdgcn_raw_buffer_load_b128(rws, WS_FLAGS + lane * 16, 0, AUX_SC1);
          v4u g1 = __builtin_amdgcn_raw_buffer_load_b128(rws, WS_FLAGS + 1024 + lane * 16, 0, AUX_SC1);
          CBAR();
          const u32 tl = (u32)tag, th = (u32)(tag >> 32);
          const bool ok = g0[0] == tl && g0[1] == th && g0[2] == tl && g0[3] == th && g1[0] == tl && g1[1] == th && g1[2] == tl && g1[3] == th;
          if (__all(ok)) break;
        }
        lds_arrive(FL + F_INOK, lane);
      }
      STAMP(2);
    } else {
      __builtin_amdgcn_s_barrier();
    }
    yt = w < 4 ? w : w - 8;
    if (isX) {
      const int xw = yt, hh = 4 * xcd + (xw >> 1), hf = xw & 1;
      lds_wait(FL + F_VISS, 4);
      const rsrc_t ro = mkrs(wo, QD * D * 2);
      const int voff = (hh * 128 + hf * 64 + g4 * 16) * (D * 2) + (xc * 64 + r16 * 4) * 2;
      v2u t[16];
#pragma unroll
      for (int i = 0; i < 16; ++i) t[i] = __builtin_amdgcn_raw_buffer_load_b64(ro, voff, i * (D * 2), AUX_NT);
      SCHED();
      STAMP(4);
      lds_wait(FL + F_INOK, 1);
      v2u ml;
      _Pragma("nounroll") for (int n = 0; n < SPIN_GLB; ++n) {
        ml = __builtin_amdgcn_raw_buffer_load_b64(rws, WS_ML + (hh * 16 + r16) * 8, 0, AUX_SC1);
        CBAR();
        if (__all(ml[0] != SENT && ml[1] != SENT)) break;
      }
      float M = asf(ml[0]);
      M = fmaxf(M, dpp<XOR1>(M)); M = fmaxf(M, dpp<XOR2>(M)); M = fmaxf(M, dpp<HMIR>(M));
      const float e = __builtin_amdgcn_exp2f(asf(ml[0]) - M);
      float Ls = e * asf(ml[1]);
      Ls += dpp<XOR1>(Ls); Ls += dpp<XOR2>(Ls); Ls += dpp<HMIR>(Ls);
      const float wr = e / Ls;
      float wg[4];
#pragma unroll
      for (int i = 0; i < 4; ++i) wg[i] = __shfl(wr, g4 * 4 + i);
      v4u f0[4], f1[4];
      tr4(t, f0); tr4(t + 8, f1);
      PIN4(f0); PIN4(f1); PIN4(wg);
      STAMP(5);
      v4u a0, a1;
      const int ao = WS_OP + (hh * 8 + hf * 4 + g4) * 512 + r16 * 32;
      _Pragma("nounroll") for (int n = 0; n < SPIN_GLB; ++n) {
        a0 = __builtin_amdgcn_raw_buffer_load_b128(rws, ao, 0, AUX_SC1);
        a1 = __builtin_amdgcn_raw_buffer_load_b128(rws, ao + 16, 0, AUX_SC1);
        CBAR();
        if (__all(ok4(a0) && ok4(a1))) break;
      }
      STAMP(6);
      f4 o;
#pragma unroll
      for (int j = 0; j < 4; ++j) {
        f4 d = mfma(a0, f0[j], f4{0, 0, 0, 0});
        d = mfma(a1, f1[j], d);
        o[j] = x16_sum((wg[0] * d[0] + wg[1] * d[1]) + (wg[2] * d[2] + wg[3] * d[3]));
      }
      if (!(g4 & 1)) *(LAS f4*)(L + L_R + xw * 512 + ((r16 >> 1) & 3) * 128 + (g4 >> 1) * 64 + (r16 & 1) * 32 + (r16 >> 3) * 16) = o;
      if (lds_arrive(FL + F_RCNT, lane) == 7) {
        const int hb = lane >> 5, l5 = lane & 31;
        LAS char* rp = L + L_R + hb * 2048 + l5 * 16;
        f4 s = (*(LAS f4*)(rp) + *(LAS f4*)(rp + 512)) + (*(LAS f4*)(rp + 1024) + *(LAS f4*)(rp + 1536));
#pragma unroll
        for (int j = 0; j < 4; ++j) s[j] = x32_sum(s[j]);
        if (lane < 32) __builtin_amdgcn_raw_buffer_store_b128(nosent4(s), rws, WS_AP + (xc * 8 + xcd) * 512 + lane * 16, 0, AUX_SC1);
        STAMP(7);
        const int z = xcd * 9 + xc, zb = z / 36, zz = z % 36;
        const int col = (zz >> 3) * 128 + ((zz >> 1) & 3) * 32 + (lane & 3) * 8 + (zz & 1) * 4;
        const rsrc_t rx = mkrs(x, 2 * D * 2);
        const v2u xv = __builtin_amdgcn_raw_buffer_load_b64(rx, (zb * D + col) * 2, 0, 0);
        const int c6 = col & 63;
        const int ho = WS_AP + ((col >> 6) * 8 + ((lane >> 2) & 7)) * 512 + ((c6 >> 3) & 3) * 128 + zb * 64 + ((c6 >> 2) & 1) * 32 + (c6 >> 5) * 16;
        v4u hv;
        _Pragma("nounroll") for (int n = 0; n < SPIN_GLB; ++n) {
          hv = __builtin_amdgcn_raw_buffer_load_b128(rws, ho, 0, AUX_SC1);
          CBAR();
          if (__all(ok4(hv))) break;
        }
        float h[4];
#pragma unroll
        for (int j = 0; j < 4; ++j) { float a = asf(hv[j]); a += dpp<ROR4>(a); a += dpp<ROR8>(a); h[j] = x16_sum(a); }
        h[0] = rbf(blo(xv[0]) + rbf(h[0])); h[1] = rbf(bhi(xv[0]) + rbf(h[1]));
        h[2] = rbf(blo(xv[1]) + rbf(h[2])); h[3] = rbf(bhi(xv[1]) + rbf(h[3]));
        PIN4(h);
        v4u p[9];
        _Pragma("nounroll") for (int n = 0; n < SPIN_GLB; ++n) {
          bool ok = true;
#pragma unroll
          for (int k = 0; k < 9; ++k) p[k] = __builtin_amdgcn_raw_buffer_load_b128(rws, WS_MP + z * 9216 + lane * 16, k * 1024, AUX_SC1);
          CBAR();
#pragma unroll
          for (int k = 0; k < 9; ++k) ok = ok && ok4(p[k]);
          if (__all(ok)) break;
        }
        float r[4];
#pragma unroll
        for (int j = 0; j < 4; ++j) {
          float a = ((asf(p[0][j]) + asf(p[1][j])) + (asf(p[2][j]) + asf(p[3][j]))) + ((asf(p[4][j]) + asf(p[5][j])) + (asf(p[6][j]) + asf(p[7][j]))) + asf(p[8][j]);
          a += dpp<ROR4>(a); a += dpp<ROR8>(a);
          r[j] = h[j] + rbf(x32_sum(x16_sum(a)));
        }
        if (lane < 4) *(GAS v2u*)(out + zb * D + col) = v2u{cvtpk(r[0], r[1]), cvtpk(r[2], r[3])};
        if (z == 0 && lane == 0) *(GAS u64*)(ws + WS_EPOCH) = tag;
      }
      return;
    }
  }
  if (!isY) return;
  if (yt >= 0) {
    lds_wait(FL + F_VISS, 4);
    const GAS char* wp = (const GAS char*)((lane & 4) ? w3 : w1) + (size_t)((yt * 64 + (r16 >> 3) * 32 + g4 * 8) * (FF * 2) + (yi * 32 + (lane & 3) * 8) * 2);
    v4u t[8];
#pragma unroll
    for (int i = 0; i < 8; ++i) t[i] = __builtin_nontemporal_load((const GAS v4u*)(wp + i * (FF * 2)));
    const rsrc_t rx = mkrs(x, 2 * D * 2);
    const v2u xv = __builtin_amdgcn_raw_buffer_load_b64(rx, (((lane >> 2) & 1) * D + yt * 64 + (lane & 1) * 32 + g4 * 8 + ((lane >> 1) & 1) * 4) * 2, 0, 0);
    SCHED();
    STAMP(4);
    lds_wait(FL + F_INOK, 1);
    v4u f[8];
    tr8(t, f);
    PIN8(f);
    STAMP(5);
    const int ao = WS_AP + (yt * 8 + (r16 >> 3)) * 512 + g4 * 128 + (lane & 7) * 16;
    v4u p[4];
    _Pragma("nounroll") for (int n = 0; n < SPIN_GLB; ++n) {
#pragma unroll
      for (int k = 0; k < 4; ++k) p[k] = __builtin_amdgcn_raw_buffer_load_b128(rws, ao, k * 1024, AUX_SC1);
      CBAR();
      if (__all(ok4(p[0]) && ok4(p[1]) && ok4(p[2]) && ok4(p[3]))) break;
    }
    STAMP(6);
    float h[4];
#pragma unroll
    for (int j = 0; j < 4; ++j) { float a = (asf(p[0][j]) + asf(p[1][j])) + (asf(p[2][j]) + asf(p[3][j])); h[j] = a + dpp<ROR8>(a); }
    const u32 c0 = cvtpk(h[0], h[1]), c1 = cvtpk(h[2], h[3]);
    const u32 h0 = cvtpk(blo(xv[0]) + blo(c0), bhi(xv[0]) + bhi(c0)), h1 = cvtpk(blo(xv[1]) + blo(c1), bhi(xv[1]) + bhi(c1));
    const u32 h2 = dppu<XOR2>(h0), h3 = dppu<XOR2>(h1);
    const bool one = lane & 8;
    const v4u a = {one ? 0x3F803F80u : h0, one ? 0x3F803F80u : h1, one ? 0x3F803F80u : h2, one ? 0x3F803F80u : h3};
    f4 acc[8];
#pragma unroll
    for (int j = 0; j < 8; ++j) acc[j] = mfma(a, f[j], f4{0, 0, 0, 0});
    const f4 gm = mfma(a, a, f4{0, 0, 0, 0});
    float o[8];
#pragma unroll
    for (int j = 0; j < 8; ++j) o[j] = acc[j][0] + dpp<ROR8>(acc[j][1]);
    if (r16 < 8) {
      LAS char* yp = L + L_R + yt * 1024 + ((lane & 3) * 16 + g4 * 4 + (r16 >> 2) * 2) * 16;
      *(LAS f4*)yp = f4{o[0], o[1], o[2], o[3]};
      *(LAS f4*)(yp + 16) = f4{o[4], o[5], o[6], o[7]};
    }
    const float sv = gm[0] + dpp<SHL1>(gm[1]);
    if (g4 < 2 && (r16 == 8 || r16 == 4 * g4)) *(LAS float*)(L + L_ST + ((r16 == 8 ? 0 : 2) + g4) * 64 + yt * 4) = sv;
    lds_arrive(FL + F_RCNT, lane);
    STAMP(7);
  } else if (v2 >= 0) {
    const rsrc_t r2 = mkrs(w2, FF * D * 2);
    const int voff = (v2 < 4 || r16 < 8) ? (yi * 32 + g4 * 8) * (D * 2) + (v2 * 128 + r16 * 8) * 2 : 1 << 30;
    v4u t[8];
#pragma unroll
    for (int i = 0; i < 8; ++i) t[i] = __builtin_amdgcn_raw_buffer_load_b128(r2, voff, i * (D * 2), AUX_NT);
    SCHED();
    v4u f[8];
    tr8(t, f);
    PIN8(f);
    STAMP(5);
    lds_wait(FL + F_RCNT, 9);
    STAMP(6);
    float st = r16 < 9 ? *(LAS float*)(L + L_ST + lane * 4) : 0.f;
    f4 s = *(LAS f4*)(L + L_R + lane * 16);
#pragma unroll
    for (int k = 1; k < 9; ++k) s += *(LAS f4*)(L + L_R + k * 1024 + lane * 16);
    st += dpp<XOR1>(st); st += dpp<XOR2>(st); st += dpp<HMIR>(st); st += dpp<ROR8>(st);
    const float a0 = asf(__builtin_amdgcn_readlane(asu(st), 0)), a1 = asf(__builtin_amdgcn_readlane(asu(st), 16));
    const float q0 = asf(__builtin_amdgcn_readlane(asu(st), 32)), q1 = asf(__builtin_amdgcn_readlane(asu(st), 48));
    const float mean = ((lane & 4) ? a1 : a0) * (1.f / D);
    const float var = ((lane & 4) ? q1 : q0) * (1.f / D) - mean * mean;
    const float rs = __builtin_amdgcn_rsqf(fmaxf(var, 0.f) + 1e-5f);
    float hid[4];
#pragma unroll
    for (int j = 0; j < 4; ++j) {
      const float zv = rs * (s[j] - mean * dpp<ROR8>(s[j]));
      const float up = dpp<XOR2>(zv);
      hid[j] = zv * __builtin_amdgcn_rcpf(1.f + __builtin_amdgcn_exp2f(-1.4426950408889634f * zv)) * up;
    }
    const u32 h0 = cvtpk(hid[0], hid[1]), h1 = cvtpk(hid[2], hid[3]);
    const v4u a = {h0, h1, dppu<XOR1>(h0), dppu<XOR1>(h1)};
    f4 acc[8];
#pragma unroll
    for (int j = 0; j < 8; ++j) acc[j] = mfma(a, f[j], f4{0, 0, 0, 0});
    if (g4 < 2 && (v2 < 4 || r16 < 8)) {
      const int mo = WS_MP + (g4 * 36 + v2 * 8 + (r16 >> 2) * 2) * 9216 + yi * 64 + (lane & 3) * 16;
      __builtin_amdgcn_raw_buffer_store_b128(nosent4(f4{acc[0][0], acc[1][0], acc[2][0], acc[3][0]}), rws, mo, 0, AUX_SC1);
      __builtin_amdgcn_raw_buffer_store_b128(nosent4(f4{acc[4][0], acc[5][0], acc[6][0], acc[7][0]}), rws, mo + 9216, 0, AUX_SC1);
    }
    STAMP(7);
  }
}

extern "C" void kernel_launch(void* const* d_in, const int* in_sizes, int n_in, void* d_out, int out_size, void* d_ws, size_t ws_size,
                              hipStream_t stream) {
  fused_decode<<<256, 1024, 0, stream>>>((const u16*)d_in[0], (const u16*)d_in[1], (const u16*)d_in[2], (const u16*)d_in[3],
                                         (const u16*)d_in[4], (const u16*)d_in[6], (const u16*)d_in[7], (const u16*)d_in[8], (u16*)d_out,
                                         (char*)d_ws);
}
```

```cpp
#include <hip/hip_runtime.h>
#include <hip/hip_bf16.h>
#include <stdint.h>

#define GAS __attribute__((address_space(1)))
#define LAS __attribute__((address_space(3)))
#define CAS __attribute__((address_space(4)))
typedef unsigned u32;
typedef unsigned long long u64;
typedef unsigned short u16;
typedef u32 v4u __attribute__((ext_vector_type(4)));
typedef u32 v2u __attribute__((ext_vector_type(2)));
typedef float f4 __attribute__((ext_vector_type(4)));
typedef float f2 __attribute__((ext_vector_type(2)));
typedef __bf16 bf8 __attribute__((ext_vector_type(8)));
typedef __bf16 bf2 __attribute__((ext_vector_type(2)));
typedef __amdgpu_buffer_rsrc_t rsrc_t;

#define QSLEEP 12
#define KWN 4
#define VPRE 4
#define VWN 5
#define VCRED 2
constexpr int D = 576, SK = 8192, WIN = 2048, FF = 4608, QD = 4096;
constexpr int WS_EPOCH = 0, WS_FLAGS = 4096, WS_ML = 8192, WS_OP = 12288, WS_AP = WS_OP + 131072, WS_MP = WS_AP + 36864,
              WS_QG = WS_MP + 663552, WS_END = WS_QG + 262144;
constexpr int WS_STAMP = 2 << 20;
constexpr u32 SENT = 0xFFFFFFFFu;
constexpr int AUX_NT = 2, AUX_SC1 = 16, OOB = 1 << 30;
constexpr int SPIN_LDS = 1 << 18, SPIN_GLB = 1 << 15;
constexpr int L_FLAG = 0;
constexpr int L_Q = 128;
constexpr int L_P = 640;
constexpr int L_MX = 1664;
constexpr int L_ST = 1792;
constexpr int L_QP = 2048;
constexpr int L_XN = 3584;
constexpr int L_O = 6144;
constexpr int L_R = 10240;
constexpr int L_STAMP = L_R + 9216;
constexpr int L_END = L_STAMP + 1024;
enum { F_QCNT = 0, F_QRDY, F_MXCNT, F_PCNT, F_OCNT, F_VISS, F_CLR, F_INOK, F_RCNT, F_XN };

__device__ __forceinline__ rsrc_t mkrs(const void* p, int bytes) { return __builtin_amdgcn_make_buffer_rsrc((void*)p, 0, bytes, 0x00020000); }
__device__ __forceinline__ float asf(u32 u) { return __uint_as_float(u); }
__device__ __forceinline__ u32 asu(float f) { return __float_as_uint(f); }
__device__ __forceinline__ u32 cvtpk(float lo, float hi) { return __builtin_bit_cast(u32, __builtin_convertvector(f2{lo, hi}, bf2)); }
__device__ __forceinline__ float rbf(float f) { return asf(cvtpk(f, f) & 0xffff0000u); }
__device__ __forceinline__ float blo(u32 u) { return asf(u << 16); }
__device__ __forceinline__ float bhi(u32 u) { return asf(u & 0xffff0000u); }
template <int C> __device__ __forceinline__ u32 dppu(u32 x) { return (u32)__builtin_amdgcn_mov_dpp((int)x, C, 0xf, 0xf, true); }
template <int C> __device__ __forceinline__ float dpp(float x) { return asf(dppu<C>(asu(x))); }
constexpr int XOR1 = 0xB1, XOR2 = 0x4E, HMIR = 0x141, ROR4 = 0x124, ROR8 = 0x128, SHL1 = 0x101;
__device__ __forceinline__ float x16_sum(float x) { auto s = __builtin_amdgcn_permlane16_swap(asu(x), asu(x), false, false); return asf(s[0]) + asf(s[1]); }
__device__ __forceinline__ float x32_sum(float x) { auto s = __builtin_amdgcn_permlane32_swap(asu(x), asu(x), false, false); return asf(s[0]) + asf(s[1]); }
__device__ __forceinline__ float x16_max(float x) { auto s = __builtin_amdgcn_permlane16_swap(asu(x), asu(x), false, false); return fmaxf(asf(s[0]), asf(s[1])); }
__device__ __forceinline__ float x32_max(float x) { auto s = __builtin_amdgcn_permlane32_swap(asu(x), asu(x), false, false); return fmaxf(asf(s[0]), asf(s[1])); }
__device__ __forceinline__ f4 mfma(v4u a, v4u b, f4 c) { return __builtin_amdgcn_mfma_f32_16x16x32_bf16(__builtin_bit_cast(bf8, a), __builtin_bit_cast(bf8, b), c, 0, 0, 0); }
__device__ __forceinline__ u32 plo(u32 a, u32 b) { return __builtin_amdgcn_perm(b, a, 0x05040100u); }
__device__ __forceinline__ u32 phi(u32 a, u32 b) { return __builtin_amdgcn_perm(b, a, 0x07060302u); }
__device__ __forceinline__ void tr8(const v4u* r, v4u* f) {
#pragma unroll
  for (int e = 0; e < 4; ++e) {
    f[2 * e] = v4u{plo(r[0][e], r[1][e]), plo(r[2][e], r[3][e]), plo(r[4][e], r[5][e]), plo(r[6][e], r[7][e])};
    f[2 * e + 1] = v4u{phi(r[0][e], r[1][e]), phi(r[2][e], r[3][e]), phi(r[4][e], r[5][e]), phi(r[6][e], r[7][e])};
  }
}
__device__ __forceinline__ void tr4(const v2u* r, v4u* f) {
#pragma unroll
  for (int e = 0; e < 2; ++e) {
    f[2 * e] = v4u{plo(r[0][e], r[1][e]), plo(r[2][e], r[3][e]), plo(r[4][e], r[5][e]), plo(r[6][e], r[7][e])};
    f[2 * e + 1] = v4u{phi(r[0][e], r[1][e]), phi(r[2][e], r[3][e]), phi(r[4][e], r[5][e]), phi(r[6][e], r[7][e])};
  }
}
__device__ __forceinline__ u32 umax(u32 a, u32 b) { return a > b ? a : b; }
__device__ __forceinline__ u32 nosent(u32 x) { return x < 0xFFFFFFFEu ? x : 0xFFFFFFFEu; }
__device__ __forceinline__ v4u nosent4(f4 v) { return v4u{nosent(asu(v[0])), nosent(asu(v[1])), nosent(asu(v[2])), nosent(asu(v[3]))}; }
__device__ __forceinline__ u32 mx4(v4u v, u32 m) { return umax(umax(umax(v[0], v[1]), m), umax(v[2], v[3])); }
__device__ __forceinline__ bool none_sent(u32 m) { return __builtin_amdgcn_ballot_w64(m == SENT) == 0; }
#define PIN4(f) asm volatile("" : "+v"(f[0]), "+v"(f[1]), "+v"(f[2]), "+v"(f[3]))
#define PIN8(f) asm volatile("" : "+v"(f[0]), "+v"(f[1]), "+v"(f[2]), "+v"(f[3]), "+v"(f[4]), "+v"(f[5]), "+v"(f[6]), "+v"(f[7]))
#define CBAR() asm volatile("" ::: "memory")
#define SCHED() __builtin_amdgcn_sched_barrier(0)
template <int N> __device__ __forceinline__ void waitvm() { asm volatile("s_waitcnt vmcnt(%0)" ::"i"(N) : "memory"); }
#define NOUNROLL _Pragma("nounroll")
#define LD128(off, soff) __builtin_amdgcn_raw_buffer_load_b128(rws, off, soff, AUX_SC1)

__device__ __forceinline__ u32 lds_ld(LAS u32* p) { return __hip_atomic_load(p, __ATOMIC_RELAXED, __HIP_MEMORY_SCOPE_WORKGROUP); }
__device__ __forceinline__ void lds_wait(LAS u32* p, u32 want) {
  CBAR();
  NOUNROLL for (int n = 0; n < SPIN_LDS; ++n) {
    if ((u32)__builtin_amdgcn_readfirstlane((int)lds_ld(p)) >= want) break;
    __builtin_amdgcn_s_sleep(1);
  }
  CBAR();
}
__device__ __forceinline__ u32 lds_arrive(LAS u32* p, int lane) {
  CBAR();
  u32 old = 0;
  if (lane == 0) old = __hip_atomic_fetch_add(p, 1u, __ATOMIC_RELAXED, __HIP_MEMORY_SCOPE_WORKGROUP);
  old = (u32)__builtin_amdgcn_readfirstlane((int)old);
  CBAR();
  return old;
}

#define STAMP(k) do {} while (0)
#define STAMP_INIT() do {} while (0)
#define STAMP_FLUSH() do {} while (0)

__global__ void __launch_bounds__(1024) fused_decode(const u16* __restrict__ x, const u16* __restrict__ kc, const u16* __restrict__ vc,
                                                     const u16* __restrict__ wq, const u16* __restrict__ wo, const u16* __restrict__ w1,
                                                     const u16* __restrict__ w3, const u16* __restrict__ w2, u16* __restrict__ out,
                                                     char* ws) {
  __shared__ __attribute__((aligned(16))) char smem[L_END];
  LAS char* L = (LAS char*)smem;
  LAS u32* FL = (LAS u32*)(L + L_FLAG);
  const int bid = blockIdx.x, lane = threadIdx.x & 63, w = __builtin_amdgcn_readfirstlane(threadIdx.x >> 6);
  const int xcd = bid & 7, ii = bid >> 3;
  const int kvh = 2 * xcd + (ii >> 4), jj = ii & 15, bb = jj >> 3;
  const bool isY = ii < 18, isX = ii >= 18 && ii < 27;
  const int yi = xcd * 18 + ii, xc = ii - 18;
  const int g4 = lane >> 4, r16 = lane & 15, hb = lane >> 5, l5 = lane & 31;
  STAMP_INIT();
  STAMP(0);
  if (threadIdx.x < 32) FL[lane] = 0;
  asm volatile("s_waitcnt lgkmcnt(0)\n\ts_barrier" ::: "memory");
  asm volatile("" ::"s"(x), "s"(kc), "s"(vc), "s"(wq), "s"(wo), "s"(w1), "s"(w3), "s"(w2), "s"(out), "s"(ws));
  const rsrc_t rws = mkrs(ws, WS_END);
  const rsrc_t rx = mkrs(x, 2 * D * 2);

  int yt = -1;
  int v2 = -1;
  const int kw = w & 3;
  v4u t[16];
  if (w < 4) {
    const char* bp; int bytes, voff, step;
    if (w < 3) {
      bp = (const char*)wq; bytes = D * QD * 2; step = QD * 2;
      const int k0 = w * 64 + (r16 >> 3) * 32 + g4 * 8;
      voff = k0 < 144 ? ((jj & 3) * 144 + k0) * (QD * 2) + (kvh * 256 + (jj >> 2) * 64 + (lane & 7) * 8) * 2 : OOB;
    } else {
      bp = (const char*)x; bytes = 2 * D * 2; step = 384;
      voff = l5 < 24 ? hb * (D * 2) + l5 * 16 : OOB;
    }
    const rsrc_t rk = mkrs(bp, bytes);
#pragma unroll
    for (int i = 0; i < 3; ++i) t[i] = __builtin_amdgcn_raw_buffer_load_b128(rk, voff, i * step, 0);
    if (w != 3) {
#pragma unroll
      for (int i = 3; i < 8; ++i) t[i] = __builtin_amdgcn_raw_buffer_load_b128(rk, voff, i * step, 0);
    }
    SCHED();
  } else if (w < 12) {
    const char* bp = (const char*)(w < 8 ? kc : vc) + ((size_t)(bb * 16 + kvh) * SK + (SK - WIN) + (jj & 7) * 256 + kw * 64) * 256;
    const int rot = bid * 5;
    const int voff = w < 8 ? ((r16 >> 2) * 256 + (g4 * 4 + (lane & 3)) * 16) : lane * 16;
    const rsrc_t rk = mkrs(bp, 64 * 256);
    if (w < 8) {
#pragma unroll
      for (int i = 0; i < 16; ++i) {
        if (i >= KWN) waitvm<KWN - 1>();
        t[i] = __builtin_amdgcn_raw_buffer_load_b128(rk, voff, ((i + rot) & 15) * 1024, AUX_NT);
        SCHED();
      }
    } else {
#pragma unroll
      for (int i = 0; i < 16; ++i) {
        if (i >= 1 && i <= VPRE) waitvm<0>();
        else if (i >= VPRE + VWN) waitvm<VWN - 1>();
        t[i] = __builtin_amdgcn_raw_buffer_load_b128(rk, voff, ((i + rot) & 15) * 1024, AUX_NT);
        SCHED();
      }
    }
  } else if (w == 12) {
    u64 pc; asm volatile("s_getpc_b64 %0" : "=s"(pc));
    const GAS v4u* cp = (const GAS v4u*)((pc & ~(u64)1023) + (u64)(ii & 15) * 1024) + lane;
    v4u c = *cp;
    asm volatile("" ::"v"(c));
  } else if (w == 13) {
    const u16* mp = g4 == 0 ? wo : g4 == 1 ? w1 : g4 == 2 ? w3 : w2;
    const int pg = ii + 32 * r16;
    if (pg < (g4 == 0 ? 72 : 81)) {
      u32 c = __builtin_nontemporal_load((const GAS u32*)((const GAS char*)mp + (size_t)pg * 65536));
      asm volatile("" ::"v"(c));
    }
  }
  const u64 tag = *(const CAS u64*)(ws + WS_EPOCH) + 1;

  if (w >= 4 && w < 8) {
    STAMP(1);
    lds_wait(FL + F_QRDY, 1);
    STAMP(2);
    v4u qf = *(LAS v4u*)(L + L_Q + ((r16 >> 2) & 1) * 256 + (g4 * 4 + (lane & 3)) * 16);
    const v4u z4 = {0, 0, 0, 0};
    const v4u q0 = r16 < 8 ? qf : z4, q1 = r16 < 8 ? z4 : qf;
    float sc[8];
#pragma unroll
    for (int m = 0; m < 8; ++m) {
      f4 d = mfma(t[m], q0, f4{0, 0, 0, 0});
      d = mfma(t[m + 8], q1, d);
      sc[m] = (dpp<0x00>(d[0]) + dpp<0x55>(d[1])) + (dpp<0xAA>(d[2]) + dpp<0xFF>(d[3]));
    }
    const bool b0 = lane & 1, b1 = lane & 2;
    float s0 = b1 ? (b0 ? sc[6] : sc[4]) : (b0 ? sc[2] : sc[0]);
    float s1 = b1 ? (b0 ? sc[7] : sc[5]) : (b0 ? sc[3] : sc[1]);
    u32 pk = cvtpk(s0, s1);
    pk = cvtpk(blo(pk) * 0.08838834764831845f, bhi(pk) * 0.08838834764831845f);
    s0 = blo(pk) * 1.4426950408889634f; s1 = bhi(pk) * 1.4426950408889634f;
    float mx = fmaxf(s0, s1);
    mx = fmaxf(mx, dpp<XOR1>(mx)); mx = fmaxf(mx, dpp<XOR2>(mx)); mx = fmaxf(mx, dpp<ROR8>(mx));
    mx = x32_max(x16_max(mx));
    const int hd = (r16 >> 2) & 1;
    LAS float* MX = (LAS float*)(L + L_MX);
    if ((lane & 0x3B) == 0) MX[hd * 4 + kw] = mx;
    lds_arrive(FL + F_MXCNT, lane);
    lds_wait(FL + F_MXCNT, 4);
    f4 m4 = *(LAS f4*)(MX + hd * 4);
    mx = fmaxf(fmaxf(m4[0], m4[1]), fmaxf(m4[2], m4[3]));
    const float p0 = __builtin_amdgcn_exp2f(s0 - mx), p1 = __builtin_amdgcn_exp2f(s1 - mx);
    *(LAS u32*)(L + L_P + kw * 256 + hd * 128 + (r16 >> 3) * 64 + g4 * 16 + (lane & 3) * 4) = cvtpk(p0, p1);
    float ls = p0 + p1;
    ls += dpp<XOR1>(ls); ls += dpp<XOR2>(ls); ls += dpp<ROR8>(ls);
    ls = x32_sum(x16_sum(ls));
    if ((lane & 0x3B) == 0) MX[8 + hd * 4 + kw] = ls;
    if (lds_arrive(FL + F_PCNT, lane) == 3) {
      f4 l4 = *(LAS f4*)(MX + 8 + hd * 4);
      lds_wait(FL + F_CLR, 1);
      if ((lane & 0x3B) == 0)
        __builtin_amdgcn_raw_buffer_store_b64(v2u{nosent(asu(mx)), nosent(asu((l4[0] + l4[1]) + (l4[2] + l4[3])))}, rws,
                                              WS_ML + ((2 * kvh + hd) * 16 + jj) * 8, 0, AUX_SC1);
    }
    STAMP(3);
    if (kw == 3 && (isX || isY)) {
      const u32 tl = (u32)tag, th = (u32)(tag >> 32);
      NOUNROLL for (int n = 0; n < SPIN_GLB; ++n) {
        v4u g0 = LD128(WS_FLAGS + lane * 16, 0);
        v4u g1 = LD128(WS_FLAGS + lane * 16, 1024);
        CBAR();
        const u32 bad = ((g0[0] ^ tl) | (g0[1] ^ th) | (g0[2] ^ tl) | (g0[3] ^ th)) | ((g1[0] ^ tl) | (g1[1] ^ th) | (g1[2] ^ tl) | (g1[3] ^ th));
        if (__builtin_amdgcn_ballot_w64(bad != 0) == 0) break;
        __builtin_amdgcn_s_sleep(8);
      }
      lds_arrive(FL + F_INOK, lane);
      STAMP(4);
    }
    if (isY) { if (kw == 0) yt = 8; if (kw == 1) v2 = 4; }
  } else if (w >= 8 && w < 12) {
    STAMP(1);
    waitvm<VCRED>();
    lds_arrive(FL + F_VISS, lane);
    lds_wait(FL + F_PCNT, 4);
    STAMP(2);
    const v4u pa = *(LAS v4u*)(L + L_P + kw * 256 + ((r16 >> 2) & 1) * 128 + g4 * 16);
    const v4u pb = *(LAS v4u*)(L + L_P + kw * 256 + ((r16 >> 2) & 1) * 128 + 64 + g4 * 16);
    f4 acc[8];
    v4u f[8];
    tr8(t, f);
#pragma unroll
    for (int j = 0; j < 8; ++j) acc[j] = mfma(pa, f[j], f4{0, 0, 0, 0});
    SCHED();
    tr8(t + 8, f);
#pragma unroll
    for (int j = 0; j < 8; ++j) acc[j] = mfma(pb, f[j], acc[j]);
    STAMP(3);
    {
      const bool hi = g4 >= 2;
      f4 o = {hi ? acc[4][0] : acc[0][0], hi ? acc[5][0] : acc[1][0], hi ? acc[6][0] : acc[2][0], hi ? acc[7][0] : acc[3][0]};
      *(LAS f4*)(L + L_O + kw * 1024 + (g4 & 1) * 512 + r16 * 32 + (g4 >> 1) * 16) = o;
    }
    if (lds_arrive(FL + F_OCNT, lane) == 3) {
      f4 o = (*(LAS f4*)(L + L_O + lane * 16) + *(LAS f4*)(L + L_O + 1024 + lane * 16)) +
             (*(LAS f4*)(L + L_O + 2048 + lane * 16) + *(LAS f4*)(L + L_O + 3072 + lane * 16));
      lds_wait(FL + F_CLR, 1);
      __builtin_amdgcn_s_setprio(3);
      __builtin_amdgcn_raw_buffer_store_b64(v2u{nosent(cvtpk(o[0], o[1])), nosent(cvtpk(o[2], o[3]))}, rws,
                                            WS_OP + (2 * kvh * 8 + (lane >> 2)) * 512 + jj * 32 + (lane & 3) * 8, 0, AUX_SC1);
      __builtin_amdgcn_s_setprio(0);
    }
    STAMP(4);
    if (isY) v2 = kw;
  } else {
    if (w < 3) {
      v4u f[8];
      tr8(t, f);
      lds_wait(FL + F_XN, 1);
      const v4u a = *(LAS v4u*)(L + L_XN + ((r16 >> 2) & 1) * (D * 2) + ((jj & 3) * 144 + w * 64 + (lane & 1) * 32 + g4 * 8) * 2);
      f4 acc[8];
#pragma unroll
      for (int j = 0; j < 8; ++j) acc[j] = mfma(a, f[j], f4{0, 0, 0, 0});
      float o[8];
#pragma unroll
      for (int j = 0; j < 8; ++j) o[j] = acc[j][0] + dpp<ROR8>(acc[j][1]);
      STAMP(1);
      if (lane < 32 && r16 < 8) {
        LAS char* qp = L + L_QP + w * 512 + g4 * 256 + r16 * 32;
        *(LAS f4*)qp = f4{o[0], o[1], o[2], o[3]};
        *(LAS f4*)(qp + 16) = f4{o[4], o[5], o[6], o[7]};
      }
      if (lds_arrive(FL + F_QCNT, lane) == 2) {
        const f2 q = (*(LAS f2*)(L + L_QP + lane * 8) + *(LAS f2*)(L + L_QP + 512 + lane * 8)) + *(LAS f2*)(L + L_QP + 1024 + lane * 8);
        const u32 th = (u32)(tag >> 8), tl = (u32)tag & 0xffu;
        __builtin_amdgcn_raw_buffer_store_b128(v4u{((asu(q[0]) + 0x80u) & ~0xffu) | tl, th, ((asu(q[1]) + 0x80u) & ~0xffu) | tl, th}, rws,
                                               WS_QG + ((hb * 16 + kvh) * 4 + (jj & 3)) * 2048 + (jj >> 2) * 512 + l5 * 16, 0, AUX_SC1);
        STAMP(2);
        const int go = WS_QG + (bb * 16 + kvh) * 8192 + lane * 16;
        v4u g[8];
        __builtin_amdgcn_s_sleep(QSLEEP);
        NOUNROLL for (int n = 0; n < SPIN_GLB; ++n) {
#pragma unroll
          for (int k = 0; k < 8; ++k) g[k] = LD128(go, k * 1024);
          CBAR();
          u32 bad = 0, lo = 0;
#pragma unroll
          for (int k = 0; k < 8; ++k) { bad |= (g[k][1] ^ th) | (g[k][3] ^ th); lo |= (g[k][0] ^ tl) | (g[k][2] ^ tl); }
          bad |= lo & 0xffu;
          if (__builtin_amdgcn_ballot_w64(bad != 0) == 0) break;
          __builtin_amdgcn_s_sleep(4);
        }
#pragma unroll
        for (int c = 0; c < 2; ++c) {
          const float a0 = (asf(g[c][0] & ~0xffu) + asf(g[2 + c][0] & ~0xffu)) + (asf(g[4 + c][0] & ~0xffu) + asf(g[6 + c][0] & ~0xffu));
          const float a1 = (asf(g[c][2] & ~0xffu) + asf(g[2 + c][2] & ~0xffu)) + (asf(g[4 + c][2] & ~0xffu) + asf(g[6 + c][2] & ~0xffu));
          *(LAS u32*)(L + L_Q + c * 256 + lane * 4) = cvtpk(a0, a1);
        }
        lds_arrive(FL + F_QRDY, lane);
        STAMP(3);
      }
    } else if (w == 3) {
      float s = 0;
#pragma unroll
      for (int e = 0; e < 4; ++e) s += (blo(t[0][e]) + bhi(t[0][e])) + (blo(t[1][e]) + bhi(t[1][e])) + (blo(t[2][e]) + bhi(t[2][e]));
      s += dpp<XOR1>(s); s += dpp<XOR2>(s); s += dpp<HMIR>(s); s += dpp<ROR8>(s); s = x16_sum(s);
      const float mean = s * (1.f / D);
      float v = 0;
#pragma unroll
      for (int i = 0; i < 3; ++i)
#pragma unroll
        for (int e = 0; e < 4; ++e) { const float a = blo(t[i][e]) - mean, b = bhi(t[i][e]) - mean; v += a * a + b * b; }
      v = l5 < 24 ? v : 0.f;
      v += dpp<XOR1>(v); v += dpp<XOR2>(v); v += dpp<HMIR>(v); v += dpp<ROR8>(v); v = x16_sum(v);
      const float mm = rbf(mean);
      const float rr = __builtin_amdgcn_rcpf(rbf(__builtin_amdgcn_sqrtf(rbf(rbf(v * (1.f / D)) + 1e-5f))));
      if (l5 < 24) {
#pragma unroll
        for (int i = 0; i < 3; ++i) {
          v4u a;
#pragma unroll
          for (int e = 0; e < 4; ++e) { u32 c = cvtpk(blo(t[i][e]) - mm, bhi(t[i][e]) - mm); a[e] = cvtpk(blo(c) * rr, bhi(c) * rr); }
          *(LAS v4u*)(L + L_XN + hb * (D * 2) + i * 384 + l5 * 16) = a;
        }
      } else if (hb) *(LAS v4u*)(L + L_XN + 2 * D * 2 + (l5 - 24) * 16) = v4u{0, 0, 0, 0};
      lds_arrive(FL + F_XN, lane);
      STAMP(1);
      const v4u s4 = {SENT, SENT, SENT, SENT};
      __builtin_amdgcn_raw_buffer_store_b64(v2u{SENT, SENT}, rws, WS_OP + (2 * kvh * 8 + (lane >> 2)) * 512 + jj * 32 + (lane & 3) * 8, 0, AUX_SC1);
      __builtin_amdgcn_raw_buffer_store_b64(v2u{SENT, SENT}, rws, lane < 2 ? WS_ML + ((2 * kvh + lane) * 16 + jj) * 8 : OOB, 0, AUX_SC1);
      if (isX) __builtin_amdgcn_raw_buffer_store_b128(s4, rws, lane < 32 ? WS_AP + (xc * 8 + xcd) * 512 + lane * 16 : OOB, 0, AUX_SC1);
      if (isY) {
        const int mo = WS_MP + (lane >> 2) * 9216 + yi * 64 + (lane & 3) * 16;
#pragma unroll
        for (int n = 0; n < 4; ++n) __builtin_amdgcn_raw_buffer_store_b128(s4, rws, mo, n * 16 * 9216, AUX_SC1);
        __builtin_amdgcn_raw_buffer_store_b128(s4, rws, lane < 32 ? mo : OOB, 4 * 16 * 9216, AUX_SC1);
      }
      SCHED();
      asm volatile("s_waitcnt vmcnt(0)" ::: "memory");
      if (lane == 0) __builtin_amdgcn_raw_buffer_store_b64(v2u{(u32)tag, (u32)(tag >> 32)}, rws, WS_FLAGS + bid * 8, 0, AUX_SC1);
      lds_arrive(FL + F_CLR, lane);
      STAMP(2);
    }
    yt = w < 4 ? w + 4 : w - 12;
    if (isX) {
      const int xw = w < 4 ? w : w - 8, hh = 4 * xcd + (xw >> 1), hf = xw & 1;
      lds_wait(FL + F_VISS, ((xw + 4) & 7) + 1);
      const rsrc_t ro = mkrs(wo, QD * D * 2);
      const int voff = (hh * 128 + hf * 64 + g4 * 16) * (D * 2) + (xc * 64 + r16 * 4) * 2;
      v2u u[16];
#pragma unroll
      for (int i = 0; i < 16; ++i) u[i] = __builtin_amdgcn_raw_buffer_load_b64(ro, voff, i * (D * 2), 0);
      SCHED();
      STAMP(4);
      waitvm<0>();
      lds_arrive(FL + F_VISS, lane);
      lds_wait(FL + F_INOK, 1);
      v2u ml;
      NOUNROLL for (int n = 0; n < SPIN_GLB; ++n) {
        ml = __builtin_amdgcn_raw_buffer_load_b64(rws, WS_ML + (hh * 16 + r16) * 8, 0, AUX_SC1);
        CBAR();
        if (none_sent(umax(ml[0], ml[1]))) break;
        __builtin_amdgcn_s_sleep(4);
      }
      float M = asf(ml[0]);
      M = fmaxf(M, dpp<XOR1>(M)); M = fmaxf(M, dpp<XOR2>(M)); M = fmaxf(M, dpp<HMIR>(M));
      const float e = __builtin_amdgcn_exp2f(asf(ml[0]) - M);
      float Ls = e * asf(ml[1]);
      Ls += dpp<XOR1>(Ls); Ls += dpp<XOR2>(Ls); Ls += dpp<HMIR>(Ls);
      const float wr = e * __builtin_amdgcn_rcpf(Ls);
      float wg[4];
#pragma unroll
      for (int i = 0; i < 4; ++i) wg[i] = __shfl(wr, g4 * 4 + i);
      v4u f0[4], f1[4];
      tr4(u, f0); tr4(u + 8, f1);
      PIN4(f0); PIN4(f1); PIN4(wg);
      STAMP(5);
      lds_wait(FL + F_OCNT, 4);
      v4u a0, a1;
      const int ao = WS_OP + (hh * 8 + hf * 4 + g4) * 512 + r16 * 32;
      NOUNROLL for (int n = 0; n < SPIN_GLB; ++n) {
        a0 = LD128(ao, 0);
        a1 = LD128(ao + 16, 0);
        CBAR();
        if (none_sent(mx4(a1, mx4(a0, 0)))) break;
        __builtin_amdgcn_s_sleep(1);
      }
      STAMP(6);
      f4 o;
#pragma unroll
      for (int j = 0; j < 4; ++j) {
        f4 d = mfma(a0, f0[j], f4{0, 0, 0, 0});
        d = mfma(a1, f1[j], d);
        o[j] = x16_sum((wg[0] * d[0] + wg[1] * d[1]) + (wg[2] * d[2] + wg[3] * d[3]));
      }
      if (!(g4 & 1)) *(LAS f4*)(L + L_R + xw * 512 + ((r16 >> 1) & 3) * 128 + (g4 >> 1) * 64 + (r16 & 1) * 32 + (r16 >> 3) * 16) = o;
      if (lds_arrive(FL + F_RCNT, lane) == 7) {
        LAS char* rp = L + L_R + hb * 2048 + l5 * 16;
        f4 s = (*(LAS f4*)(rp) + *(LAS f4*)(rp + 512)) + (*(LAS f4*)(rp + 1024) + *(LAS f4*)(rp + 1536));
#pragma unroll
        for (int j = 0; j < 4; ++j) s[j] = x32_sum(s[j]);
        __builtin_amdgcn_raw_buffer_store_b128(nosent4(s), rws, lane < 32 ? WS_AP + (xc * 8 + xcd) * 512 + lane * 16 : OOB, 0, AUX_SC1);
        STAMP(7);
        const int z = xcd * 9 + xc, zb = z / 36, zz = z % 36;
        const int col = (zz >> 3) * 128 + ((zz >> 1) & 3) * 32 + (lane & 3) * 8 + (zz & 1) * 4;
        const v2u xv = __builtin_amdgcn_raw_buffer_load_b64(rx, (zb * D + col) * 2, 0, 0);
        const int c6 = col & 63;
        const int ho = WS_AP + ((col >> 6) * 8 + ((lane >> 2) & 7)) * 512 + ((c6 >> 3) & 3) * 128 + zb * 64 + ((c6 >> 2) & 1) * 32 + (c6 >> 5) * 16;
        v4u hv;
        NOUNROLL for (int n = 0; n < SPIN_GLB; ++n) {
          hv = LD128(ho, 0);
          CBAR();
          if (none_sent(mx4(hv, 0))) break;
          __builtin_amdgcn_s_sleep(2);
        }
        float h[4];
#pragma unroll
        for (int j = 0; j < 4; ++j) { float a = asf(hv[j]); a += dpp<ROR4>(a); a += dpp<ROR8>(a); h[j] = x16_sum(a); }
        h[0] = rbf(blo(xv[0]) + rbf(h[0])); h[1] = rbf(bhi(xv[0]) + rbf(h[1]));
        h[2] = rbf(blo(xv[1]) + rbf(h[2])); h[3] = rbf(bhi(xv[1]) + rbf(h[3]));
        PIN4(h);
        v4u p[9];
        NOUNROLL for (int n = 0; n < SPIN_GLB; ++n) {
#pragma unroll
          for (int k = 0; k < 9; ++k) p[k] = LD128(WS_MP + z * 9216 + lane * 16, k * 1024);
          CBAR();
          u32 m = 0;
#pragma unroll
          for (int k = 0; k < 9; ++k) m = mx4(p[k], m);
          if (none_sent(m)) break;
          __builtin_amdgcn_s_sleep(1);
        }
        STAMP(1);
        float r[4];
#pragma unroll
        for (int j = 0; j < 4; ++j) {
          float a = ((asf(p[0][j]) + asf(p[1][j])) + (asf(p[2][j]) + asf(p[3][j]))) + ((asf(p[4][j]) + asf(p[5][j])) + (asf(p[6][j]) + asf(p[7][j]))) + asf(p[8][j]);
          a += dpp<ROR4>(a); a += dpp<ROR8>(a);
          r[j] = h[j] + rbf(x32_sum(x16_sum(a)));
        }
        if (lane < 4) *(GAS v2u*)(out + zb * D + col) = v2u{cvtpk(r[0], r[1]), cvtpk(r[2], r[3])};
        if (z == 0 && lane == 0) *(GAS u64*)(ws + WS_EPOCH) = tag;
        STAMP(2);
      }
      STAMP_FLUSH();
      return;
    }
  }
  if (isY) {
    if (yt >= 0) {
      lds_wait(FL + F_VISS, yt + 1);
      if (ii & 1) yt = 8 - yt;
      const GAS char* wp = (const GAS char*)((lane & 4) ? w3 : w1) + (size_t)((yt * 64 + (r16 >> 3) * 32 + g4 * 8) * (FF * 2) + (yi * 32 + (lane & 3) * 8) * 2);
#pragma unroll
      for (int i = 0; i < 8; ++i) t[i] = *(const GAS v4u*)(wp + i * (FF * 2));
      const v2u xv = __builtin_amdgcn_raw_buffer_load_b64(rx, (((lane >> 2) & 1) * D + yt * 64 + (lane & 1) * 32 + g4 * 8 + ((lane >> 1) & 1) * 4) * 2, 0, 0);
      SCHED();
      STAMP(4);
      waitvm<0>();
      lds_arrive(FL + F_VISS, lane);
      lds_wait(FL + F_INOK, 1);
      v4u f[8];
      tr8(t, f);
      PIN8(f);
      STAMP(5);
      const int ao = WS_AP + (yt * 8 + (r16 >> 3)) * 512 + g4 * 128 + (lane & 7) * 16;
      const int kx = (ii & 3) * 1024;
      v4u p[4];
      NOUNROLL for (int n = 0; n < SPIN_GLB; ++n) {
#pragma unroll
        for (int k = 0; k < 4; ++k) p[k] = LD128(ao, (k * 1024) ^ kx);
        CBAR();
        if (none_sent(mx4(p[3], mx4(p[2], mx4(p[1], mx4(p[0], 0)))))) break;
        NOUNROLL for (; n < SPIN_GLB && !none_sent(mx4(p[0], 0)); ++n) {
          __builtin_amdgcn_s_sleep(3);
          p[0] = LD128(ao, kx);
          CBAR();
        }
      }
      STAMP(6);
      float h[4];
#pragma unroll
      for (int j = 0; j < 4; ++j) { float a = (asf(p[0][j]) + asf(p[1][j])) + (asf(p[2][j]) + asf(p[3][j])); h[j] = a + dpp<ROR8>(a); }
      const u32 c0 = cvtpk(h[0], h[1]), c1 = cvtpk(h[2], h[3]);
      const u32 h0 = cvtpk(blo(xv[0]) + blo(c0), bhi(xv[0]) + bhi(c0)), h1 = cvtpk(blo(xv[1]) + blo(c1), bhi(xv[1]) + bhi(c1));
      const u32 h2 = dppu<XOR2>(h0), h3 = dppu<XOR2>(h1);
      const bool one = lane & 8;
      const v4u a = {one ? 0x3F803F80u : h0, one ? 0x3F803F80u : h1, one ? 0x3F803F80u : h2, one ? 0x3F803F80u : h3};
      f4 acc[8];
#pragma unroll
      for (int j = 0; j < 8; ++j) acc[j] = mfma(a, f[j], f4{0, 0, 0, 0});
      const f4 gm = mfma(a, a, f4{0, 0, 0, 0});
      float o[8];
#pragma unroll
      for (int j = 0; j < 8; ++j) o[j] = acc[j][0] + dpp<ROR8>(acc[j][1]);
      if (r16 < 8) {
        LAS char* yp = L + L_R + yt * 1024 + ((lane & 3) * 16 + g4 * 4 + (r16 >> 2) * 2) * 16;
        *(LAS f4*)yp = f4{o[0], o[1], o[2], o[3]};
        *(LAS f4*)(yp + 16) = f4{o[4], o[5], o[6], o[7]};
      }
      const float sv = gm[0] + dpp<SHL1>(gm[1]);
      if (g4 < 2 && (r16 == 8 || r16 == 4 * g4)) *(LAS float*)(L + L_ST + ((r16 == 8 ? 0 : 2) + g4) * 64 + yt * 4) = sv;
      lds_arrive(FL + F_RCNT, lane);
      STAMP(7);
    } else if (v2 >= 0) {
      lds_wait(FL + F_VISS, v2 < 4 ? 10 + v2 : 10);
      const rsrc_t r2 = mkrs(w2, FF * D * 2);
      const int voff = (v2 < 4 || r16 < 8) ? (yi * 32 + g4 * 8) * (D * 2) + (v2 * 128 + r16 * 8) * 2 : OOB;
#pragma unroll
      for (int i = 0; i < 8; ++i) t[i] = __builtin_amdgcn_raw_buffer_load_b128(r2, voff, i * (D * 2), 0);
      SCHED();
      v4u f[8];
      tr8(t, f);
      PIN8(f);
      STAMP(5);
      lds_wait(FL + F_RCNT, 9);
      STAMP(6);
      float st = r16 < 9 ? *(LAS float*)(L + L_ST + lane * 4) : 0.f;
      f4 s = *(LAS f4*)(L + L_R + lane * 16);
#pragma unroll
      for (int k = 1; k < 9; ++k) s += *(LAS f4*)(L + L_R + k * 1024 + lane * 16);
      st += dpp<XOR1>(st); st += dpp<XOR2>(st); st += dpp<HMIR>(st); st += dpp<ROR8>(st);
      const float a0 = asf(__builtin_amdgcn_readlane(asu(st), 0)), a1 = asf(__builtin_amdgcn_readlane(asu(st), 16));
      const float q0 = asf(__builtin_amdgcn_readlane(asu(st), 32)), q1 = asf(__builtin_amdgcn_readlane(asu(st), 48));
      const float mean = ((lane & 4) ? a1 : a0) * (1.f / D);
      const float var = ((lane & 4) ? q1 : q0) * (1.f / D) - mean * mean;
      const float rs = __builtin_amdgcn_rsqf(fmaxf(var, 0.f) + 1e-5f);
      float hid[4];
#pragma unroll
      for (int j = 0; j < 4; ++j) {
        const float zv = rs * (s[j] - mean * dpp<ROR8>(s[j]));
        const float up = dpp<XOR2>(zv);
        hid[j] = zv * __builtin_amdgcn_rcpf(1.f + __builtin_amdgcn_exp2f(-1.4426950408889634f * zv)) * up;
      }
      const u32 h0 = cvtpk(hid[0], hid[1]), h1 = cvtpk(hid[2], hid[3]);
      const v4u a = {h0, h1, dppu<XOR1>(h0), dppu<XOR1>(h1)};
      f4 acc[8];
#pragma unroll
      for (int j = 0; j < 8; ++j) acc[j] = mfma(a, f[j], f4{0, 0, 0, 0});
      const int mo = (g4 < 2 && (v2 < 4 || r16 < 8)) ? WS_MP + (g4 * 36 + v2 * 8 + (r16 >> 2) * 2) * 9216 + yi * 64 + (lane & 3) * 16 : OOB;
      __builtin_amdgcn_raw_buffer_store_b128(nosent4(f4{acc[0][0], acc[1][0], acc[2][0], acc[3][0]}), rws, mo, 0, AUX_SC1);
      __builtin_amdgcn_raw_buffer_store_b128(nosent4(f4{acc[4][0], acc[5][0], acc[6][0], acc[7][0]}), rws, mo, 9216, AUX_SC1);
      STAMP(7);
    }
  }
  STAMP_FLUSH();
}

extern "C" void kernel_launch(void* const* d_in, const int* in_sizes, int n_in, void* d_out, int out_size, void* d_ws, size_t ws_size,
                              hipStream_t stream) {
  fused_decode<<<256, 1024, 0, stream>>>((const u16*)d_in[0], (const u16*)d_in[1], (const u16*)d_in[2], (const u16*)d_in[3],
                                         (const u16*)d_in[4], (const u16*)d_in[6], (const u16*)d_in[7], (const u16*)d_in[8], (u16*)d_out,
                                         (char*)d_ws);
}
```

```cpp
#include <hip/hip_runtime.h>
#include <hip/hip_bf16.h>
#include <stdint.h>

#define GAS __attribute__((address_space(1)))
#define LAS __attribute__((address_space(3)))
#define CAS __attribute__((address_space(4)))
typedef unsigned u32;
typedef unsigned long long u64;
typedef unsigned short u16;
typedef u32 v4u __attribute__((ext_vector_type(4)));
typedef u32 v2u __attribute__((ext_vector_type(2)));
typedef float f4 __attribute__((ext_vector_type(4)));
typedef float f2 __attribute__((ext_vector_type(2)));
typedef __bf16 bf8 __attribute__((ext_vector_type(8)));
typedef __bf16 bf2 __attribute__((ext_vector_type(2)));
typedef __amdgpu_buffer_rsrc_t rsrc_t;

#define QSLEEP 12
#define KWN 4
#define VPRE 4
#define VWN 5
#define VCRED 2
constexpr int D = 576, SK = 8192, WIN = 2048, FF = 4608, QD = 4096;
constexpr int WS_EPOCH = 0, WS_FLAGS = 4096, WS_ML = 8192, WS_OP = 12288, WS_AP = WS_OP + 131072, WS_MP = WS_AP + 36864,
              WS_QG = WS_MP + 663552, WS_END = WS_QG + 262144;
constexpr int WS_STAMP = 2 << 20;
constexpr u32 SENT = 0xFFFFFFFFu;
constexpr int AUX_NT = 2, AUX_SC1 = 16, OOB = 1 << 30;
constexpr int SPIN_LDS = 1 << 18, SPIN_GLB = 1 << 15;
constexpr int L_FLAG = 0;
constexpr int L_Q = 128;
constexpr int L_P = 640;
constexpr int L_MX = 1664;
constexpr int L_ST = 1792;
constexpr int L_QP = 2048;
constexpr int L_XN = 3584;
constexpr int L_O = 6144;
constexpr int L_R = 10240;
constexpr int L_STAMP = L_R + 9216;
constexpr int L_END = L_STAMP + 1024;
enum { F_QCNT = 0, F_QRDY, F_MXCNT, F_PCNT, F_OCNT, F_VISS, F_CLR, F_INOK, F_RCNT, F_XN };

__device__ __forceinline__ rsrc_t mkrs(const void* p, int bytes) { return __builtin_amdgcn_make_buffer_rsrc((void*)p, 0, bytes, 0x00020000); }
__device__ __forceinline__ float asf(u32 u) { return __uint_as_float(u); }
__device__ __forceinline__ u32 asu(float f) { return __float_as_uint(f); }
__device__ __forceinline__ u32 cvtpk(float lo, float hi) { return __builtin_bit_cast(u32, __builtin_convertvector(f2{lo, hi}, bf2)); }
__device__ __forceinline__ float rbf(float f) { return asf(cvtpk(f, f) & 0xffff0000u); }
__device__ __forceinline__ float blo(u32 u) { return asf(u << 16); }
__device__ __forceinline__ float bhi(u32 u) { return asf(u & 0xffff0000u); }
template <int C> __device__ __forceinline__ u32 dppu(u32 x) { return (u32)__builtin_amdgcn_mov_dpp((int)x, C, 0xf, 0xf, true); }
template <int C> __device__ __forceinline__ float dpp(float x) { return asf(dppu<C>(asu(x))); }
constexpr int XOR1 = 0xB1, XOR2 = 0x4E, HMIR = 0x141, ROR4 = 0x124, ROR8 = 0x128, SHL1 = 0x101;
__device__ __forceinline__ float x16_sum(float x) { auto s = __builtin_amdgcn_permlane16_swap(asu(x), asu(x), false, false); return asf(s[0]) + asf(s[1]); }
__device__ __forceinline__ float x32_sum(float x) { auto s = __builtin_amdgcn_permlane32_swap(asu(x), asu(x), false, false); return asf(s[0]) + asf(s[1]); }
__device__ __forceinline__ float x16_max(float x) { auto s = __builtin_amdgcn_permlane16_swap(asu(x), asu(x), false, false); return fmaxf(asf(s[0]), asf(s[1])); }
__device__ __forceinline__ float x32_max(float x) { auto s = __builtin_amdgcn_permlane32_swap(asu(x), asu(x), false, false); return fmaxf(asf(s[0]), asf(s[1])); }
__device__ __forceinline__ f4 mfma(v4u a, v4u b, f4 c) { return __builtin_amdgcn_mfma_f32_16x16x32_bf16(__builtin_bit_cast(bf8, a), __builtin_bit_cast(bf8, b), c, 0, 0, 0); }
__device__ __forceinline__ u32 plo(u32 a, u32 b) { return __builtin_amdgcn_perm(b, a, 0x05040100u); }
__device__ __forceinline__ u32 phi(u32 a, u32 b) { return __builtin_amdgcn_perm(b, a, 0x07060302u); }
__device__ __forceinline__ void tr8(const v4u* r, v4u* f) {
#pragma unroll
  for (int e = 0; e < 4; ++e) {
    f[2 * e] = v4u{plo(r[0][e], r[1][e]), plo(r[2][e], r[3][e]), plo(r[4][e], r[5][e]), plo(r[6][e], r[7][e])};
    f[2 * e + 1] = v4u{phi(r[0][e], r[1][e]), phi(r[2][e], r[3][e]), phi(r[4][e], r[5][e]), phi(r[6][e], r[7][e])};
  }
}
__device__ __forceinline__ void tr4(const v2u* r, v4u* f) {
#pragma unroll
  for (int e = 0; e < 2; ++e) {
    f[2 * e] = v4u{plo(r[0][e], r[1][e]), plo(r[2][e], r[3][e]), plo(r[4][e], r[5][e]), plo(r[6][e], r[7][e])};
    f[2 * e + 1] = v4u{phi(r[0][e], r[1][e]), phi(r[2][e], r[3][e]), phi(r[4][e], r[5][e]), phi(r[6][e], r[7][e])};
  }
}
__device__ __forceinline__ u32 umax(u32 a, u32 b) { return a > b ? a : b; }
__device__ __forceinline__ u32 nosent(u32 x) { return x < 0xFFFFFFFEu ? x : 0xFFFFFFFEu; }
__device__ __forceinline__ v4u nosent4(f4 v) { return v4u{nosent(asu(v[0])), nosent(asu(v[1])), nosent(asu(v[2])), nosent(asu(v[3]))}; }
__device__ __forceinline__ u32 mx4(v4u v, u32 m) { return umax(umax(umax(v[0], v[1]), m), umax(v[2], v[3])); }
__device__ __forceinline__ bool none_sent(u32 m) { return __builtin_amdgcn_ballot_w64(m == SENT) == 0; }
#define PIN4(f) asm volatile("" : "+v"(f[0]), "+v"(f[1]), "+v"(f[2]), "+v"(f[3]))
#define PIN8(f) asm volatile("" : "+v"(f[0]), "+v"(f[1]), "+v"(f[2]), "+v"(f[3]), "+v"(f[4]), "+v"(f[5]), "+v"(f[6]), "+v"(f[7]))
#define CBAR() asm volatile("" ::: "memory")
#define SCHED() __builtin_amdgcn_sched_barrier(0)
template <int N> __device__ __forceinline__ void waitvm() { asm volatile("s_waitcnt vmcnt(%0)" ::"i"(N) : "memory"); }
#define NOUNROLL _Pragma("nounroll")
#define LD128(off, soff) __builtin_amdgcn_raw_buffer_load_b128(rws, off, soff, AUX_SC1)

__device__ __forceinline__ u32 lds_ld(LAS u32* p) { return __hip_atomic_load(p, __ATOMIC_RELAXED, __HIP_MEMORY_SCOPE_WORKGROUP); }
__device__ __forceinline__ void lds_wait(LAS u32* p, u32 want) {
  CBAR();
  NOUNROLL for (int n = 0; n < SPIN_LDS; ++n) {
    if ((u32)__builtin_amdgcn_readfirstlane((int)lds_ld(p)) >= want) break;
    __builtin_amdgcn_s_sleep(1);
  }
  CBAR();
}
__device__ __forceinline__ u32 lds_arrive(LAS u32* p, int lane) {
  CBAR();
  u32 old = 0;
  if (lane == 0) old = __hip_atomic_fetch_add(p, 1u, __ATOMIC_RELAXED, __HIP_MEMORY_SCOPE_WORKGROUP);
  old = (u32)__builtin_amdgcn_readfirstlane((int)old);
  CBAR();
  return old;
}

#define STAMP(k) do {} while (0)
#define STAMP_INIT() do {} while (0)
#define STAMP_FLUSH() do {} while (0)

__global__ void __launch_bounds__(1024) fused_decode(const u16* __restrict__ x, const u16* __restrict__ kc, const u16* __restrict__ vc,
                                                     const u16* __restrict__ wq, const u16* __restrict__ wo, const u16* __restrict__ w1,
                                                     const u16* __restrict__ w3, const u16* __restrict__ w2, u16* __restrict__ out,
                                                     char* ws) {
  __shared__ __attribute__((aligned(16))) char smem[L_END];
  LAS char* L = (LAS char*)smem;
  LAS u32* FL = (LAS u32*)(L + L_FLAG);
  const int bid = blockIdx.x, lane = threadIdx.x & 63, w = __builtin_amdgcn_readfirstlane(threadIdx.x >> 6);
  const int xcd = bid & 7, ii = bid >> 3;
  const int kvh = 2 * xcd + (ii >> 4), jj = ii & 15, bb = jj >> 3;
  const bool isY = ii < 18, isX = ii >= 18 && ii < 27;
  const int yi = xcd * 18 + ii, xc = ii - 18;
  const int g4 = lane >> 4, r16 = lane & 15, hb = lane >> 5, l5 = lane & 31;
  STAMP_INIT();
  STAMP(0);
  if (threadIdx.x < 32) FL[lane] = 0;
  asm volatile("s_waitcnt lgkmcnt(0)\n\ts_barrier" ::: "memory");
  asm volatile("" ::"s"(x), "s"(kc), "s"(vc), "s"(wq), "s"(wo), "s"(w1), "s"(w3), "s"(w2), "s"(out), "s"(ws));
  const rsrc_t rws = mkrs(ws, WS_END);
  const rsrc_t rx = mkrs(x, 2 * D * 2);

  int yt = -1;
  int v2 = -1;
  const int kw = w & 3;
  v4u t[16];
  if (w < 4) {
    const char* bp; int bytes, voff, step;
    if (w < 3) {
      bp = (const char*)wq; bytes = D * QD * 2; step = QD * 2;
      const int k0 = w * 64 + (r16 >> 3) * 32 + g4 * 8;
      voff = k0 < 144 ? ((jj & 3) * 144 + k0) * (QD * 2) + (kvh * 256 + (jj >> 2) * 64 + (lane & 7) * 8) * 2 : OOB;
    } else {
      bp = (const char*)x; bytes = 2 * D * 2; step = 384;
      voff = l5 < 24 ? hb * (D * 2) + l5 * 16 : OOB;
    }
    const rsrc_t rk = mkrs(bp, bytes);
#pragma unroll
    for (int i = 0; i < 3; ++i) t[i] = __builtin_amdgcn_raw_buffer_load_b128(rk, voff, i * step, AUX_NT);
    if (w != 3) {
#pragma unroll
      for (int i = 3; i < 8; ++i) t[i] = __builtin_amdgcn_raw_buffer_load_b128(rk, voff, i * step, AUX_NT);
    }
    SCHED();
  } else if (w < 12) {
    const char* bp = (const char*)(w < 8 ? kc : vc) + ((size_t)(bb * 16 + kvh) * SK + (SK - WIN) + (jj & 7) * 256 + kw * 64) * 256;
    const int rot = bid * 5;
    const int voff = w < 8 ? ((r16 >> 2) * 256 + (g4 * 4 + (lane & 3)) * 16) : lane * 16;
    const rsrc_t rk = mkrs(bp, 64 * 256);
    if (w < 8) {
#pragma unroll
      for (int i = 0; i < 16; ++i) {
        if (i >= KWN) waitvm<KWN - 1>();
        t[i] = __builtin_amdgcn_raw_buffer_load_b128(rk, voff, ((i + rot) & 15) * 1024, AUX_NT);
        SCHED();
      }
    } else {
#pragma unroll
      for (int i = 0; i < 16; ++i) {
        if (i >= 1 && i <= VPRE) waitvm<0>();
        else if (i >= VPRE + VWN) waitvm<VWN - 1>();
        t[i] = __builtin_amdgcn_raw_buffer_load_b128(rk, voff, ((i + rot) & 15) * 1024, AUX_NT);
        SCHED();
      }
    }
  } else if (w == 12) {
    u64 pc; asm volatile("s_getpc_b64 %0" : "=s"(pc));
    const GAS v4u* cp = (const GAS v4u*)((pc & ~(u64)1023) + (u64)(ii & 15) * 1024) + lane;
    v4u c = *cp;
    asm volatile("" ::"v"(c));
  } else if (w == 13) {
    const u16* mp = g4 == 0 ? wo : g4 == 1 ? w1 : g4 == 2 ? w3 : w2;
    const int pg = ii + 32 * r16;
    if (pg < (g4 == 0 ? 72 : 81)) {
      u32 c = __builtin_nontemporal_load((const GAS u32*)((const GAS char*)mp + (size_t)pg * 65536));
      asm volatile("" ::"v"(c));
    }
  }
  const u64 tag = *(const CAS u64*)(ws + WS_EPOCH) + 1;

  if (w >= 4 && w < 8) {
    STAMP(1);
    lds_wait(FL + F_QRDY, 1);
    STAMP(2);
    v4u qf = *(LAS v4u*)(L + L_Q + ((r16 >> 2) & 1) * 256 + (g4 * 4 + (lane & 3)) * 16);
    const v4u z4 = {0, 0, 0, 0};
    const v4u q0 = r16 < 8 ? qf : z4, q1 = r16 < 8 ? z4 : qf;
    float sc[8];
#pragma unroll
    for (int m = 0; m < 8; ++m) {
      f4 d = mfma(t[m], q0, f4{0, 0, 0, 0});
      d = mfma(t[m + 8], q1, d);
      sc[m] = (dpp<0x00>(d[0]) + dpp<0x55>(d[1])) + (dpp<0xAA>(d[2]) + dpp<0xFF>(d[3]));
    }
    const bool b0 = lane & 1, b1 = lane & 2;
    float s0 = b1 ? (b0 ? sc[6] : sc[4]) : (b0 ? sc[2] : sc[0]);
    float s1 = b1 ? (b0 ? sc[7] : sc[5]) : (b0 ? sc[3] : sc[1]);
    u32 pk = cvtpk(s0, s1);
    pk = cvtpk(blo(pk) * 0.08838834764831845f, bhi(pk) * 0.08838834764831845f);
    s0 = blo(pk) * 1.4426950408889634f; s1 = bhi(pk) * 1.4426950408889634f;
    float mx = fmaxf(s0, s1);
    mx = fmaxf(mx, dpp<XOR1>(mx)); mx = fmaxf(mx, dpp<XOR2>(mx)); mx = fmaxf(mx, dpp<ROR8>(mx));
    mx = x32_max(x16_max(mx));
    const int hd = (r16 >> 2) & 1;
    LAS float* MX = (LAS float*)(L + L_MX);
    if ((lane & 0x3B) == 0) MX[hd * 4 + kw] = mx;
    lds_arrive(FL + F_MXCNT, lane);
    lds_wait(FL + F_MXCNT, 4);
    f4 m4 = *(LAS f4*)(MX + hd * 4);
    mx = fmaxf(fmaxf(m4[0], m4[1]), fmaxf(m4[2], m4[3]));
    const float p0 = __builtin_amdgcn_exp2f(s0 - mx), p1 = __builtin_amdgcn_exp2f(s1 - mx);
    *(LAS u32*)(L + L_P + kw * 256 + hd * 128 + (r16 >> 3) * 64 + g4 * 16 + (lane & 3) * 4) = cvtpk(p0, p1);
    float ls = p0 + p1;
    ls += dpp<XOR1>(ls); ls += dpp<XOR2>(ls); ls += dpp<ROR8>(ls);
    ls = x32_sum(x16_sum(ls));
    if ((lane & 0x3B) == 0) MX[8 + hd * 4 + kw] = ls;
    if (lds_arrive(FL + F_PCNT, lane) == 3) {
      f4 l4 = *(LAS f4*)(MX + 8 + hd * 4);
      lds_wait(FL + F_CLR, 1);
      if ((lane & 0x3B) == 0)
        __builtin_amdgcn_raw_buffer_store_b64(v2u{nosent(asu(mx)), nosent(asu((l4[0] + l4[1]) + (l4[2] + l4[3])))}, rws,
                                              WS_ML + ((2 * kvh + hd) * 16 + jj) * 8, 0, AUX_SC1);
    }
    STAMP(3);
    if (kw == 3 && (isX || isY)) {
      const u32 tl = (u32)tag, th = (u32)(tag >> 32);
      NOUNROLL for (int n = 0; n < SPIN_GLB; ++n) {
        v4u g0 = LD128(WS_FLAGS + lane * 16, 0);
        v4u g1 = LD128(WS_FLAGS + lane * 16, 1024);
        CBAR();
        const u32 bad = ((g0[0] ^ tl) | (g0[1] ^ th) | (g0[2] ^ tl) | (g0[3] ^ th)) | ((g1[0] ^ tl) | (g1[1] ^ th) | (g1[2] ^ tl) | (g1[3] ^ th));
        if (__builtin_amdgcn_ballot_w64(bad != 0) == 0) break;
        __builtin_amdgcn_s_sleep(8);
      }
      lds_arrive(FL + F_INOK, lane);
      STAMP(4);
    }
    if (isY) { if (kw == 0) yt = 8; if (kw == 1) v2 = 4; }
  } else if (w >= 8 && w < 12) {
    STAMP(1);
    waitvm<VCRED>();
    lds_arrive(FL + F_VISS, lane);
    lds_wait(FL + F_PCNT, 4);
    STAMP(2);
    const v4u pa = *(LAS v4u*)(L + L_P + kw * 256 + ((r16 >> 2) & 1) * 128 + g4 * 16);
    const v4u pb = *(LAS v4u*)(L + L_P + kw * 256 + ((r16 >> 2) & 1) * 128 + 64 + g4 * 16);
    f4 acc[8];
    v4u f[8];
    tr8(t, f);
#pragma unroll
    for (int j = 0; j < 8; ++j) acc[j] = mfma(pa, f[j], f4{0, 0, 0, 0});
    SCHED();
    tr8(t + 8, f);
#pragma unroll
    for (int j = 0; j < 8; ++j) acc[j] = mfma(pb, f[j], acc[j]);
    STAMP(3);
    {
      const bool hi = g4 >= 2;
      f4 o = {hi ? acc[4][0] : acc[0][0], hi ? acc[5][0] : acc[1][0], hi ? acc[6][0] : acc[2][0], hi ? acc[7][0] : acc[3][0]};
      *(LAS f4*)(L + L_O + kw * 1024 + (g4 & 1) * 512 + r16 * 32 + (g4 >> 1) * 16) = o;
    }
    if (lds_arrive(FL + F_OCNT, lane) == 3) {
      f4 o = (*(LAS f4*)(L + L_O + lane * 16) + *(LAS f4*)(L + L_O + 1024 + lane * 16)) +
             (*(LAS f4*)(L + L_O + 2048 + lane * 16) + *(LAS f4*)(L + L_O + 3072 + lane * 16));
      lds_wait(FL + F_CLR, 1);
      __builtin_amdgcn_s_setprio(3);
      __builtin_amdgcn_raw_buffer_store_b64(v2u{nosent(cvtpk(o[0], o[1])), nosent(cvtpk(o[2], o[3]))}, rws,
                                            WS_OP + (2 * kvh * 8 + (lane >> 2)) * 512 + jj * 32 + (lane & 3) * 8, 0, AUX_SC1);
      __builtin_amdgcn_s_setprio(0);
    }
    STAMP(4);
    if (isY) v2 = kw;
  } else {
    if (w < 3) {
      v4u f[8];
      tr8(t, f);
      lds_wait(FL + F_XN, 1);
      const v4u a = *(LAS v4u*)(L + L_XN + ((r16 >> 2) & 1) * (D * 2) + ((jj & 3) * 144 + w * 64 + (lane & 1) * 32 + g4 * 8) * 2);
      f4 acc[8];
#pragma unroll
      for (int j = 0; j < 8; ++j) acc[j] = mfma(a, f[j], f4{0, 0, 0, 0});
      float o[8];
#pragma unroll
      for (int j = 0; j < 8; ++j) o[j] = acc[j][0] + dpp<ROR8>(acc[j][1]);
      STAMP(1);
      if (lane < 32 && r16 < 8) {
        LAS char* qp = L + L_QP + w * 512 + g4 * 256 + r16 * 32;
        *(LAS f4*)qp = f4{o[0], o[1], o[2], o[3]};
        *(LAS f4*)(qp + 16) = f4{o[4], o[5], o[6], o[7]};
      }
      if (lds_arrive(FL + F_QCNT, lane) == 2) {
        const f2 q = (*(LAS f2*)(L + L_QP + lane * 8) + *(LAS f2*)(L + L_QP + 512 + lane * 8)) + *(LAS f2*)(L + L_QP + 1024 + lane * 8);
        const u32 th = (u32)(tag >> 8), tl = (u32)tag & 0xffu;
        __builtin_amdgcn_raw_buffer_store_b128(v4u{((asu(q[0]) + 0x80u) & ~0xffu) | tl, th, ((asu(q[1]) + 0x80u) & ~0xffu) | tl, th}, rws,
                                               WS_QG + ((hb * 16 + kvh) * 4 + (jj & 3)) * 2048 + (jj >> 2) * 512 + l5 * 16, 0, AUX_SC1);
        STAMP(2);
        const int go = WS_QG + (bb * 16 + kvh) * 8192 + lane * 16;
        v4u g[8];
        __builtin_amdgcn_s_sleep(QSLEEP);
        NOUNROLL for (int n = 0; n < SPIN_GLB; ++n) {
#pragma unroll
          for (int k = 0; k < 8; ++k) g[k] = LD128(go, k * 1024);
          CBAR();
          u32 bad = 0, lo = 0;
#pragma unroll
          for (int k = 0; k < 8; ++k) { bad |= (g[k][1] ^ th) | (g[k][3] ^ th); lo |= (g[k][0] ^ tl) | (g[k][2] ^ tl); }
          bad |= lo & 0xffu;
          if (__builtin_amdgcn_ballot_w64(bad != 0) == 0) break;
          __builtin_amdgcn_s_sleep(4);
        }
#pragma unroll
        for (int c = 0; c < 2; ++c) {
          const float a0 = (asf(g[c][0] & ~0xffu) + asf(g[2 + c][0] & ~0xffu)) + (asf(g[4 + c][0] & ~0xffu) + asf(g[6 + c][0] & ~0xffu));
          const float a1 = (asf(g[c][2] & ~0xffu) + asf(g[2 + c][2] & ~0xffu)) + (asf(g[4 + c][2] & ~0xffu) + asf(g[6 + c][2] & ~0xffu));
          *(LAS u32*)(L + L_Q + c * 256 + lane * 4) = cvtpk(a0, a1);
        }
        lds_arrive(FL + F_QRDY, lane);
        STAMP(3);
      }
    } else if (w == 3) {
      float s = 0;
#pragma unroll
      for (int e = 0; e < 4; ++e) s += (blo(t[0][e]) + bhi(t[0][e])) + (blo(t[1][e]) + bhi(t[1][e])) + (blo(t[2][e]) + bhi(t[2][e]));
      s += dpp<XOR1>(s); s += dpp<XOR2>(s); s += dpp<HMIR>(s); s += dpp<ROR8>(s); s = x16_sum(s);
      const float mean = s * (1.f / D);
      float v = 0;
#pragma unroll
      for (int i = 0; i < 3; ++i)
#pragma unroll
        for (int e = 0; e < 4; ++e) { const float a = blo(t[i][e]) - mean, b = bhi(t[i][e]) - mean; v += a * a + b * b; }
      v = l5 < 24 ? v : 0.f;
      v += dpp<XOR1>(v); v += dpp<XOR2>(v); v += dpp<HMIR>(v); v += dpp<ROR8>(v); v = x16_sum(v);
      const float mm = rbf(mean);
      const float rr = __builtin_amdgcn_rcpf(rbf(__builtin_amdgcn_sqrtf(rbf(rbf(v * (1.f / D)) + 1e-5f))));
      if (l5 < 24) {
#pragma unroll
        for (int i = 0; i < 3; ++i) {
          v4u a;
#pragma unroll
          for (int e = 0; e < 4; ++e) { u32 c = cvtpk(blo(t[i][e]) - mm, bhi(t[i][e]) - mm); a[e] = cvtpk(blo(c) * rr, bhi(c) * rr); }
          *(LAS v4u*)(L + L_XN + hb * (D * 2) + i * 384 + l5 * 16) = a;
        }
      } else if (hb) *(LAS v4u*)(L + L_XN + 2 * D * 2 + (l5 - 24) * 16) = v4u{0, 0, 0, 0};
      lds_arrive(FL + F_XN, lane);
      STAMP(1);
      const v4u s4 = {SENT, SENT, SENT, SENT};
      __builtin_amdgcn_raw_buffer_store_b64(v2u{SENT, SENT}, rws, WS_OP + (2 * kvh * 8 + (lane >> 2)) * 512 + jj * 32 + (lane & 3) * 8, 0, AUX_SC1);
      __builtin_amdgcn_raw_buffer_store_b64(v2u{SENT, SENT}, rws, lane < 2 ? WS_ML + ((2 * kvh + lane) * 16 + jj) * 8 : OOB, 0, AUX_SC1);
      if (isX) __builtin_amdgcn_raw_buffer_store_b128(s4, rws, lane < 32 ? WS_AP + (xc * 8 + xcd) * 512 + lane * 16 : OOB, 0, AUX_SC1);
      if (isY) {
        const int mo = WS_MP + (lane >> 2) * 9216 + yi * 64 + (lane & 3) * 16;
#pragma unroll
        for (int n = 0; n < 4; ++n) __builtin_amdgcn_raw_buffer_store_b128(s4, rws, mo, n * 16 * 9216, AUX_SC1);
        __builtin_amdgcn_raw_buffer_store_b128(s4, rws, lane < 32 ? mo : OOB, 4 * 16 * 9216, AUX_SC1);
      }
      SCHED();
      asm volatile("s_waitcnt vmcnt(0)" ::: "memory");
      if (lane == 0) __builtin_amdgcn_raw_buffer_store_b64(v2u{(u32)tag, (u32)(tag >> 32)}, rws, WS_FLAGS + bid * 8, 0, AUX_SC1);
      lds_arrive(FL + F_CLR, lane);
      STAMP(2);
    }
    yt = w < 4 ? w + 4 : w - 12;
    if (isX) {
      const int xw = w < 4 ? w : w - 8, hh = 4 * xcd + (xw >> 1), hf = xw & 1;
      lds_wait(FL + F_VISS, ((xw + 4) & 7) + 1);
      const rsrc_t ro = mkrs(wo, QD * D * 2);
      const int voff = (hh * 128 + hf * 64 + g4 * 16) * (D * 2) + (xc * 64 + r16 * 4) * 2;
      v2u u[16];
#pragma unroll
      for (int i = 0; i < 16; ++i) u[i] = __builtin_amdgcn_raw_buffer_load_b64(ro, voff, i * (D * 2), AUX_NT);
      SCHED();
      STAMP(4);
      waitvm<0>();
      lds_arrive(FL + F_VISS, lane);
      lds_wait(FL + F_INOK, 1);
      v2u ml;
      NOUNROLL for (int n = 0; n < SPIN_GLB; ++n) {
        ml = __builtin_amdgcn_raw_buffer_load_b64(rws, WS_ML + (hh * 16 + r16) * 8, 0, AUX_SC1);
        CBAR();
        if (none_sent(umax(ml[0], ml[1]))) break;
        __builtin_amdgcn_s_sleep(4);
      }
      float M = asf(ml[0]);
      M = fmaxf(M, dpp<XOR1>(M)); M = fmaxf(M, dpp<XOR2>(M)); M = fmaxf(M, dpp<HMIR>(M));
      const float e = __builtin_amdgcn_exp2f(asf(ml[0]) - M);
      float Ls = e * asf(ml[1]);
      Ls += dpp<XOR1>(Ls); Ls += dpp<XOR2>(Ls); Ls += dpp<HMIR>(Ls);
      const float wr = e * __builtin_amdgcn_rcpf(Ls);
      float wg[4];
#pragma unroll
      for (int i = 0; i < 4; ++i) wg[i] = __shfl(wr, g4 * 4 + i);
      v4u f0[4], f1[4];
      tr4(u, f0); tr4(u + 8, f1);
      PIN4(f0); PIN4(f1); PIN4(wg);
      STAMP(5);
      lds_wait(FL + F_OCNT, 4);
      v4u a0, a1;
      const int ao = WS_OP + (hh * 8 + hf * 4 + g4) * 512 + r16 * 32;
      NOUNROLL for (int n = 0; n < SPIN_GLB; ++n) {
        a0 = LD128(ao, 0);
        a1 = LD128(ao + 16, 0);
        CBAR();
        if (none_sent(mx4(a1, mx4(a0, 0)))) break;
        __builtin_amdgcn_s_sleep(1);
      }
      STAMP(6);
      f4 o;
#pragma unroll
      for (int j = 0; j < 4; ++j) {
        f4 d = mfma(a0, f0[j], f4{0, 0, 0, 0});
        d = mfma(a1, f1[j], d);
        o[j] = x16_sum((wg[0] * d[0] + wg[1] * d[1]) + (wg[2] * d[2] + wg[3] * d[3]));
      }
      if (!(g4 & 1)) *(LAS f4*)(L + L_R + xw * 512 + ((r16 >> 1) & 3) * 128 + (g4 >> 1) * 64 + (r16 & 1) * 32 + (r16 >> 3) * 16) = o;
      if (lds_arrive(FL + F_RCNT, lane) == 7) {
        LAS char* rp = L + L_R + hb * 2048 + l5 * 16;
        f4 s = (*(LAS f4*)(rp) + *(LAS f4*)(rp + 512)) + (*(LAS f4*)(rp + 1024) + *(LAS f4*)(rp + 1536));
#pragma unroll
        for (int j = 0; j < 4; ++j) s[j] = x32_sum(s[j]);
        __builtin_amdgcn_raw_buffer_store_b128(nosent4(s), rws, lane < 32 ? WS_AP + (xc * 8 + xcd) * 512 + lane * 16 : OOB, 0, AUX_SC1);
        STAMP(7);
        const int z = xcd * 9 + xc, zb = z / 36, zz = z % 36;
        const int col = (zz >> 3) * 128 + ((zz >> 1) & 3) * 32 + (lane & 3) * 8 + (zz & 1) * 4;
        const v2u xv = __builtin_amdgcn_raw_buffer_load_b64(rx, (zb * D + col) * 2, 0, 0);
        const int c6 = col & 63;
        const int ho = WS_AP + ((col >> 6) * 8 + ((lane >> 2) & 7)) * 512 + ((c6 >> 3) & 3) * 128 + zb * 64 + ((c6 >> 2) & 1) * 32 + (c6 >> 5) * 16;
        v4u hv;
        NOUNROLL for (int n = 0; n < SPIN_GLB; ++n) {
          hv = LD128(ho, 0);
          CBAR();
          if (none_sent(mx4(hv, 0))) break;
          __builtin_amdgcn_s_sleep(2);
        }
        float h[4];
#pragma unroll
        for (int j = 0; j < 4; ++j) { float a = asf(hv[j]); a += dpp<ROR4>(a); a += dpp<ROR8>(a); h[j] = x16_sum(a); }
        h[0] = rbf(blo(xv[0]) + rbf(h[0])); h[1] = rbf(bhi(xv[0]) + rbf(h[1]));
        h[2] = rbf(blo(xv[1]) + rbf(h[2])); h[3] = rbf(bhi(xv[1]) + rbf(h[3]));
        PIN4(h);
        v4u p[9];
        NOUNROLL for (int n = 0; n < SPIN_GLB; ++n) {
#pragma unroll
          for (int k = 0; k < 9; ++k) p[k] = LD128(WS_MP + z * 9216 + lane * 16, k * 1024);
          CBAR();
          u32 m = 0;
#pragma unroll
          for (int k = 0; k < 9; ++k) m = mx4(p[k], m);
          if (none_sent(m)) break;
          __builtin_amdgcn_s_sleep(1);
        }
        STAMP(1);
        float r[4];
#pragma unroll
        for (int j = 0; j < 4; ++j) {
          float a = ((asf(p[0][j]) + asf(p[1][j])) + (asf(p[2][j]) + asf(p[3][j]))) + ((asf(p[4][j]) + asf(p[5][j])) + (asf(p[6][j]) + asf(p[7][j]))) + asf(p[8][j]);
          a += dpp<ROR4>(a); a += dpp<ROR8>(a);
          r[j] = h[j] + rbf(x32_sum(x16_sum(a)));
        }
        if (lane < 4) *(GAS v2u*)(out + zb * D + col) = v2u{cvtpk(r[0], r[1]), cvtpk(r[2], r[3])};
        if (z == 0 && lane == 0) *(GAS u64*)(ws + WS_EPOCH) = tag;
        STAMP(2);
      }
      STAMP_FLUSH();
      return;
    }
  }
  if (isY) {
    if (yt >= 0) {
      lds_wait(FL + F_VISS, yt + 1);
      if (ii & 1) yt = 8 - yt;
      const GAS char* wp = (const GAS char*)((lane & 4) ? w3 : w1) + (size_t)((yt * 64 + (r16 >> 3) * 32 + g4 * 8) * (FF * 2) + (yi * 32 + (lane & 3) * 8) * 2);
#pragma unroll
      for (int i = 0; i < 8; ++i) t[i] = __builtin_nontemporal_load((const GAS v4u*)(wp + i * (FF * 2)));
      const v2u xv = __builtin_amdgcn_raw_buffer_load_b64(rx, (((lane >> 2) & 1) * D + yt * 64 + (lane & 1) * 32 + g4 * 8 + ((lane >> 1) & 1) * 4) * 2, 0, 0);
      SCHED();
      STAMP(4);
      waitvm<0>();
      lds_arrive(FL + F_VISS, lane);
      lds_wait(FL + F_INOK, 1);
      v4u f[8];
      tr8(t, f);
      PIN8(f);
      STAMP(5);
      const int ao = WS_AP + (yt * 8 + (r16 >> 3)) * 512 + g4 * 128 + (lane & 7) * 16;
      const int kx = (ii & 3) * 1024;
      v4u p[4];
      NOUNROLL for (int n = 0; n < SPIN_GLB; ++n) {
#pragma unroll
        for (int k = 0; k < 4; ++k) p[k] = LD128(ao, (k * 1024) ^ kx);
        CBAR();
        if (none_sent(mx4(p[3], mx4(p[2], mx4(p[1], mx4(p[0], 0)))))) break;
        NOUNROLL for (; n < SPIN_GLB && !none_sent(mx4(p[0], 0)); ++n) {
          __builtin_amdgcn_s_sleep(3);
          p[0] = LD128(ao, kx);
          CBAR();
        }
      }
      STAMP(6);
      float h[4];
#pragma unroll
      for (int j = 0; j < 4; ++j) { float a = (asf(p[0][j]) + asf(p[1][j])) + (asf(p[2][j]) + asf(p[3][j])); h[j] = a + dpp<ROR8>(a); }
      const u32 c0 = cvtpk(h[0], h[1]), c1 = cvtpk(h[2], h[3]);
      const u32 h0 = cvtpk(blo(xv[0]) + blo(c0), bhi(xv[0]) + bhi(c0)), h1 = cvtpk(blo(xv[1]) + blo(c1), bhi(xv[1]) + bhi(c1));
      const u32 h2 = dppu<XOR2>(h0), h3 = dppu<XOR2>(h1);
      const bool one = lane & 8;
      const v4u a = {one ? 0x3F803F80u : h0, one ? 0x3F803F80u : h1, one ? 0x3F803F80u : h2, one ? 0x3F803F80u : h3};
      f4 acc[8];
#pragma unroll
      for (int j = 0; j < 8; ++j) acc[j] = mfma(a, f[j], f4{0, 0, 0, 0});
      const f4 gm = mfma(a, a, f4{0, 0, 0, 0});
      float o[8];
#pragma unroll
      for (int j = 0; j < 8; ++j) o[j] = acc[j][0] + dpp<ROR8>(acc[j][1]);
      if (r16 < 8) {
        LAS char* yp = L + L_R + yt * 1024 + ((lane & 3) * 16 + g4 * 4 + (r16 >> 2) * 2) * 16;
        *(LAS f4*)yp = f4{o[0], o[1], o[2], o[3]};
        *(LAS f4*)(yp + 16) = f4{o[4], o[5], o[6], o[7]};
      }
      const float sv = gm[0] + dpp<SHL1>(gm[1]);
      if (g4 < 2 && (r16 == 8 || r16 == 4 * g4)) *(LAS float*)(L + L_ST + ((r16 == 8 ? 0 : 2) + g4) * 64 + yt * 4) = sv;
      lds_arrive(FL + F_RCNT, lane);
      STAMP(7);
    } else if (v2 >= 0) {
      lds_wait(FL + F_VISS, v2 < 4 ? 10 + v2 : 10);
      const rsrc_t r2 = mkrs(w2, FF * D * 2);
      const int voff = (v2 < 4 || r16 < 8) ? (yi * 32 + g4 * 8) * (D * 2) + (v2 * 128 + r16 * 8) * 2 : OOB;
#pragma unroll
      for (int i = 0; i < 8; ++i) t[i] = __builtin_amdgcn_raw_buffer_load_b128(r2, voff, i * (D * 2), AUX_NT);
      SCHED();
      v4u f[8];
      tr8(t, f);
      PIN8(f);
      STAMP(5);
      lds_wait(FL + F_RCNT, 9);
      STAMP(6);
      float st = r16 < 9 ? *(LAS float*)(L + L_ST + lane * 4) : 0.f;
      f4 s = *(LAS f4*)(L + L_R + lane * 16);
#pragma unroll
      for (int k = 1; k < 9; ++k) s += *(LAS f4*)(L + L_R + k * 1024 + lane * 16);
      st += dpp<XOR1>(st); st += dpp<XOR2>(st); st += dpp<HMIR>(st); st += dpp<ROR8>(st);
      const float a0 = asf(__builtin_amdgcn_readlane(asu(st), 0)), a1 = asf(__builtin_amdgcn_readlane(asu(st), 16));
      const float q0 = asf(__builtin_amdgcn_readlane(asu(st), 32)), q1 = asf(__builtin_amdgcn_readlane(asu(st), 48));
      const float mean = ((lane & 4) ? a1 : a0) * (1.f / D);
      const float var = ((lane & 4) ? q1 : q0) * (1.f / D) - mean * mean;
      const float rs = __builtin_amdgcn_rsqf(fmaxf(var, 0.f) + 1e-5f);
      float hid[4];
#pragma unroll
      for (int j = 0; j < 4; ++j) {
        const float zv = rs * (s[j] - mean * dpp<ROR8>(s[j]));
        const float up = dpp<XOR2>(zv);
        hid[j] = zv * __builtin_amdgcn_rcpf(1.f + __builtin_amdgcn_exp2f(-1.4426950408889634f * zv)) * up;
      }
      const u32 h0 = cvtpk(hid[0], hid[1]), h1 = cvtpk(hid[2], hid[3]);
      const v4u a = {h0, h1, dppu<XOR1>(h0), dppu<XOR1>(h1)};
      f4 acc[8];
#pragma unroll
      for (int j = 0; j < 8; ++j) acc[j] = mfma(a, f[j], f4{0, 0, 0, 0});
      const int mo = (g4 < 2 && (v2 < 4 || r16 < 8)) ? WS_MP + (g4 * 36 + v2 * 8 + (r16 >> 2) * 2) * 9216 + yi * 64 + (lane & 3) * 16 : OOB;
      __builtin_amdgcn_raw_buffer_store_b128(nosent4(f4{acc[0][0], acc[1][0], acc[2][0], acc[3][0]}), rws, mo, 0, AUX_SC1);
      __builtin_amdgcn_raw_buffer_store_b128(nosent4(f4{acc[4][0], acc[5][0], acc[6][0], acc[7][0]}), rws, mo, 9216, AUX_SC1);
      STAMP(7);
    }
  }
  STAMP_FLUSH();
}

extern "C" void kernel_launch(void* const* d_in, const int* in_sizes, int n_in, void* d_out, int out_size, void* d_ws, size_t ws_size,
                              hipStream_t stream) {
  fused_decode<<<256, 1024, 0, stream>>>((const u16*)d_in[0], (const u16*)d_in[1], (const u16*)d_in[2], (const u16*)d_in[3],
                                         (const u16*)d_in[4], (const u16*)d_in[6], (const u16*)d_in[7], (const u16*)d_in[8], (u16*)d_out,
                                         (char*)d_ws);
}
```

```cpp
#define AMPSEL 1
#include <hip/hip_runtime.h>
#include <hip/hip_bf16.h>
#include <stdint.h>

#define GAS __attribute__((address_space(1)))
#define LAS __attribute__((address_space(3)))
#define CAS __attribute__((address_space(4)))
typedef unsigned u32;
typedef unsigned long long u64;
typedef unsigned short u16;
typedef u32 v4u __attribute__((ext_vector_type(4)));
typedef u32 v2u __attribute__((ext_vector_type(2)));
typedef float f4 __attribute__((ext_vector_type(4)));
typedef float f2 __attribute__((ext_vector_type(2)));
typedef __bf16 bf8 __attribute__((ext_vector_type(8)));
typedef __bf16 bf2 __attribute__((ext_vector_type(2)));
typedef __amdgpu_buffer_rsrc_t rsrc_t;

#define QSLEEP 12
#define KWN 4
#define VPRE 4
#define VWN 5
#define VCRED 2
constexpr int D = 576, SK = 8192, WIN = 2048, FF = 4608, QD = 4096;
constexpr int WS_EPOCH = 0, WS_FLAGS = 4096, WS_ML = 8192, WS_OP = 12288, WS_AP = WS_OP + 131072, WS_MP = WS_AP + 36864,
              WS_QG = WS_MP + 663552, WS_END = WS_QG + 262144;
constexpr int WS_STAMP = 2 << 20;
constexpr u32 SENT = 0xFFFFFFFFu;
constexpr int AUX_NT = 2, AUX_SC1 = 16, OOB = 1 << 30;
constexpr int SPIN_LDS = 1 << 18, SPIN_GLB = 1 << 15;
constexpr int L_FLAG = 0;
constexpr int L_Q = 128;
constexpr int L_P = 640;
constexpr int L_MX = 1664;
constexpr int L_ST = 1792;
constexpr int L_QP = 2048;
constexpr int L_XN = 3584;
constexpr int L_O = 6144;
constexpr int L_R = 10240;
constexpr int L_STAMP = L_R + 9216;
constexpr int L_END = L_STAMP + 1024;
enum { F_QCNT = 0, F_QRDY, F_MXCNT, F_PCNT, F_OCNT, F_VISS, F_CLR, F_INOK, F_RCNT, F_XN };

__device__ __forceinline__ rsrc_t mkrs(const void* p, int bytes) { return __builtin_amdgcn_make_buffer_rsrc((void*)p, 0, bytes, 0x00020000); }
__device__ __forceinline__ float asf(u32 u) { return __uint_as_float(u); }
__device__ __forceinline__ u32 asu(float f) { return __float_as_uint(f); }
__device__ __forceinline__ u32 cvtpk(float lo, float hi) { return __builtin_bit_cast(u32, __builtin_convertvector(f2{lo, hi}, bf2)); }
__device__ __forceinline__ float rbf(float f) { return asf(cvtpk(f, f) & 0xffff0000u); }
__device__ __forceinline__ float blo(u32 u) { return asf(u << 16); }
__device__ __forceinline__ float bhi(u32 u) { return asf(u & 0xffff0000u); }
template <int C> __device__ __forceinline__ u32 dppu(u32 x) { return (u32)__builtin_amdgcn_mov_dpp((int)x, C, 0xf, 0xf, true); }
template <int C> __device__ __forceinline__ float dpp(float x) { return asf(dppu<C>(asu(x))); }
constexpr int XOR1 = 0xB1, XOR2 = 0x4E, HMIR = 0x141, ROR4 = 0x124, ROR8 = 0x128, SHL1 = 0x101;
__device__ __forceinline__ float x16_sum(float x) { auto s = __builtin_amdgcn_permlane16_swap(asu(x), asu(x), false, false); return asf(s[0]) + asf(s[1]); }
__device__ __forceinline__ float x32_sum(float x) { auto s = __builtin_amdgcn_permlane32_swap(asu(x), asu(x), false, false); return asf(s[0]) + asf(s[1]); }
__device__ __forceinline__ float x16_max(float x) { auto s = __builtin_amdgcn_permlane16_swap(asu(x), asu(x), false, false); return fmaxf(asf(s[0]), asf(s[1])); }
__device__ __forceinline__ float x32_max(float x) { auto s = __builtin_amdgcn_permlane32_swap(asu(x), asu(x), false, false); return fmaxf(asf(s[0]), asf(s[1])); }
__device__ __forceinline__ f4 mfma(v4u a, v4u b, f4 c) { return __builtin_amdgcn_mfma_f32_16x16x32_bf16(__builtin_bit_cast(bf8, a), __builtin_bit_cast(bf8, b), c, 0, 0, 0); }
__device__ __forceinline__ u32 plo(u32 a, u32 b) { return __builtin_amdgcn_perm(b, a, 0x05040100u); }
__device__ __forceinline__ u32 phi(u32 a, u32 b) { return __builtin_amdgcn_perm(b, a, 0x07060302u); }
__device__ __forceinline__ void tr8(const v4u* r, v4u* f) {
#pragma unroll
  for (int e = 0; e < 4; ++e) {
    f[2 * e] = v4u{plo(r[0][e], r[1][e]), plo(r[2][e], r[3][e]), plo(r[4][e], r[5][e]), plo(r[6][e], r[7][e])};
    f[2 * e + 1] = v4u{phi(r[0][e], r[1][e]), phi(r[2][e], r[3][e]), phi(r[4][e], r[5][e]), phi(r[6][e], r[7][e])};
  }
}
__device__ __forceinline__ void tr4(const v2u* r, v4u* f) {
#pragma unroll
  for (int e = 0; e < 2; ++e) {
    f[2 * e] = v4u{plo(r[0][e], r[1][e]), plo(r[2][e], r[3][e]), plo(r[4][e], r[5][e]), plo(r[6][e], r[7][e])};
    f[2 * e + 1] = v4u{phi(r[0][e], r[1][e]), phi(r[2][e], r[3][e]), phi(r[4][e], r[5][e]), phi(r[6][e], r[7][e])};
  }
}
__device__ __forceinline__ u32 umax(u32 a, u32 b) { return a > b ? a : b; }
__device__ __forceinline__ u32 nosent(u32 x) { return x < 0xFFFFFFFEu ? x : 0xFFFFFFFEu; }
__device__ __forceinline__ v4u nosent4(f4 v) { return v4u{nosent(asu(v[0])), nosent(asu(v[1])), nosent(asu(v[2])), nosent(asu(v[3]))}; }
__device__ __forceinline__ u32 mx4(v4u v, u32 m) { return umax(umax(umax(v[0], v[1]), m), umax(v[2], v[3])); }
__device__ __forceinline__ bool none_sent(u32 m) { return __builtin_amdgcn_ballot_w64(m == SENT) == 0; }
#define PIN4(f) asm volatile("" : "+v"(f[0]), "+v"(f[1]), "+v"(f[2]), "+v"(f[3]))
#define PIN8(f) asm volatile("" : "+v"(f[0]), "+v"(f[1]), "+v"(f[2]), "+v"(f[3]), "+v"(f[4]), "+v"(f[5]), "+v"(f[6]), "+v"(f[7]))
#define CBAR() asm volatile("" ::: "memory")
#define SCHED() __builtin_amdgcn_sched_barrier(0)
template <int N> __device__ __forceinline__ void waitvm() { asm volatile("s_waitcnt vmcnt(%0)" ::"i"(N) : "memory"); }
#define NOUNROLL _Pragma("nounroll")
#define LD128(off, soff) __builtin_amdgcn_raw_buffer_load_b128(rws, off, soff, AUX_SC1)

__device__ __forceinline__ u32 lds_ld(LAS u32* p) { return __hip_atomic_load(p, __ATOMIC_RELAXED, __HIP_MEMORY_SCOPE_WORKGROUP); }
__device__ __forceinline__ void lds_wait(LAS u32* p, u32 want) {
  CBAR();
  NOUNROLL for (int n = 0; n < SPIN_LDS; ++n) {
    if ((u32)__builtin_amdgcn_readfirstlane((int)lds_ld(p)) >= want) break;
    __builtin_amdgcn_s_sleep(1);
  }
  CBAR();
}
__device__ __forceinline__ u32 lds_arrive(LAS u32* p, int lane) {
  CBAR();
  u32 old = 0;
  if (lane == 0) old = __hip_atomic_fetch_add(p, 1u, __ATOMIC_RELAXED, __HIP_MEMORY_SCOPE_WORKGROUP);
  old = (u32)__builtin_amdgcn_readfirstlane((int)old);
  CBAR();
  return old;
}

#define STAMP(k) do {} while (0)
#define STAMP_INIT() do {} while (0)
#define STAMP_FLUSH() do {} while (0)

__global__ void __launch_bounds__(1024) fused_decode(const u16* __restrict__ x, const u16* __restrict__ kc, const u16* __restrict__ vc,
                                                     const u16* __restrict__ wq, const u16* __restrict__ wo, const u16* __restrict__ w1,
                                                     const u16* __restrict__ w3, const u16* __restrict__ w2, u16* __restrict__ out,
                                                     char* ws) {
  __shared__ __attribute__((aligned(16))) char smem[L_END];
  LAS char* L = (LAS char*)smem;
  LAS u32* FL = (LAS u32*)(L + L_FLAG);
  const int bid = blockIdx.x, lane = threadIdx.x & 63, w = __builtin_amdgcn_readfirstlane(threadIdx.x >> 6);
  const int xcd = bid & 7, ii = bid >> 3;
  const int kvh = 2 * xcd + (ii >> 4), jj = ii & 15, bb = jj >> 3;
  const bool isY = ii < 18, isX = ii >= 18 && ii < 27;
  const int yi = xcd * 18 + ii, xc = ii - 18;
  const int g4 = lane >> 4, r16 = lane & 15, hb = lane >> 5, l5 = lane & 31;
  u64 amp_t0; asm volatile("s_memrealtime %0\n\ts_waitcnt lgkmcnt(0)" : "=s"(amp_t0) :: "memory");
  STAMP_INIT();
  STAMP(0);
  if (threadIdx.x < 32) FL[lane] = 0;
  asm volatile("s_waitcnt lgkmcnt(0)\n\ts_barrier" ::: "memory");
  asm volatile("" ::"s"(x), "s"(kc), "s"(vc), "s"(wq), "s"(wo), "s"(w1), "s"(w3), "s"(w2), "s"(out), "s"(ws));
  const rsrc_t rws = mkrs(ws, WS_END);
  const rsrc_t rx = mkrs(x, 2 * D * 2);

  int yt = -1;
  int v2 = -1;
  const int kw = w & 3;
  v4u t[16];
  if (w < 4) {
    const char* bp; int bytes, voff, step;
    if (w < 3) {
      bp = (const char*)wq; bytes = D * QD * 2; step = QD * 2;
      const int k0 = w * 64 + (r16 >> 3) * 32 + g4 * 8;
      voff = k0 < 144 ? ((jj & 3) * 144 + k0) * (QD * 2) + (kvh * 256 + (jj >> 2) * 64 + (lane & 7) * 8) * 2 : OOB;
    } else {
      bp = (const char*)x; bytes = 2 * D * 2; step = 384;
      voff = l5 < 24 ? hb * (D * 2) + l5 * 16 : OOB;
    }
    const rsrc_t rk = mkrs(bp, bytes);
#pragma unroll
    for (int i = 0; i < 3; ++i) t[i] = __builtin_amdgcn_raw_buffer_load_b128(rk, voff, i * step, AUX_NT);
    if (w != 3) {
#pragma unroll
      for (int i = 3; i < 8; ++i) t[i] = __builtin_amdgcn_raw_buffer_load_b128(rk, voff, i * step, AUX_NT);
    }
    SCHED();
  } else if (w < 12) {
    const char* bp = (const char*)(w < 8 ? kc : vc) + ((size_t)(bb * 16 + kvh) * SK + (SK - WIN) + (jj & 7) * 256 + kw * 64) * 256;
    const int rot = bid * 5;
    const int voff = w < 8 ? ((r16 >> 2) * 256 + (g4 * 4 + (lane & 3)) * 16) : lane * 16;
    const rsrc_t rk = mkrs(bp, 64 * 256);
    if (w < 8) {
#pragma unroll
      for (int i = 0; i < 16; ++i) {
        if (i >= KWN) waitvm<KWN - 1>();
        t[i] = __builtin_amdgcn_raw_buffer_load_b128(rk, voff, ((i + rot) & 15) * 1024, AUX_NT);
        SCHED();
      }
    } else {
#pragma unroll
      for (int i = 0; i < 16; ++i) {
        if (i >= 1 && i <= VPRE) waitvm<0>();
        else if (i >= VPRE + VWN) waitvm<VWN - 1>();
        t[i] = __builtin_amdgcn_raw_buffer_load_b128(rk, voff, ((i + rot) & 15) * 1024, AUX_NT);
        SCHED();
      }
    }
  } else if (w == 12) {
    u64 pc; asm volatile("s_getpc_b64 %0" : "=s"(pc));
    const GAS v4u* cp = (const GAS v4u*)((pc & ~(u64)1023) + (u64)(ii & 15) * 1024) + lane;
    v4u c = *cp;
    asm volatile("" ::"v"(c));
  } else if (w == 13) {
    const u16* mp = g4 == 0 ? wo : g4 == 1 ? w1 : g4 == 2 ? w3 : w2;
    const int pg = ii + 32 * r16;
    if (pg < (g4 == 0 ? 72 : 81)) {
      u32 c = __builtin_nontemporal_load((const GAS u32*)((const GAS char*)mp + (size_t)pg * 65536));
      asm volatile("" ::"v"(c));
    }
  }
  if (w == 14 && lane == 0) __hip_atomic_store((GAS u64*)(ws + (4 << 20) + bid * 128), amp_t0, __ATOMIC_RELAXED, __HIP_MEMORY_SCOPE_AGENT);
  const u64 tag = *(const CAS u64*)(ws + WS_EPOCH) + 1;

  if (w >= 4 && w < 8) {
    STAMP(1);
    lds_wait(FL + F_QRDY, 1);
    STAMP(2);
    v4u qf = *(LAS v4u*)(L + L_Q + ((r16 >> 2) & 1) * 256 + (g4 * 4 + (lane & 3)) * 16);
    const v4u z4 = {0, 0, 0, 0};
    const v4u q0 = r16 < 8 ? qf : z4, q1 = r16 < 8 ? z4 : qf;
    float sc[8];
#pragma unroll
    for (int m = 0; m < 8; ++m) {
      f4 d = mfma(t[m], q0, f4{0, 0, 0, 0});
      d = mfma(t[m + 8], q1, d);
      sc[m] = (dpp<0x00>(d[0]) + dpp<0x55>(d[1])) + (dpp<0xAA>(d[2]) + dpp<0xFF>(d[3]));
    }
    const bool b0 = lane & 1, b1 = lane & 2;
    float s0 = b1 ? (b0 ? sc[6] : sc[4]) : (b0 ? sc[2] : sc[0]);
    float s1 = b1 ? (b0 ? sc[7] : sc[5]) : (b0 ? sc[3] : sc[1]);
    u32 pk = cvtpk(s0, s1);
    pk = cvtpk(blo(pk) * 0.08838834764831845f, bhi(pk) * 0.08838834764831845f);
    s0 = blo(pk) * 1.4426950408889634f; s1 = bhi(pk) * 1.4426950408889634f;
    float mx = fmaxf(s0, s1);
    mx = fmaxf(mx, dpp<XOR1>(mx)); mx = fmaxf(mx, dpp<XOR2>(mx)); mx = fmaxf(mx, dpp<ROR8>(mx));
    mx = x32_max(x16_max(mx));
    const int hd = (r16 >> 2) & 1;
    LAS float* MX = (LAS float*)(L + L_MX);
    if ((lane & 0x3B) == 0) MX[hd * 4 + kw] = mx;
    lds_arrive(FL + F_MXCNT, lane);
    lds_wait(FL + F_MXCNT, 4);
    f4 m4 = *(LAS f4*)(MX + hd * 4);
    mx = fmaxf(fmaxf(m4[0], m4[1]), fmaxf(m4[2], m4[3]));
    const float p0 = __builtin_amdgcn_exp2f(s0 - mx), p1 = __builtin_amdgcn_exp2f(s1 - mx);
    *(LAS u32*)(L + L_P + kw * 256 + hd * 128 + (r16 >> 3) * 64 + g4 * 16 + (lane & 3) * 4) = cvtpk(p0, p1);
    float ls = p0 + p1;
    ls += dpp<XOR1>(ls); ls += dpp<XOR2>(ls); ls += dpp<ROR8>(ls);
    ls = x32_sum(x16_sum(ls));
    if ((lane & 0x3B) == 0) MX[8 + hd * 4 + kw] = ls;
    if (lds_arrive(FL + F_PCNT, lane) == 3) {
      f4 l4 = *(LAS f4*)(MX + 8 + hd * 4);
      lds_wait(FL + F_CLR, 1);
      if ((lane & 0x3B) == 0)
        __builtin_amdgcn_raw_buffer_store_b64(v2u{nosent(asu(mx)), nosent(asu((l4[0] + l4[1]) + (l4[2] + l4[3])))}, rws,
                                              WS_ML + ((2 * kvh + hd) * 16 + jj) * 8, 0, AUX_SC1);
    }
    STAMP(3);
    if (kw == 3 && (isX || isY)) {
      const u32 tl = (u32)tag, th = (u32)(tag >> 32);
      NOUNROLL for (int n = 0; n < SPIN_GLB; ++n) {
        v4u g0 = LD128(WS_FLAGS + lane * 16, 0);
        v4u g1 = LD128(WS_FLAGS + lane * 16, 1024);
        CBAR();
        const u32 bad = ((g0[0] ^ tl) | (g0[1] ^ th) | (g0[2] ^ tl) | (g0[3] ^ th)) | ((g1[0] ^ tl) | (g1[1] ^ th) | (g1[2] ^ tl) | (g1[3] ^ th));
        if (__builtin_amdgcn_ballot_w64(bad != 0) == 0) break;
        __builtin_amdgcn_s_sleep(8);
      }
      lds_arrive(FL + F_INOK, lane);
      STAMP(4);
    }
    if (isY) { if (kw == 0) yt = 8; if (kw == 1) v2 = 4; }
  } else if (w >= 8 && w < 12) {
    STAMP(1);
    waitvm<VCRED>();
    lds_arrive(FL + F_VISS, lane);
    lds_wait(FL + F_PCNT, 4);
    STAMP(2);
    const v4u pa = *(LAS v4u*)(L + L_P + kw * 256 + ((r16 >> 2) & 1) * 128 + g4 * 16);
    const v4u pb = *(LAS v4u*)(L + L_P + kw * 256 + ((r16 >> 2) & 1) * 128 + 64 + g4 * 16);
    f4 acc[8];
    v4u f[8];
    tr8(t, f);
#pragma unroll
    for (int j = 0; j < 8; ++j) acc[j] = mfma(pa, f[j], f4{0, 0, 0, 0});
    SCHED();
    tr8(t + 8, f);
#pragma unroll
    for (int j = 0; j < 8; ++j) acc[j] = mfma(pb, f[j], acc[j]);
    STAMP(3);
    {
      const bool hi = g4 >= 2;
      f4 o = {hi ? acc[4][0] : acc[0][0], hi ? acc[5][0] : acc[1][0], hi ? acc[6][0] : acc[2][0], hi ? acc[7][0] : acc[3][0]};
      *(LAS f4*)(L + L_O + kw * 1024 + (g4 & 1) * 512 + r16 * 32 + (g4 >> 1) * 16) = o;
    }
    if (lds_arrive(FL + F_OCNT, lane) == 3) {
      f4 o = (*(LAS f4*)(L + L_O + lane * 16) + *(LAS f4*)(L + L_O + 1024 + lane * 16)) +
             (*(LAS f4*)(L + L_O + 2048 + lane * 16) + *(LAS f4*)(L + L_O + 3072 + lane * 16));
      lds_wait(FL + F_CLR, 1);
      __builtin_amdgcn_s_setprio(3);
      __builtin_amdgcn_raw_buffer_store_b64(v2u{nosent(cvtpk(o[0], o[1])), nosent(cvtpk(o[2], o[3]))}, rws,
                                            WS_OP + (2 * kvh * 8 + (lane >> 2)) * 512 + jj * 32 + (lane & 3) * 8, 0, AUX_SC1);
      __builtin_amdgcn_s_setprio(0);
    }
    STAMP(4);
    if (isY) v2 = kw;
  } else {
    if (w < 3) {
      v4u f[8];
      tr8(t, f);
      lds_wait(FL + F_XN, 1);
      const v4u a = *(LAS v4u*)(L + L_XN + ((r16 >> 2) & 1) * (D * 2) + ((jj & 3) * 144 + w * 64 + (lane & 1) * 32 + g4 * 8) * 2);
      f4 acc[8];
#pragma unroll
      for (int j = 0; j < 8; ++j) acc[j] = mfma(a, f[j], f4{0, 0, 0, 0});
      float o[8];
#pragma unroll
      for (int j = 0; j < 8; ++j) o[j] = acc[j][0] + dpp<ROR8>(acc[j][1]);
      STAMP(1);
      if (lane < 32 && r16 < 8) {
        LAS char* qp = L + L_QP + w * 512 + g4 * 256 + r16 * 32;
        *(LAS f4*)qp = f4{o[0], o[1], o[2], o[3]};
        *(LAS f4*)(qp + 16) = f4{o[4], o[5], o[6], o[7]};
      }
      if (lds_arrive(FL + F_QCNT, lane) == 2) {
        const f2 q = (*(LAS f2*)(L + L_QP + lane * 8) + *(LAS f2*)(L + L_QP + 512 + lane * 8)) + *(LAS f2*)(L + L_QP + 1024 + lane * 8);
        const u32 th = (u32)(tag >> 8), tl = (u32)tag & 0xffu;
        __builtin_amdgcn_raw_buffer_store_b128(v4u{((asu(q[0]) + 0x80u) & ~0xffu) | tl, th, ((asu(q[1]) + 0x80u) & ~0xffu) | tl, th}, rws,
                                               WS_QG + ((hb * 16 + kvh) * 4 + (jj & 3)) * 2048 + (jj >> 2) * 512 + l5 * 16, 0, AUX_SC1);
        STAMP(2);
        const int go = WS_QG + (bb * 16 + kvh) * 8192 + lane * 16;
        v4u g[8];
        __builtin_amdgcn_s_sleep(QSLEEP);
        NOUNROLL for (int n = 0; n < SPIN_GLB; ++n) {
#pragma unroll
          for (int k = 0; k < 8; ++k) g[k] = LD128(go, k * 1024);
          CBAR();
          u32 bad = 0, lo = 0;
#pragma unroll
          for (int k = 0; k < 8; ++k) { bad |= (g[k][1] ^ th) | (g[k][3] ^ th); lo |= (g[k][0] ^ tl) | (g[k][2] ^ tl); }
          bad |= lo & 0xffu;
          if (__builtin_amdgcn_ballot_w64(bad != 0) == 0) break;
          __builtin_amdgcn_s_sleep(4);
        }
#pragma unroll
        for (int c = 0; c < 2; ++c) {
          const float a0 = (asf(g[c][0] & ~0xffu) + asf(g[2 + c][0] & ~0xffu)) + (asf(g[4 + c][0] & ~0xffu) + asf(g[6 + c][0] & ~0xffu));
          const float a1 = (asf(g[c][2] & ~0xffu) + asf(g[2 + c][2] & ~0xffu)) + (asf(g[4 + c][2] & ~0xffu) + asf(g[6 + c][2] & ~0xffu));
          *(LAS u32*)(L + L_Q + c * 256 + lane * 4) = cvtpk(a0, a1);
        }
        lds_arrive(FL + F_QRDY, lane);
        STAMP(3);
      }
    } else if (w == 3) {
      float s = 0;
#pragma unroll
      for (int e = 0; e < 4; ++e) s += (blo(t[0][e]) + bhi(t[0][e])) + (blo(t[1][e]) + bhi(t[1][e])) + (blo(t[2][e]) + bhi(t[2][e]));
      s += dpp<XOR1>(s); s += dpp<XOR2>(s); s += dpp<HMIR>(s); s += dpp<ROR8>(s); s = x16_sum(s);
      const float mean = s * (1.f / D);
      float v = 0;
#pragma unroll
      for (int i = 0; i < 3; ++i)
#pragma unroll
        for (int e = 0; e < 4; ++e) { const float a = blo(t[i][e]) - mean, b = bhi(t[i][e]) - mean; v += a * a + b * b; }
      v = l5 < 24 ? v : 0.f;
      v += dpp<XOR1>(v); v += dpp<XOR2>(v); v += dpp<HMIR>(v); v += dpp<ROR8>(v); v = x16_sum(v);
      const float mm = rbf(mean);
      const float rr = __builtin_amdgcn_rcpf(rbf(__builtin_amdgcn_sqrtf(rbf(rbf(v * (1.f / D)) + 1e-5f))));
      if (l5 < 24) {
#pragma unroll
        for (int i = 0; i < 3; ++i) {
          v4u a;
#pragma unroll
          for (int e = 0; e < 4; ++e) { u32 c = cvtpk(blo(t[i][e]) - mm, bhi(t[i][e]) - mm); a[e] = cvtpk(blo(c) * rr, bhi(c) * rr); }
          *(LAS v4u*)(L + L_XN + hb * (D * 2) + i * 384 + l5 * 16) = a;
        }
      } else if (hb) *(LAS v4u*)(L + L_XN + 2 * D * 2 + (l5 - 24) * 16) = v4u{0, 0, 0, 0};
      lds_arrive(FL + F_XN, lane);
      STAMP(1);
      const v4u s4 = {SENT, SENT, SENT, SENT};
      __builtin_amdgcn_raw_buffer_store_b64(v2u{SENT, SENT}, rws, WS_OP + (2 * kvh * 8 + (lane >> 2)) * 512 + jj * 32 + (lane & 3) * 8, 0, AUX_SC1);
      __builtin_amdgcn_raw_buffer_store_b64(v2u{SENT, SENT}, rws, lane < 2 ? WS_ML + ((2 * kvh + lane) * 16 + jj) * 8 : OOB, 0, AUX_SC1);
      if (isX) __builtin_amdgcn_raw_buffer_store_b128(s4, rws, lane < 32 ? WS_AP + (xc * 8 + xcd) * 512 + lane * 16 : OOB, 0, AUX_SC1);
      if (isY) {
        const int mo = WS_MP + (lane >> 2) * 9216 + yi * 64 + (lane & 3) * 16;
#pragma unroll
        for (int n = 0; n < 4; ++n) __builtin_amdgcn_raw_buffer_store_b128(s4, rws, mo, n * 16 * 9216, AUX_SC1);
        __builtin_amdgcn_raw_buffer_store_b128(s4, rws, lane < 32 ? mo : OOB, 4 * 16 * 9216, AUX_SC1);
      }
      SCHED();
      asm volatile("s_waitcnt vmcnt(0)" ::: "memory");
      if (lane == 0) __builtin_amdgcn_raw_buffer_store_b64(v2u{(u32)tag, (u32)(tag >> 32)}, rws, WS_FLAGS + bid * 8, 0, AUX_SC1);
      lds_arrive(FL + F_CLR, lane);
      STAMP(2);
    }
    yt = w < 4 ? w + 4 : w - 12;
    if (isX) {
      const int xw = w < 4 ? w : w - 8, hh = 4 * xcd + (xw >> 1), hf = xw & 1;
      lds_wait(FL + F_VISS, ((xw + 4) & 7) + 1);
      const rsrc_t ro = mkrs(wo, QD * D * 2);
      const int voff = (hh * 128 + hf * 64 + g4 * 16) * (D * 2) + (xc * 64 + r16 * 4) * 2;
      v2u u[16];
#pragma unroll
      for (int i = 0; i < 16; ++i) u[i] = __builtin_amdgcn_raw_buffer_load_b64(ro, voff, i * (D * 2), AUX_NT);
      SCHED();
      STAMP(4);
      waitvm<0>();
      lds_arrive(FL + F_VISS, lane);
      lds_wait(FL + F_INOK, 1);
      v2u ml;
      NOUNROLL for (int n = 0; n < SPIN_GLB; ++n) {
        ml = __builtin_amdgcn_raw_buffer_load_b64(rws, WS_ML + (hh * 16 + r16) * 8, 0, AUX_SC1);
        CBAR();
        if (none_sent(umax(ml[0], ml[1]))) break;
        __builtin_amdgcn_s_sleep(4);
      }
      float M = asf(ml[0]);
      M = fmaxf(M, dpp<XOR1>(M)); M = fmaxf(M, dpp<XOR2>(M)); M = fmaxf(M, dpp<HMIR>(M));
      const float e = __builtin_amdgcn_exp2f(asf(ml[0]) - M);
      float Ls = e * asf(ml[1]);
      Ls += dpp<XOR1>(Ls); Ls += dpp<XOR2>(Ls); Ls += dpp<HMIR>(Ls);
      const float wr = e * __builtin_amdgcn_rcpf(Ls);
      float wg[4];
#pragma unroll
      for (int i = 0; i < 4; ++i) wg[i] = __shfl(wr, g4 * 4 + i);
      v4u f0[4], f1[4];
      tr4(u, f0); tr4(u + 8, f1);
      PIN4(f0); PIN4(f1); PIN4(wg);
      STAMP(5);
      lds_wait(FL + F_OCNT, 4);
      v4u a0, a1;
      const int ao = WS_OP + (hh * 8 + hf * 4 + g4) * 512 + r16 * 32;
      NOUNROLL for (int n = 0; n < SPIN_GLB; ++n) {
        a0 = LD128(ao, 0);
        a1 = LD128(ao + 16, 0);
        CBAR();
        if (none_sent(mx4(a1, mx4(a0, 0)))) break;
        __builtin_amdgcn_s_sleep(1);
      }
      STAMP(6);
      f4 o;
#pragma unroll
      for (int j = 0; j < 4; ++j) {
        f4 d = mfma(a0, f0[j], f4{0, 0, 0, 0});
        d = mfma(a1, f1[j], d);
        o[j] = x16_sum((wg[0] * d[0] + wg[1] * d[1]) + (wg[2] * d[2] + wg[3] * d[3]));
      }
      if (!(g4 & 1)) *(LAS f4*)(L + L_R + xw * 512 + ((r16 >> 1) & 3) * 128 + (g4 >> 1) * 64 + (r16 & 1) * 32 + (r16 >> 3) * 16) = o;
      if (lds_arrive(FL + F_RCNT, lane) == 7) {
        LAS char* rp = L + L_R + hb * 2048 + l5 * 16;
        f4 s = (*(LAS f4*)(rp) + *(LAS f4*)(rp + 512)) + (*(LAS f4*)(rp + 1024) + *(LAS f4*)(rp + 1536));
#pragma unroll
        for (int j = 0; j < 4; ++j) s[j] = x32_sum(s[j]);
        __builtin_amdgcn_raw_buffer_store_b128(nosent4(s), rws, lane < 32 ? WS_AP + (xc * 8 + xcd) * 512 + lane * 16 : OOB, 0, AUX_SC1);
        STAMP(7);
        const int z = xcd * 9 + xc, zb = z / 36, zz = z % 36;
        const int col = (zz >> 3) * 128 + ((zz >> 1) & 3) * 32 + (lane & 3) * 8 + (zz & 1) * 4;
        const v2u xv = __builtin_amdgcn_raw_buffer_load_b64(rx, (zb * D + col) * 2, 0, 0);
        const int c6 = col & 63;
        const int ho = WS_AP + ((col >> 6) * 8 + ((lane >> 2) & 7)) * 512 + ((c6 >> 3) & 3) * 128 + zb * 64 + ((c6 >> 2) & 1) * 32 + (c6 >> 5) * 16;
        v4u hv;
        NOUNROLL for (int n = 0; n < SPIN_GLB; ++n) {
          hv = LD128(ho, 0);
          CBAR();
          if (none_sent(mx4(hv, 0))) break;
          __builtin_amdgcn_s_sleep(2);
        }
        float h[4];
#pragma unroll
        for (int j = 0; j < 4; ++j) { float a = asf(hv[j]); a += dpp<ROR4>(a); a += dpp<ROR8>(a); h[j] = x16_sum(a); }
        h[0] = rbf(blo(xv[0]) + rbf(h[0])); h[1] = rbf(bhi(xv[0]) + rbf(h[1]));
        h[2] = rbf(blo(xv[1]) + rbf(h[2])); h[3] = rbf(bhi(xv[1]) + rbf(h[3]));
        PIN4(h);
        v4u p[9];
        NOUNROLL for (int n = 0; n < SPIN_GLB; ++n) {
#pragma unroll
          for (int k = 0; k < 9; ++k) p[k] = LD128(WS_MP + z * 9216 + lane * 16, k * 1024);
          CBAR();
          u32 m = 0;
#pragma unroll
          for (int k = 0; k < 9; ++k) m = mx4(p[k], m);
          if (none_sent(m)) break;
          __builtin_amdgcn_s_sleep(1);
        }
        STAMP(1);
        float r[4];
#pragma unroll
        for (int j = 0; j < 4; ++j) {
          float a = ((asf(p[0][j]) + asf(p[1][j])) + (asf(p[2][j]) + asf(p[3][j]))) + ((asf(p[4][j]) + asf(p[5][j])) + (asf(p[6][j]) + asf(p[7][j]))) + asf(p[8][j]);
          a += dpp<ROR4>(a); a += dpp<ROR8>(a);
          r[j] = h[j] + rbf(x32_sum(x16_sum(a)));
        }
        if (lane < 4) *(GAS v2u*)(out + zb * D + col) = v2u{cvtpk(r[0], r[1]), cvtpk(r[2], r[3])};
        if (z == 0 && lane == 0) *(GAS u64*)(ws + WS_EPOCH) = tag;
        if (z == 0) { const u64 te = __builtin_amdgcn_s_memrealtime();
          u32 mn = 0, mxx = 0xffffffffu;
          for (int k = 0; k < 4; ++k) { u64 v = __hip_atomic_load((GAS u64*)(ws + (4 << 20) + (k * 64 + lane) * 128), __ATOMIC_RELAXED, __HIP_MEMORY_SCOPE_AGENT); u32 d = (u32)(te - v); mn = d > mn ? d : mn; mxx = d < mxx ? d : mxx; }
          for (int m = 32; m >= 1; m >>= 1) { u32 a = __shfl_xor(mn, m), b = __shfl_xor(mxx, m); mn = a > mn ? a : mn; mxx = b < mxx ? b : mxx; }
          u64 d = AMPSEL == 0 ? mn : (mn - mxx) * 4; d = (u32)__builtin_amdgcn_readfirstlane((int)d); if (d > 10000) d = 10000;
          for (int g = 0; g < 1000000 && __builtin_amdgcn_s_memrealtime() < te + 4 * d; ++g) __builtin_amdgcn_s_sleep(2); }
        STAMP(2);
      }
      STAMP_FLUSH();
      return;
    }
  }
  if (isY) {
    if (yt >= 0) {
      lds_wait(FL + F_VISS, yt + 1);
      if (ii & 1) yt = 8 - yt;
      const GAS char* wp = (const GAS char*)((lane & 4) ? w3 : w1) + (size_t)((yt * 64 + (r16 >> 3) * 32 + g4 * 8) * (FF * 2) + (yi * 32 + (lane & 3) * 8) * 2);
#pragma unroll
      for (int i = 0; i < 8; ++i) t[i] = __builtin_nontemporal_load((const GAS v4u*)(wp + i * (FF * 2)));
      const v2u xv = __builtin_amdgcn_raw_buffer_load_b64(rx, (((lane >> 2) & 1) * D + yt * 64 + (lane & 1) * 32 + g4 * 8 + ((lane >> 1) & 1) * 4) * 2, 0, 0);
      SCHED();
      STAMP(4);
      waitvm<0>();
      lds_arrive(FL + F_VISS, lane);
      lds_wait(FL + F_INOK, 1);
      v4u f[8];
      tr8(t, f);
      PIN8(f);
      STAMP(5);
      const int ao = WS_AP + (yt * 8 + (r16 >> 3)) * 512 + g4 * 128 + (lane & 7) * 16;
      const int kx = (ii & 3) * 1024;
      v4u p[4];
      NOUNROLL for (int n = 0; n < SPIN_GLB; ++n) {
#pragma unroll
        for (int k = 0; k < 4; ++k) p[k] = LD128(ao, (k * 1024) ^ kx);
        CBAR();
        if (none_sent(mx4(p[3], mx4(p[2], mx4(p[1], mx4(p[0], 0)))))) break;
        NOUNROLL for (; n < SPIN_GLB && !none_sent(mx4(p[0], 0)); ++n) {
          __builtin_amdgcn_s_sleep(3);
          p[0] = LD128(ao, kx);
          CBAR();
        }
      }
      STAMP(6);
      float h[4];
#pragma unroll
      for (int j = 0; j < 4; ++j) { float a = (asf(p[0][j]) + asf(p[1][j])) + (asf(p[2][j]) + asf(p[3][j])); h[j] = a + dpp<ROR8>(a); }
      const u32 c0 = cvtpk(h[0], h[1]), c1 = cvtpk(h[2], h[3]);
      const u32 h0 = cvtpk(blo(xv[0]) + blo(c0), bhi(xv[0]) + bhi(c0)), h1 = cvtpk(blo(xv[1]) + blo(c1), bhi(xv[1]) + bhi(c1));
      const u32 h2 = dppu<XOR2>(h0), h3 = dppu<XOR2>(h1);
      const bool one = lane & 8;
      const v4u a = {one ? 0x3F803F80u : h0, one ? 0x3F803F80u : h1, one ? 0x3F803F80u : h2, one ? 0x3F803F80u : h3};
      f4 acc[8];
#pragma unroll
      for (int j = 0; j < 8; ++j) acc[j] = mfma(a, f[j], f4{0, 0, 0, 0});
      const f4 gm = mfma(a, a, f4{0, 0, 0, 0});
      float o[8];
#pragma unroll
      for (int j = 0; j < 8; ++j) o[j] = acc[j][0] + dpp<ROR8>(acc[j][1]);
      if (r16 < 8) {
        LAS char* yp = L + L_R + yt * 1024 + ((lane & 3) * 16 + g4 * 4 + (r16 >> 2) * 2) * 16;
        *(LAS f4*)yp = f4{o[0], o[1], o[2], o[3]};
        *(LAS f4*)(yp + 16) = f4{o[4], o[5], o[6], o[7]};
      }
      const float sv = gm[0] + dpp<SHL1>(gm[1]);
      if (g4 < 2 && (r16 == 8 || r16 == 4 * g4)) *(LAS float*)(L + L_ST + ((r16 == 8 ? 0 : 2) + g4) * 64 + yt * 4) = sv;
      lds_arrive(FL + F_RCNT, lane);
      STAMP(7);
    } else if (v2 >= 0) {
      lds_wait(FL + F_VISS, v2 < 4 ? 10 + v2 : 10);
      const rsrc_t r2 = mkrs(w2, FF * D * 2);
      const int voff = (v2 < 4 || r16 < 8) ? (yi * 32 + g4 * 8) * (D * 2) + (v2 * 128 + r16 * 8) * 2 : OOB;
#pragma unroll
      for (int i = 0; i < 8; ++i) t[i] = __builtin_amdgcn_raw_buffer_load_b128(r2, voff, i * (D * 2), AUX_NT);
      SCHED();
      v4u f[8];
      tr8(t, f);
      PIN8(f);
      STAMP(5);
      lds_wait(FL + F_RCNT, 9);
      STAMP(6);
      float st = r16 < 9 ? *(LAS float*)(L + L_ST + lane * 4) : 0.f;
      f4 s = *(LAS f4*)(L + L_R + lane * 16);
#pragma unroll
      for (int k = 1; k < 9; ++k) s += *(LAS f4*)(L + L_R + k * 1024 + lane * 16);
      st += dpp<XOR1>(st); st += dpp<XOR2>(st); st += dpp<HMIR>(st); st += dpp<ROR8>(st);
      const float a0 = asf(__builtin_amdgcn_readlane(asu(st), 0)), a1 = asf(__builtin_amdgcn_readlane(asu(st), 16));
      const float q0 = asf(__builtin_amdgcn_readlane(asu(st), 32)), q1 = asf(__builtin_amdgcn_readlane(asu(st), 48));
      const float mean = ((lane & 4) ? a1 : a0) * (1.f / D);
      const float var = ((lane & 4) ? q1 : q0) * (1.f / D) - mean * mean;
      const float rs = __builtin_amdgcn_rsqf(fmaxf(var, 0.f) + 1e-5f);
      float hid[4];
#pragma unroll
      for (int j = 0; j < 4; ++j) {
        const float zv = rs * (s[j] - mean * dpp<ROR8>(s[j]));
        const float up = dpp<XOR2>(zv);
        hid[j] = zv * __builtin_amdgcn_rcpf(1.f + __builtin_amdgcn_exp2f(-1.4426950408889634f * zv)) * up;
      }
      const u32 h0 = cvtpk(hid[0], hid[1]), h1 = cvtpk(hid[2], hid[3]);
      const v4u a = {h0, h1, dppu<XOR1>(h0), dppu<XOR1>(h1)};
      f4 acc[8];
#pragma unroll
      for (int j = 0; j < 8; ++j) acc[j] = mfma(a, f[j], f4{0, 0, 0, 0});
      const int mo = (g4 < 2 && (v2 < 4 || r16 < 8)) ? WS_MP + (g4 * 36 + v2 * 8 + (r16 >> 2) * 2) * 9216 + yi * 64 + (lane & 3) * 16 : OOB;
      __builtin_amdgcn_raw_buffer_store_b128(nosent4(f4{acc[0][0], acc[1][0], acc[2][0], acc[3][0]}), rws, mo, 0, AUX_SC1);
      __builtin_amdgcn_raw_buffer_store_b128(nosent4(f4{acc[4][0], acc[5][0], acc[6][0], acc[7][0]}), rws, mo, 9216, AUX_SC1);
      STAMP(7);
    }
  }
  STAMP_FLUSH();
}

extern "C" void kernel_launch(void* const* d_in, const int* in_sizes, int n_in, void* d_out, int out_size, void* d_ws, size_t ws_size,
                              hipStream_t stream) {
  fused_decode<<<256, 1024, 0, stream>>>((const u16*)d_in[0], (const u16*)d_in[1], (const u16*)d_in[2], (const u16*)d_in[3],
                                         (const u16*)d_in[4], (const u16*)d_in[6], (const u16*)d_in[7], (const u16*)d_in[8], (u16*)d_out,
                                         (char*)d_ws);
}
```

```cpp
#include <hip/hip_runtime.h>
#include <hip/hip_bf16.h>
#include <stdint.h>

#define GAS __attribute__((address_space(1)))
#define LAS __attribute__((address_space(3)))
#define CAS __attribute__((address_space(4)))
typedef unsigned u32;
typedef unsigned long long u64;
typedef unsigned short u16;
typedef u32 v4u __attribute__((ext_vector_type(4)));
typedef u32 v2u __attribute__((ext_vector_type(2)));
typedef float f4 __attribute__((ext_vector_type(4)));
typedef float f2 __attribute__((ext_vector_type(2)));
typedef __bf16 bf8 __attribute__((ext_vector_type(8)));
typedef __bf16 bf2 __attribute__((ext_vector_type(2)));
typedef __amdgpu_buffer_rsrc_t rsrc_t;

#define QSLEEP 30
#define KW0 2
#define KWN 3
#define KOPEN 10
#define KWN2 5
#define VPRE 4
#define VWN 8
#define VTRIG 15
#define PWIN 4
#define KVDELAY 8
#define PFFLAG F_OCNT
#define PFGATE 5
#define PFDELAY 0
#define XFLAG F_OCNT
#define XGATE 1
#define XWN 32
#define WOGATE 2
#define XDELAY 0
#define HOLDT 0
constexpr int D = 576, SK = 8192, WIN = 2048, FF = 4608, QD = 4096;
constexpr int WS_EPOCH = 0, WS_FLAGS = 4096, WS_ML = WS_FLAGS + 256 * 128, WS_OP = WS_ML + 256 * 128, WS_AP = WS_OP + 131072,
              WS_MP = WS_AP + 36864, WS_QG = WS_MP + 663552, WS_END = WS_QG + 262144;
constexpr int WS_STAMP = 2 << 20;
constexpr u32 SENT = 0xFFFFFFFFu;
constexpr int AUX_NT = 2, AUX_SC1 = 16, OOB = 1 << 30;
constexpr int SPIN_LDS = 1 << 18, SPIN_GLB = 1 << 15;
constexpr int L_FLAG = 0;
constexpr int L_Q = 256;
constexpr int L_P = 768;
constexpr int L_MX = 1792;
constexpr int L_ST = 1920;
constexpr int L_QP = 2304;
constexpr int L_O = 4096;
constexpr int L_R = 8192;
constexpr int L_DUMMY = L_R + 9216;
constexpr int L_STAMP = L_DUMMY + 2048;
constexpr int L_END = L_STAMP + 2048;
enum { F_QCNT = 0, F_QRDY, F_PCNT, F_OCNT, F_VISS, F_CLR, F_INOK, F_RCNT, F_PF, F_BI, F_TAG = 16 };

__device__ __forceinline__ rsrc_t mkrs(const void* p, int bytes) { return __builtin_amdgcn_make_buffer_rsrc((void*)p, 0, bytes, 0x00020000); }
__device__ __forceinline__ float asf(u32 u) { return __uint_as_float(u); }
__device__ __forceinline__ u32 asu(float f) { return __float_as_uint(f); }
__device__ __forceinline__ u32 cvtpk(float lo, float hi) { return __builtin_bit_cast(u32, __builtin_convertvector(f2{lo, hi}, bf2)); }
__device__ __forceinline__ float rbf(float f) { return asf(cvtpk(f, f) & 0xffff0000u); }
__device__ __forceinline__ float blo(u32 u) { return asf(u << 16); }
__device__ __forceinline__ float bhi(u32 u) { return asf(u & 0xffff0000u); }
__device__ __forceinline__ float ex2(float x) { return __builtin_amdgcn_exp2f(x); }
__device__ __forceinline__ float dot2(u32 a, u32 b, float c) { return __builtin_amdgcn_fdot2_f32_bf16(__builtin_bit_cast(bf2, a), __builtin_bit_cast(bf2, b), c, false); }
template <int C> __device__ __forceinline__ u32 dppu(u32 x) { return (u32)__builtin_amdgcn_mov_dpp((int)x, C, 0xf, 0xf, true); }
template <int C> __device__ __forceinline__ float dpp(float x) { return asf(dppu<C>(asu(x))); }
constexpr int XOR1 = 0xB1, XOR2 = 0x4E, HMIR = 0x141, ROR4 = 0x124, ROR8 = 0x128, SHL1 = 0x101;
__device__ __forceinline__ float x16_sum(float x) { auto s = __builtin_amdgcn_permlane16_swap(asu(x), asu(x), false, false); return asf(s[0]) + asf(s[1]); }
__device__ __forceinline__ float x32_sum(float x) { auto s = __builtin_amdgcn_permlane32_swap(asu(x), asu(x), false, false); return asf(s[0]) + asf(s[1]); }
__device__ __forceinline__ float x16_max(float x) { auto s = __builtin_amdgcn_permlane16_swap(asu(x), asu(x), false, false); return fmaxf(asf(s[0]), asf(s[1])); }
__device__ __forceinline__ float x32_max(float x) { auto s = __builtin_amdgcn_permlane32_swap(asu(x), asu(x), false, false); return fmaxf(asf(s[0]), asf(s[1])); }
__device__ __forceinline__ f4 mfma(v4u a, v4u b, f4 c) { return __builtin_amdgcn_mfma_f32_16x16x32_bf16(__builtin_bit_cast(bf8, a), __builtin_bit_cast(bf8, b), c, 0, 0, 0); }
__device__ __forceinline__ u32 plo(u32 a, u32 b) { return __builtin_amdgcn_perm(b, a, 0x05040100u); }
__device__ __forceinline__ u32 phi(u32 a, u32 b) { return __builtin_amdgcn_perm(b, a, 0x07060302u); }
__device__ __forceinline__ void tr8(const v4u* r, v4u* f) {
#pragma unroll
  for (int d = 0; d < 4; ++d)
#pragma unroll
    for (int e = 0; e < 4; ++e) { f[2 * e][d] = plo(r[2 * d][e], r[2 * d + 1][e]); f[2 * e + 1][d] = phi(r[2 * d][e], r[2 * d + 1][e]); }
}
__device__ __forceinline__ void tr4(const v2u* r, v4u* f) {
#pragma unroll
  for (int d = 0; d < 4; ++d)
#pragma unroll
    for (int e = 0; e < 2; ++e) { f[2 * e][d] = plo(r[2 * d][e], r[2 * d + 1][e]); f[2 * e + 1][d] = phi(r[2 * d][e], r[2 * d + 1][e]); }
}
__device__ __forceinline__ u32 umax(u32 a, u32 b) { return a > b ? a : b; }
__device__ __forceinline__ u32 nosent(u32 x) { return x < 0xFFFFFFFEu ? x : 0xFFFFFFFEu; }
__device__ __forceinline__ v4u nosent4(f4 v) { return v4u{nosent(asu(v[0])), nosent(asu(v[1])), nosent(asu(v[2])), nosent(asu(v[3]))}; }
__device__ __forceinline__ u32 mx4(v4u v, u32 m) { return umax(umax(umax(v[0], v[1]), m), umax(v[2], v[3])); }
__device__ __forceinline__ bool none_sent(u32 m) { return __builtin_amdgcn_ballot_w64(m == SENT) == 0; }
#define PIN4(f) asm volatile("" : "+v"((f)[0]), "+v"((f)[1]), "+v"((f)[2]), "+v"((f)[3]))
#define PIN8(f) asm volatile("" : "+v"(f[0]), "+v"(f[1]), "+v"(f[2]), "+v"(f[3]), "+v"(f[4]), "+v"(f[5]), "+v"(f[6]), "+v"(f[7]))
#define CBAR() asm volatile("" ::: "memory")
#define SCHED() __builtin_amdgcn_sched_barrier(0)
#define OPAQUE(x) asm volatile("" : "+s"(x))
template <int N> __device__ __forceinline__ void waitvm() { asm volatile("s_waitcnt vmcnt(%0)" ::"i"(N) : "memory"); }
#define NOUNROLL _Pragma("nounroll")
#define LD128(off, soff) __builtin_amdgcn_raw_buffer_load_b128(rws, off, soff, AUX_SC1)
#define LD64(off, soff) __builtin_amdgcn_raw_buffer_load_b64(rws, off, soff, AUX_SC1)
#define ST128(v, off, soff) __builtin_amdgcn_raw_buffer_store_b128(v, rws, off, soff, AUX_SC1)
#define ST64(v, off, soff) __builtin_amdgcn_raw_buffer_store_b64(v, rws, off, soff, AUX_SC1)
#define BARRIER() do { SCHED(); asm volatile("s_barrier" ::: "memory"); SCHED(); } while (0)

template <int F, int SLEEP> __device__ __forceinline__ void lds_wait(LAS u32* fl, u32 want) {
  CBAR();
  NOUNROLL for (int n = 0; n < SPIN_LDS; ++n) {
    if ((u32)__builtin_amdgcn_readfirstlane((int)__hip_atomic_load(fl + F, __ATOMIC_RELAXED, __HIP_MEMORY_SCOPE_WORKGROUP)) >= want) break;
    if (SLEEP) __builtin_amdgcn_s_sleep(SLEEP);
  }
  CBAR();
}
template <int F> __device__ __forceinline__ void lds_add(LAS u32* fl) {
  asm volatile("s_mov_b64 exec, 1\n\tds_add_u32 %0, %1 offset:%2\n\ts_mov_b64 exec, -1" ::"v"((u32)(size_t)fl), "v"(1u), "i"(4 * F) : "memory");
}
template <int F> __device__ __forceinline__ u32 lds_add_rtn(LAS u32* fl) {
  u32 old;
  asm volatile("s_mov_b64 exec, 1\n\tds_add_rtn_u32 %0, %1, %2 offset:%3\n\ts_mov_b64 exec, -1\n\ts_waitcnt lgkmcnt(0)"
               : "=v"(old) : "v"((u32)(size_t)fl), "v"(1u), "i"(4 * F) : "memory");
  return (u32)__builtin_amdgcn_readfirstlane((int)old);
}
__device__ __forceinline__ int sel(bool c, int a, int b) { asm volatile("" : "+v"(a)); return c ? a : b; }

#define STAMP(k) do {} while (0)
#define STAMP_INIT() do {} while (0)
#define STAMP_FLUSH() do {} while (0)

__global__ void __launch_bounds__(1024) fused_decode(const u16* x_a, const u16* kc_a, const u16* vc_a, const u16* wq_a, const u16* wo_a,
                                                     const u16* w1_a, const u16* w3_a, const u16* w2_a, u16* out_a, char* ws_a) {
  __shared__ __attribute__((aligned(16))) char smem[L_END];
  LAS char* L = (LAS char*)smem;
  LAS u32* FL = (LAS u32*)(L + L_FLAG);
  const int bid = blockIdx.x, lane = threadIdx.x & 63, w = __builtin_amdgcn_readfirstlane(threadIdx.x >> 6);
  u64 pc0; asm volatile("s_getpc_b64 %0" : "=s"(pc0));
  if (w == 0) {
    FL[lane] = 0;
    asm volatile("s_waitcnt lgkmcnt(0)" ::: "memory");
  }
  SCHED();
  const CAS u64* kargs = (const CAS u64*)__builtin_amdgcn_kernarg_segment_ptr();
  OPAQUE(kargs);
  const u16 *x = (const u16*)kargs[0], *kc = (const u16*)kargs[1], *vc = (const u16*)kargs[2], *wq = (const u16*)kargs[3],
            *wo = (const u16*)kargs[4], *w1 = (const u16*)kargs[5], *w3 = (const u16*)kargs[6], *w2 = (const u16*)kargs[7];
  u16* out = (u16*)kargs[8];
  char* ws = (char*)kargs[9];
  BARRIER();
  const u64 tstart = HOLDT ? __builtin_amdgcn_s_memrealtime() : 0;
#define HOLD() do { if (HOLDT) for (int g = 0; g < 100000 && __builtin_amdgcn_s_memrealtime() < tstart + HOLDT; ++g) __builtin_amdgcn_s_sleep(2); } while (0)
  STAMP_INIT();
  STAMP(0);
  const int xcd = bid & 7, ii = bid >> 3;
  const int kvh = 2 * xcd + (ii >> 4), jj = ii & 15, bb = jj >> 3;
  const bool isY = ii < 18, isX = ii >= 18 && ii < 27;
  const int yi = xcd * 18 + ii, xc = ii - 18;
  const int g4 = lane >> 4, r16 = lane & 15, hb = lane >> 5, l5 = lane & 31;
  const rsrc_t rws = mkrs(ws, WS_END);
  const rsrc_t rx = mkrs(x, 2 * D * 2);
  const int kw = w & 3;
  const int rot = bid * 5;
#define LOAD_TAG() (*(const CAS u64*)(ws + WS_EPOCH) + 1)
#define LDS_TAG() (*(LAS v2u*)(FL + F_TAG))

  int yt = -1;
  int v2 = -1;
  if (w < 3) {
    v4u xs[4], tq[8];
    const int k0 = w * 64 + (r16 >> 3) * 32 + g4 * 8, ka = w * 64 + (lane & 1) * 32 + g4 * 8;
    const int xo = l5 < 24 ? hb * (D * 2) + l5 * 16 : OOB;
#pragma unroll
    for (int i = 0; i < 3; ++i) xs[i] = __builtin_amdgcn_raw_buffer_load_b128(rx, xo + i * 384, 0, 0);
    xs[3] = __builtin_amdgcn_raw_buffer_load_b128(rx, sel(ka < 144, ((r16 >> 2) & 1) * (D * 2) + ka * 2, OOB), (jj & 3) * 288, 0);
    const rsrc_t rq = mkrs(wq, D * QD * 2);
    const int qo = sel(k0 < 144, k0 * (QD * 2) + (lane & 7) * 16, OOB) + (jj & 3) * (144 * QD * 2) + kvh * 512 + (jj >> 2) * 128;
#pragma unroll
    for (int i = 0; i < 8; ++i) tq[i] = __builtin_amdgcn_raw_buffer_load_b128(rq, qo, i * (QD * 2), AUX_NT);
    const u64 tag = LOAD_TAG();
    int qpo = sel((lane & 0x28) == 0, L_QP + w * 512 + g4 * 256 + r16 * 32, L_DUMMY + lane * 32);
    int qso = WS_QG + ((hb * 16 + kvh) * 4 + (jj & 3)) * 2048 + (jj >> 2) * 512 + l5 * 16;
    asm volatile("" : "+v"(qpo), "+v"(qso));
    SCHED();
    float s = 0, v = 0;
#pragma unroll
    for (int i = 0; i < 3; ++i)
#pragma unroll
      for (int e = 0; e < 4; ++e) { s = dot2(xs[i][e], 0x3F803F80u, s); v = dot2(xs[i][e], xs[i][e], v); }
    s += dpp<XOR1>(s); v += dpp<XOR1>(v); s += dpp<XOR2>(s); v += dpp<XOR2>(v);
    s += dpp<HMIR>(s); v += dpp<HMIR>(v); s += dpp<ROR8>(s); v += dpp<ROR8>(v);
    s = x16_sum(s); v = x16_sum(v);
    const float mean = s * (1.f / D);
    const float mb = rbf(mean);
    const float rd = __builtin_amdgcn_rcpf(rbf(__builtin_amdgcn_sqrtf(rbf(rbf(fmaxf(v * (1.f / D) - mean * mean, 0.f)) + 1e-5f))));
    const auto ms = __builtin_amdgcn_permlane32_swap(asu(mb), asu(mb), false, false);
    const auto rs = __builtin_amdgcn_permlane32_swap(asu(rd), asu(rd), false, false);
    const float mm = asf((lane & 4) ? ms[1] : ms[0]), rr = asf((lane & 4) ? rs[1] : rs[0]);
    v4u a;
#pragma unroll
    for (int e = 0; e < 4; ++e) { u32 c = cvtpk(blo(xs[3][e]) - mm, bhi(xs[3][e]) - mm); a[e] = cvtpk(blo(c) * rr, bhi(c) * rr); }
    asm volatile("" : "+v"(a));
    SCHED();
    STAMP(11);
    v4u f[8];
    tr8(tq, f);
    f4 acc[8];
#pragma unroll
    for (int j = 0; j < 8; ++j) acc[j] = mfma(a, f[j], f4{0, 0, 0, 0});
    float o[8];
#pragma unroll
    for (int j = 0; j < 8; ++j) o[j] = acc[j][0] + dpp<ROR8>(acc[j][1]);
    STAMP(1);
    *(LAS f4*)(L + qpo) = f4{o[0], o[1], o[2], o[3]};
    *(LAS f4*)(L + qpo + 16) = f4{o[4], o[5], o[6], o[7]};
    if (lds_add_rtn<F_QCNT>(FL) == 2) {
      const f2 q = (*(LAS f2*)(L + L_QP + lane * 8) + *(LAS f2*)(L + L_QP + 512 + lane * 8)) + *(LAS f2*)(L + L_QP + 1024 + lane * 8);
      const u32 th = (u32)(tag >> 8), tl = (u32)tag & 0xffu;
      ST128((v4u{((asu(q[0]) + 0x80u) & ~0xffu) | tl, th, ((asu(q[1]) + 0x80u) & ~0xffu) | tl, th}), qso, 0);
      lds_add<F_QCNT>(FL);
      STAMP(2);
      const int go = WS_QG + (bb * 16 + kvh) * 8192 + lane * 16;
      v4u g[8];
      __builtin_amdgcn_s_sleep(QSLEEP);
      NOUNROLL for (int n = 0; n < SPIN_GLB; ++n) {
#pragma unroll
        for (int k = 0; k < 8; ++k) g[k] = LD128(go, k * 1024);
        CBAR();
        u32 bad = 0, lo = 0;
#pragma unroll
        for (int k = 0; k < 8; ++k) { bad |= (g[k][1] ^ th) | (g[k][3] ^ th); lo |= (g[k][0] ^ tl) | (g[k][2] ^ tl); }
        bad |= lo & 0xffu;
        if (n == 0) STAMP(4);
        if (__builtin_amdgcn_ballot_w64(bad != 0) == 0) break;
        __builtin_amdgcn_s_sleep(2);
      }
#pragma unroll
      for (int c = 0; c < 2; ++c) {
        const float a0 = (asf(g[c][0] & ~0xffu) + asf(g[2 + c][0] & ~0xffu)) + (asf(g[4 + c][0] & ~0xffu) + asf(g[6 + c][0] & ~0xffu));
        const float a1 = (asf(g[c][2] & ~0xffu) + asf(g[2 + c][2] & ~0xffu)) + (asf(g[4 + c][2] & ~0xffu) + asf(g[6 + c][2] & ~0xffu));
        *(LAS u32*)(L + L_Q + c * 256 + lane * 4) = cvtpk(a0, a1);
      }
      lds_add<F_QRDY>(FL);
      STAMP(3);
    }
    yt = w + 3;
  }
  int w1_ = w;
  OPAQUE(w1_);
  v4u t[16];
  const rsrc_t rk = mkrs(w < 8 ? kc : vc, 2 * 16 * SK * 256);
  const int kvbase = ((bb * 16 + kvh) * SK + (SK - WIN) + (jj & 7) * 256 + kw * 64) * 256;
  if (w1_ >= 4 && w1_ < 8) {
    const int voff = (r16 >> 2) * 256 + (g4 * 4 + (lane & 3)) * 16 + kvbase;
    if (KVDELAY) __builtin_amdgcn_s_sleep(KVDELAY);
#pragma unroll
    for (int i = 0; i < 16; ++i) {
      if (i == KW0) waitvm<KW0 - 1>();
      else if (i > KWN && i < KOPEN) waitvm<KWN - 1>();
      else if (i >= KOPEN) waitvm<KWN2 - 1>();
      if (i == KW0) STAMP(10);
      if (i == KOPEN) STAMP(11);
      t[i] = __builtin_amdgcn_raw_buffer_load_b128(rk, voff, ((i + rot) & 15) * 1024, AUX_NT);
      SCHED();
    }
    STAMP(1);
    lds_wait<F_QRDY, 1>(FL, 1);
    STAMP(2);
    v4u qf = *(LAS v4u*)(L + L_Q + ((r16 >> 2) & 1) * 256 + (g4 * 4 + (lane & 3)) * 16);
    const v4u z4 = {0, 0, 0, 0};
    const v4u q0 = r16 < 8 ? qf : z4, q1 = r16 < 8 ? z4 : qf;
    float sc[8];
#pragma unroll
    for (int m = 0; m < 8; ++m) {
      f4 d = mfma(t[m], q0, f4{0, 0, 0, 0});
      d = mfma(t[m + 8], q1, d);
      sc[m] = dpp<0xFF>(d[3]) + (dpp<0xAA>(d[2]) + (dpp<0x55>(d[1]) + dpp<0x00>(d[0])));
    }
    const bool b0 = lane & 1, b1 = lane & 2;
    float s0 = b1 ? (b0 ? sc[6] : sc[4]) : (b0 ? sc[2] : sc[0]);
    float s1 = b1 ? (b0 ? sc[7] : sc[5]) : (b0 ? sc[3] : sc[1]);
    u32 pk = cvtpk(s0, s1);
    pk = cvtpk(blo(pk) * 0.08838834764831845f, bhi(pk) * 0.08838834764831845f);
    s0 = blo(pk) * 1.4426950408889634f; s1 = bhi(pk) * 1.4426950408889634f;
    float mx = fmaxf(s0, s1);
    mx = fmaxf(mx, dpp<XOR1>(mx)); mx = fmaxf(mx, dpp<XOR2>(mx)); mx = fmaxf(mx, dpp<ROR8>(mx));
    mx = x32_max(x16_max(mx));
    const int hd = (r16 >> 2) & 1;
    LAS float* MX = (LAS float*)(L + L_MX);
    const float p0 = ex2(s0 - mx), p1 = ex2(s1 - mx);
    *(LAS u32*)(L + L_P + kw * 256 + hd * 128 + (r16 >> 3) * 64 + g4 * 16 + (lane & 3) * 4) = cvtpk(p0, p1);
    float ls = p0 + p1;
    ls += dpp<XOR1>(ls); ls += dpp<XOR2>(ls); ls += dpp<ROR8>(ls);
    ls = x32_sum(x16_sum(ls));
    MX[hd * 4 + kw] = mx; MX[8 + hd * 4 + kw] = ls;
    lds_add<F_PCNT>(FL);
    STAMP(3);
    if (isX && kw == 0) {
      const int z = xcd * 9 + xc, zl = z >> 1, zb = zl / 18, zz = zl % 18;
      const int col = (zz >> 1) * 64 + (z & 1) * 32 + (lane & 3) * 8 + (zz & 1) * 4;
      const v2u xv = __builtin_amdgcn_raw_buffer_load_b64(rx, (zb * D + col) * 2, 0, 0);
      const int ho = WS_AP + ((zz >> 1) * 8 + ((lane >> 2) & 7)) * 512 + (lane & 3) * 128 + zb * 64 + (zz & 1) * 32 + (z & 1) * 16;
      int oo = sel(lane < 4, (zb * D + col) * 2, OOB);
      const int mo = WS_MP + zl * 18432 + (lane >> 2) * 128 + (z & 1) * 64 + (lane & 3) * 16;
      lds_wait<F_RCNT, 2>(FL, 5);
      v4u hv;
      NOUNROLL for (int n = 0; n < SPIN_GLB; ++n) {
        hv = LD128(ho, 0);
        CBAR();
        if (none_sent(mx4(hv, 0))) break;
        __builtin_amdgcn_s_sleep(4);
      }
      float h[4];
#pragma unroll
      for (int j = 0; j < 4; ++j) { float a = asf(hv[j]); a += dpp<ROR4>(a); a += dpp<ROR8>(a); h[j] = x16_sum(a); }
      h[0] = rbf(blo(xv[0]) + rbf(h[0])); h[1] = rbf(bhi(xv[0]) + rbf(h[1]));
      h[2] = rbf(blo(xv[1]) + rbf(h[2])); h[3] = rbf(bhi(xv[1]) + rbf(h[3]));
      PIN4(h);
      asm volatile("" : "+v"(oo));
      STAMP(12);
      v4u p[9];
      NOUNROLL for (int n = 0; n < SPIN_GLB; ++n) {
#pragma unroll
        for (int k = 0; k < 9; ++k) p[k] = LD128(mo, k * 2048);
        CBAR();
        u32 m = 0;
#pragma unroll
        for (int k = 0; k < 9; ++k) m = mx4(p[k], m);
        if (none_sent(m)) break;
        __builtin_amdgcn_s_sleep(1);
      }
      STAMP(13);
      float r[4];
#pragma unroll
      for (int j = 0; j < 4; ++j) {
        float a = ((asf(p[0][j]) + asf(p[1][j])) + (asf(p[2][j]) + asf(p[3][j]))) + ((asf(p[4][j]) + asf(p[5][j])) + (asf(p[6][j]) + asf(p[7][j]))) + asf(p[8][j]);
        a += dpp<ROR4>(a); a += dpp<ROR8>(a);
        r[j] = h[j] + rbf(x32_sum(x16_sum(a)));
      }
      __builtin_amdgcn_raw_buffer_store_b64(v2u{cvtpk(r[0], r[1]), cvtpk(r[2], r[3])}, mkrs(out, 2 * D * 2), oo, 0, 0);
      if (z == 0) __builtin_amdgcn_raw_buffer_store_b64(LDS_TAG(), rws, lane == 0 ? WS_EPOCH : OOB, 0, 0);
      STAMP(14);
    }
    if (kw == 0) yt = 7;
    if (kw == 1) v2 = 4;
  }
  int w2_ = w;
  OPAQUE(w2_);
  if (w2_ >= 8 && w2_ < 12) {
    const int voff = lane * 16 + kvbase;
    if (KVDELAY) __builtin_amdgcn_s_sleep(KVDELAY);
#pragma unroll
    for (int i = 0; i < 16; ++i) {
      if (i >= 1 && i <= VPRE) waitvm<0>();
      else if (i >= VPRE + VWN) waitvm<VWN - 1>();
      t[i] = __builtin_amdgcn_raw_buffer_load_b128(rk, voff, ((i + rot) & 15) * 1024, AUX_NT);
      SCHED();
      if (i == VPRE) STAMP(10);
      if (i == VTRIG) lds_add<F_VISS>(FL);
    }
    STAMP(1);
    v4u f[8];
    tr8(t, f);
    PIN8(f);
    lds_wait<F_PCNT, 1>(FL, 4);
    STAMP(2);
    const v4u pa = *(LAS v4u*)(L + L_P + kw * 256 + ((r16 >> 2) & 1) * 128 + g4 * 16);
    const v4u pb = *(LAS v4u*)(L + L_P + kw * 256 + ((r16 >> 2) & 1) * 128 + 64 + g4 * 16);
    const f4 m4 = *(LAS f4*)(L + L_MX + (g4 & 1) * 16), l4 = *(LAS f4*)(L + L_MX + 32 + (g4 & 1) * 16);
    const float mw = *(LAS float*)(L + L_MX + (g4 & 1) * 16 + kw * 4);
    f4 acc[8];
#pragma unroll
    for (int j = 0; j < 8; ++j) acc[j] = mfma(pa, f[j], f4{0, 0, 0, 0});
    const float M = fmaxf(fmaxf(m4[0], m4[1]), fmaxf(m4[2], m4[3]));
    float fs = ex2(mw - M);
    v2u ml = {nosent(asu(M)), nosent(asu((l4[0] * ex2(m4[0] - M) + l4[1] * ex2(m4[1] - M)) + (l4[2] * ex2(m4[2] - M) + l4[3] * ex2(m4[3] - M))))};
    int mlo = sel((lane & 0x2F) == 0, WS_ML + (kvh * 16 + jj) * 128 + g4 * 8, OOB);
    int opo = WS_OP + ((2 * kvh + hb) * 16 + jj) * 256 + l5 * 8;
    int lo = L_O + kw * 1024 + (g4 & 1) * 512 + r16 * 32 + (g4 >> 1) * 16;
    asm volatile("" : "+v"(fs), "+v"(ml), "+v"(mlo), "+v"(opo), "+v"(lo));
    lds_wait<F_CLR, 1>(FL, 1);
    SCHED();
    tr8(t + 8, f);
#pragma unroll
    for (int j = 0; j < 8; ++j) acc[j] = mfma(pb, f[j], acc[j]);
    STAMP(3);
    {
      const bool hi = g4 >= 2;
      f4 o = {hi ? acc[4][0] : acc[0][0], hi ? acc[5][0] : acc[1][0], hi ? acc[6][0] : acc[2][0], hi ? acc[7][0] : acc[3][0]};
      *(LAS f4*)(L + lo) = o * fs;
    }
    if (lds_add_rtn<F_OCNT>(FL) == 3) {
      ST64(ml, mlo, 0);
      f4 o = (*(LAS f4*)(L + L_O + lane * 16) + *(LAS f4*)(L + L_O + 1024 + lane * 16)) +
             (*(LAS f4*)(L + L_O + 2048 + lane * 16) + *(LAS f4*)(L + L_O + 3072 + lane * 16));
      ST64((v2u{nosent(cvtpk(o[0], o[1])), nosent(cvtpk(o[2], o[3]))}), opo, 0);
      lds_add<F_OCNT>(FL);
    }
    STAMP(4);
    v2 = kw;
  }
  int w3_ = w;
  OPAQUE(w3_);
  if (w3_ == 3) {
    const u64 tag = LOAD_TAG();
    u32 cpf = 0;
    {
      cpf = *(const GAS u32*)((const GAS char*)(pc0 & ~(u64)127) + (ii < 30 ? ii * 4 + (lane & 3) : 0) * 128);
    }
    const v4u s4 = {SENT, SENT, SENT, SENT};
    ST64((v2u{SENT, SENT}), WS_OP + ((2 * kvh + hb) * 16 + jj) * 256 + l5 * 8, 0);
    ST64((v2u{SENT, SENT}), sel(lane < 16, WS_ML + (kvh * 16 + jj) * 128 + lane * 8, OOB), 0);
    if (ii == 31) {
      const int ao = WS_AP + (hb * 8 + xcd) * 512 + l5 * 16;
#pragma unroll
      for (int n = 0; n < 4; ++n) ST128(s4, ao, n * 8192);
      ST128(s4, lane < 32 ? ao : OOB, 4 * 8192);
    }
    STAMP(1);
    asm volatile("s_waitcnt vmcnt(0)" ::"v"(cpf) : "memory");
    const v2u tg = {(u32)tag, (u32)(tag >> 32)};
    lds_wait<F_QCNT, 1>(FL, 4);
    ST64(tg, lane == 0 ? WS_FLAGS + bid * 128 : OOB, 0);
    if (isY) {
      const int mo = WS_MP + (lane >> 3) * 18432 + yi * 128 + (lane & 7) * 16;
#pragma unroll
      for (int n = 0; n < 4; ++n) ST128(s4, mo, n * 8 * 18432);
      ST128(s4, lane < 32 ? mo : OOB, 4 * 8 * 18432);
      waitvm<0>();
    }
    *(LAS v2u*)(FL + F_TAG) = tg;
    lds_add<F_CLR>(FL);
    STAMP(2);
    if (ii < 27) {
      const bool act = lane < 32 ? isX : lane < 40;
      const int fo = sel(act, WS_FLAGS + (lane < 32 ? lane * 8 + xcd : 248 + lane - 32) * 128, OOB);
      lds_wait<F_QRDY, 4>(FL, 1);
      NOUNROLL for (int n = 0; n < SPIN_GLB; ++n) {
        const v2u g = LD64(fo, 0);
        CBAR();
        if (__builtin_amdgcn_ballot_w64(act && ((g[0] ^ tg[0]) | (g[1] ^ tg[1])) != 0) == 0) break;
        __builtin_amdgcn_s_sleep(12);
      }
      lds_add<F_INOK>(FL);
      STAMP(3);
    }
    if (!isY) {
      const rsrc_t rp = mkrs(w2, (xcd + 1) * (576 * D * 2));
      const int po = lane * 128 + xcd * (576 * D * 2) + (31 - ii) * 8192;
      if (isX) lds_wait<F_RCNT, 2>(FL, 5); else lds_wait<F_BI, 2>(FL, 1);
      STAMP(4);
      u32 pf[6];
#pragma unroll
      for (int k = 0; k < 6; ++k) {
        if (k >= 4) waitvm<3>();
        pf[k] = __builtin_amdgcn_raw_buffer_load_b32(rp, po, k * 14 * 8192, 0);
        SCHED();
      }
#pragma unroll
      for (int k = 0; k < 6; ++k) asm volatile("" ::"v"(pf[k]));
      STAMP(5);
    }
    yt = 6;
  } else if (w3_ >= 12) {
    if (isX) {
      const int hh = 4 * xcd + kw;
      const rsrc_t ro = mkrs(wo, QD * D * 2);
      const int vo = (hh * 128 + g4 * 16) * (D * 2) + (xc * 64 + r16 * 4) * 2;
      lds_wait<XFLAG, 1>(FL, XGATE);
      if (XDELAY) __builtin_amdgcn_s_sleep(XDELAY);
      HOLD();
      STAMP(1);
      v2u u[32];
#pragma unroll
      for (int i = 0; i < 32; ++i) {
        if (i >= XWN) waitvm<XWN - 1>();
        u[i] = __builtin_amdgcn_raw_buffer_load_b64(ro, vo, ((i >> 4) * 64 + (i & 15)) * (D * 2), AUX_NT);
        SCHED();
      }
      STAMP(4);
      v4u f[16];
#pragma unroll
      for (int k = 0; k < 4; ++k) { tr4(u + 8 * k, f + 4 * k); PIN4(f + 4 * k); }
      int xo = L_R + kw * 512 + ((r16 >> 1) & 3) * 128 + (g4 >> 1) * 64 + (r16 & 1) * 32 + (r16 >> 3) * 16;
      int po = sel(lane < 32, WS_AP + (xc * 8 + xcd) * 512 + lane * 16, OOB);
      asm volatile("" : "+v"(xo), "+v"(po));
      STAMP(5);
      lds_wait<F_INOK, 1>(FL, 1);
      lds_wait<F_OCNT, 1>(FL, 5);
      v4u a[4];
      v2u ml[4];
      const int ao = WS_OP + (hh * 16 + r16) * 256 + g4 * 32;
      const int mo = WS_ML + ((hh >> 1) * 16 + g4 * 4) * 128 + (hh & 1) * 8;
      NOUNROLL for (int n = 0; n < SPIN_GLB; ++n) {
#pragma unroll
        for (int i = 0; i < 4; ++i) ml[i] = LD64(mo + i * 128, 0);
        a[0] = LD128(ao, 0); a[1] = LD128(ao + 16, 0); a[2] = LD128(ao + 128, 0); a[3] = LD128(ao + 144, 0);
        CBAR();
        const u32 m = mx4(v4u{ml[0][0], ml[0][1], ml[1][0], ml[1][1]}, mx4(v4u{ml[2][0], ml[2][1], ml[3][0], ml[3][1]}, 0));
        if (none_sent(mx4(a[3], mx4(a[2], mx4(a[1], mx4(a[0], m)))))) break;
        __builtin_amdgcn_s_sleep(1);
      }
      STAMP(6);
      f4 d[4];
#pragma unroll
      for (int j = 0; j < 4; ++j) d[j] = mfma(a[0], f[j], f4{0, 0, 0, 0});
#pragma unroll
      for (int k = 1; k < 4; ++k)
#pragma unroll
        for (int j = 0; j < 4; ++j) d[j] = mfma(a[k], f[4 * k + j], d[j]);
      const float M = x16_max(fmaxf(fmaxf(asf(ml[0][0]), asf(ml[1][0])), fmaxf(asf(ml[2][0]), asf(ml[3][0]))));
      float e[4];
#pragma unroll
      for (int i = 0; i < 4; ++i) e[i] = ex2(asf(ml[i][0]) - M);
      const float rl = __builtin_amdgcn_rcpf(x16_sum((e[0] * asf(ml[0][1]) + e[1] * asf(ml[1][1])) + (e[2] * asf(ml[2][1]) + e[3] * asf(ml[3][1]))));
#pragma unroll
      for (int i = 0; i < 4; ++i) e[i] *= rl;
      f4 o;
#pragma unroll
      for (int j = 0; j < 4; ++j) o[j] = x16_sum((e[0] * d[j][0] + e[1] * d[j][1]) + (e[2] * d[j][2] + e[3] * d[j][3]));
      *(LAS f4*)(L + xo) = o;
      if (lds_add_rtn<F_RCNT>(FL) == 3) {
        LAS char* rp = L + L_R + l5 * 16;
        const f4 s = (*(LAS f4*)(rp) + *(LAS f4*)(rp + 512)) + (*(LAS f4*)(rp + 1024) + *(LAS f4*)(rp + 1536));
        ST128(nosent4(s), po, 0);
        lds_add<F_RCNT>(FL);
      }
      STAMP(12);
    } else if (w == 15) {
      const int pi = isY ? ii : ii - 9;
      const int br = lane / 9;
      const int band = sel(lane < 63, br * (FF * 2) + (lane - br * 9) * 128, OOB) + xcd * 1152;
      const rsrc_t ro = mkrs(wo, (xcd + 1) * (512 * D * 2));
      const int oo = lane * 128 + xcd * (512 * D * 2) + (22 - pi) * 8192;
      u32 pf[12];
      lds_wait<F_VISS, 1>(FL, WOGATE);
      HOLD();
      STAMP(1);
#pragma unroll
      for (int k = 0; k < 4; ++k) pf[k] = __builtin_amdgcn_raw_buffer_load_b32(ro, oo, k * 23 * 8192, 0);
      SCHED();
      lds_wait<PFFLAG, 1>(FL, PFGATE);
      if (PFDELAY) __builtin_amdgcn_s_sleep(PFDELAY);
      STAMP(4);
#pragma unroll
      for (int k = 0; k < 8; ++k) {
        const int m = 22 - pi + 23 * k;
        waitvm<PWIN - 1>();
        pf[4 + k] = __builtin_amdgcn_raw_buffer_load_b32(mkrs((m & 1) ? w3 : w1, D * FF * 2), band, (m >> 1) * (7 * FF * 2), 0);
        SCHED();
      }
      STAMP(2);
      if (!isY) lds_add<F_BI>(FL);
#pragma unroll
      for (int k = 0; k < 12; ++k) asm volatile("" ::"v"(pf[k]));
      lds_add<F_PF>(FL);
      STAMP(3);
    }
    yt = w == 15 ? 8 : w - 12;
  }
  if (isY) {
    if (yt >= 0) {
      const GAS char* wp = (const GAS char*)((lane & 4) ? w3 : w1) + (size_t)((yt * 64 + (r16 >> 3) * 32 + g4 * 8) * (FF * 2) + (yi * 32 + (lane & 3) * 8) * 2);
      const int ao = WS_AP + (yt * 8 + (r16 >> 3)) * 512 + g4 * 128 + (lane & 7) * 16;
      const int kx = (ii & 3) * 1024;
      int ypo = sel(r16 < 8, L_R + yt * 1024 + ((lane & 3) * 16 + g4 * 4 + (r16 >> 2) * 2) * 16, L_DUMMY + lane * 32);
      int sto = sel(lane == 0 || lane == 8 || lane == 20 || lane == 24, L_ST + ((r16 == 8 ? 0 : 2) + g4) * 64 + yt * 4, L_DUMMY + lane * 32);
      asm volatile("" : "+v"(ypo), "+v"(sto), "+v"(wp));
      lds_wait<F_INOK, 2>(FL, 1);
      lds_wait<F_PF, 1>(FL, 1);
#pragma unroll
      for (int i = 0; i < 8; ++i) t[i] = __builtin_nontemporal_load((const GAS v4u*)(wp + i * (FF * 2)));
      const v2u xv = __builtin_amdgcn_raw_buffer_load_b64(rx, (((lane >> 2) & 1) * D + yt * 64 + (lane & 1) * 32 + g4 * 8 + ((lane >> 1) & 1) * 4) * 2, 0, 0);
      SCHED();
      lds_add<F_BI>(FL);
      STAMP(13);
      v4u f[8];
      tr8(t, f);
      PIN8(f);
      STAMP(14);
      lds_wait<F_BI, 1>(FL, 9);
      v4u p[4];
      NOUNROLL for (int n = 0; n < SPIN_GLB; ++n) {
#pragma unroll
        for (int k = 0; k < 4; ++k) p[k] = LD128(ao, (k * 1024) ^ kx);
        CBAR();
        if (none_sent(mx4(p[3], mx4(p[2], mx4(p[1], mx4(p[0], 0)))))) break;
        NOUNROLL for (; n < SPIN_GLB && !none_sent(mx4(p[0], 0)); ++n) {
          __builtin_amdgcn_s_sleep(3);
          p[0] = LD128(ao, kx);
          CBAR();
        }
      }
      STAMP(15);
      float h[4];
#pragma unroll
      for (int j = 0; j < 4; ++j) { float a = (asf(p[0][j]) + asf(p[1][j])) + (asf(p[2][j]) + asf(p[3][j])); h[j] = a + dpp<ROR8>(a); }
      const u32 c0 = cvtpk(h[0], h[1]), c1 = cvtpk(h[2], h[3]);
      const u32 h0 = cvtpk(blo(xv[0]) + blo(c0), bhi(xv[0]) + bhi(c0)), h1 = cvtpk(blo(xv[1]) + blo(c1), bhi(xv[1]) + bhi(c1));
      const u32 h2 = dppu<XOR2>(h0), h3 = dppu<XOR2>(h1);
      const bool one = lane & 8;
      const v4u a = {one ? 0x3F803F80u : h0, one ? 0x3F803F80u : h1, one ? 0x3F803F80u : h2, one ? 0x3F803F80u : h3};
      f4 acc[8];
#pragma unroll
      for (int j = 0; j < 8; ++j) acc[j] = mfma(a, f[j], f4{0, 0, 0, 0});
      const f4 gm = mfma(a, a, f4{0, 0, 0, 0});
      float o[8];
#pragma unroll
      for (int j = 0; j < 8; ++j) o[j] = acc[j][0] + dpp<ROR8>(acc[j][1]);
      *(LAS f4*)(L + ypo) = f4{o[0], o[1], o[2], o[3]};
      *(LAS f4*)(L + ypo + 16) = f4{o[4], o[5], o[6], o[7]};
      *(LAS float*)(L + sto) = gm[0] + dpp<SHL1>(gm[1]);
      lds_add<F_RCNT>(FL);
      STAMP(7);
    } else if (v2 >= 0) {
      const rsrc_t r2 = mkrs(w2, FF * D * 2);
      const int vo = sel(v2 < 4 || r16 < 8, (yi * 32 + g4 * 8) * (D * 2) + (v2 * 128 + r16 * 8) * 2, OOB);
      int mo = sel(g4 < 2 && (v2 < 4 || r16 < 8), WS_MP + (g4 * 18 + v2 * 4 + (r16 >> 3) * 2) * 18432 + yi * 128 + (lane & 7) * 16, OOB);
      asm volatile("" : "+v"(mo));
      lds_wait<F_BI, 1>(FL, 9);
#pragma unroll
      for (int i = 0; i < 8; ++i) t[i] = __builtin_amdgcn_raw_buffer_load_b128(r2, vo, i * (D * 2), AUX_NT);
      SCHED();
      STAMP(13);
      v4u f[8];
      tr8(t, f);
      PIN8(f);
      STAMP(14);
      lds_wait<F_RCNT, 0>(FL, 9);
      STAMP(15);
      float st = *(LAS float*)(L + L_ST + lane * 4);
      asm volatile("" : "+v"(st));
      st = r16 < 9 ? st : 0.f;
      f4 s = *(LAS f4*)(L + L_R + lane * 16);
#pragma unroll
      for (int k = 1; k < 9; ++k) s += *(LAS f4*)(L + L_R + k * 1024 + lane * 16);
      st += dpp<XOR1>(st); st += dpp<XOR2>(st); st += dpp<HMIR>(st); st += dpp<ROR8>(st);
      const float a0 = asf(__builtin_amdgcn_readlane(asu(st), 0)), a1 = asf(__builtin_amdgcn_readlane(asu(st), 16));
      const float q0 = asf(__builtin_amdgcn_readlane(asu(st), 32)), q1 = asf(__builtin_amdgcn_readlane(asu(st), 48));
      const float mean = ((lane & 4) ? a1 : a0) * (1.f / D);
      const float var = ((lane & 4) ? q1 : q0) * (1.f / D) - mean * mean;
      const float rs = __builtin_amdgcn_rsqf(fmaxf(var, 0.f) + 1e-5f);
      float hid[4];
#pragma unroll
      for (int j = 0; j < 4; ++j) {
        const float zv = rs * (s[j] - mean * dpp<ROR8>(s[j]));
        const float up = dpp<XOR2>(zv);
        hid[j] = zv * __builtin_amdgcn_rcpf(1.f + ex2(-1.4426950408889634f * zv)) * up;
      }
      const u32 h0 = cvtpk(hid[0], hid[1]), h1 = cvtpk(hid[2], hid[3]);
      const v4u a = {h0, h1, dppu<XOR1>(h0), dppu<XOR1>(h1)};
      f4 acc[8];
#pragma unroll
      for (int j = 0; j < 8; ++j) acc[j] = mfma(a, f[j], f4{0, 0, 0, 0});
      ST128(nosent4(f4{acc[0][0], acc[1][0], acc[2][0], acc[3][0]}), mo, 0);
      ST128(nosent4(f4{acc[4][0], acc[5][0], acc[6][0], acc[7][0]}), mo, 18432);
      STAMP(7);
    }
  }
  STAMP_FLUSH();
}

extern "C" void kernel_launch(void* const* d_in, const int* in_sizes, int n_in, void* d_out, int out_size, void* d_ws, size_t ws_size,
                              hipStream_t stream) {
  fused_decode<<<256, 1024, 0, stream>>>((const u16*)d_in[0], (const u16*)d_in[1], (const u16*)d_in[2], (const u16*)d_in[3],
                                         (const u16*)d_in[4], (const u16*)d_in[6], (const u16*)d_in[7], (const u16*)d_in[8], (u16*)d_out,
                                         (char*)d_ws);
}
```

```cpp
#include <hip/hip_runtime.h>
#include <hip/hip_bf16.h>
#include <stdint.h>

#define GAS __attribute__((address_space(1)))
#define LAS __attribute__((address_space(3)))
#define CAS __attribute__((address_space(4)))
typedef unsigned u32;
typedef unsigned long long u64;
typedef unsigned short u16;
typedef u32 v4u __attribute__((ext_vector_type(4)));
typedef u32 v2u __attribute__((ext_vector_type(2)));
typedef float f4 __attribute__((ext_vector_type(4)));
typedef float f2 __attribute__((ext_vector_type(2)));
typedef __bf16 bf8 __attribute__((ext_vector_type(8)));
typedef __bf16 bf2 __attribute__((ext_vector_type(2)));
typedef __amdgpu_buffer_rsrc_t rsrc_t;

#define CODE_LINES 140
#define QSLEEP 30
#define TK0 45
#define DKV 165
#define TWO 580
#define TPF 655
#define DPF 24
#define TXB 655
#define DWO 24
#define TXO 700
#define TW2 830
#define KVWN 8
#define PWIN 4
#define YSLEEP 3
#define W2GATE 12
#define ZW 1
constexpr int D = 576, SK = 8192, WIN = 2048, FF = 4608, QD = 4096;
constexpr int WS_EPOCH = 0, WS_FLAGS = 4096, WS_ML = WS_FLAGS + 256 * 128, WS_OP = WS_ML + 256 * 128, WS_AP = WS_OP + 131072,
              WS_MP = WS_AP + 36864, WS_QG = WS_MP + 663552, WS_END = WS_QG + 262144;
constexpr int WS_STAMP = 2 << 20;
constexpr u32 SENT = 0xFFFFFFFFu;
constexpr int AUX_NT = 2, AUX_SC1 = 16, OOB = 1 << 30;
constexpr int SPIN_LDS = 1 << 18, SPIN_GLB = 1 << 15;
constexpr int L_FLAG = 0;
constexpr int L_Q = 256;
constexpr int L_P = 768;
constexpr int L_MX = 1792;
constexpr int L_ST = 1920;
constexpr int L_QP = 2304;
constexpr int L_O = 4096;
constexpr int L_R = 8192;
constexpr int L_DUMMY = L_R + 9216;
constexpr int L_STAMP = L_DUMMY + 2048;
constexpr int L_END = L_STAMP + 2048;
enum { F_QCNT = 0, F_QRDY, F_PCNT, F_OCNT, F_VISS, F_CLR, F_INOK, F_RCNT, F_PF, F_BI, F_TAG = 16 };

__device__ __forceinline__ rsrc_t mkrs(const void* p, int bytes) { return __builtin_amdgcn_make_buffer_rsrc((void*)p, 0, bytes, 0x00020000); }
__device__ __forceinline__ float asf(u32 u) { return __uint_as_float(u); }
__device__ __forceinline__ u32 asu(float f) { return __float_as_uint(f); }
__device__ __forceinline__ u32 cvtpk(float lo, float hi) { return __builtin_bit_cast(u32, __builtin_convertvector(f2{lo, hi}, bf2)); }
__device__ __forceinline__ float rbf(float f) { return asf(cvtpk(f, f) & 0xffff0000u); }
__device__ __forceinline__ float blo(u32 u) { return asf(u << 16); }
__device__ __forceinline__ float bhi(u32 u) { return asf(u & 0xffff0000u); }
__device__ __forceinline__ float ex2(float x) { return __builtin_amdgcn_exp2f(x); }
__device__ __forceinline__ float dot2(u32 a, u32 b, float c) { return __builtin_amdgcn_fdot2_f32_bf16(__builtin_bit_cast(bf2, a), __builtin_bit_cast(bf2, b), c, false); }
template <int C> __device__ __forceinline__ u32 dppu(u32 x) { return (u32)__builtin_amdgcn_mov_dpp((int)x, C, 0xf, 0xf, true); }
template <int C> __device__ __forceinline__ float dpp(float x) { return asf(dppu<C>(asu(x))); }
constexpr int XOR1 = 0xB1, XOR2 = 0x4E, HMIR = 0x141, ROR4 = 0x124, ROR8 = 0x128, SHL1 = 0x101;
__device__ __forceinline__ float x16_sum(float x) { auto s = __builtin_amdgcn_permlane16_swap(asu(x), asu(x), false, false); return asf(s[0]) + asf(s[1]); }
__device__ __forceinline__ float x32_sum(float x) { auto s = __builtin_amdgcn_permlane32_swap(asu(x), asu(x), false, false); return asf(s[0]) + asf(s[1]); }
__device__ __forceinline__ float x16_max(float x) { auto s = __builtin_amdgcn_permlane16_swap(asu(x), asu(x), false, false); return fmaxf(asf(s[0]), asf(s[1])); }
__device__ __forceinline__ float x32_max(float x) { auto s = __builtin_amdgcn_permlane32_swap(asu(x), asu(x), false, false); return fmaxf(asf(s[0]), asf(s[1])); }
__device__ __forceinline__ f4 mfma(v4u a, v4u b, f4 c) { return __builtin_amdgcn_mfma_f32_16x16x32_bf16(__builtin_bit_cast(bf8, a), __builtin_bit_cast(bf8, b), c, 0, 0, 0); }
__device__ __forceinline__ u32 plo(u32 a, u32 b) { return __builtin_amdgcn_perm(b, a, 0x05040100u); }
__device__ __forceinline__ u32 phi(u32 a, u32 b) { return __builtin_amdgcn_perm(b, a, 0x07060302u); }
__device__ __forceinline__ void tr8(const v4u* r, v4u* f) {
#pragma unroll
  for (int d = 0; d < 4; ++d)
#pragma unroll
    for (int e = 0; e < 4; ++e) { f[2 * e][d] = plo(r[2 * d][e], r[2 * d + 1][e]); f[2 * e + 1][d] = phi(r[2 * d][e], r[2 * d + 1][e]); }
}
__device__ __forceinline__ void tr4(const v2u* r, v4u* f) {
#pragma unroll
  for (int d = 0; d < 4; ++d)
#pragma unroll
    for (int e = 0; e < 2; ++e) { f[2 * e][d] = plo(r[2 * d][e], r[2 * d + 1][e]); f[2 * e + 1][d] = phi(r[2 * d][e], r[2 * d + 1][e]); }
}
__device__ __forceinline__ u32 umax(u32 a, u32 b) { return a > b ? a : b; }
__device__ __forceinline__ u32 nosent(u32 x) { return x < 0xFFFFFFFEu ? x : 0xFFFFFFFEu; }
__device__ __forceinline__ v4u nosent4(f4 v) { return v4u{nosent(asu(v[0])), nosent(asu(v[1])), nosent(asu(v[2])), nosent(asu(v[3]))}; }
__device__ __forceinline__ u32 mx4(v4u v, u32 m) { return umax(umax(umax(v[0], v[1]), m), umax(v[2], v[3])); }
__device__ __forceinline__ bool none_sent(u32 m) { return __builtin_amdgcn_ballot_w64(m == SENT) == 0; }
#define PIN4(f) asm volatile("" : "+v"((f)[0]), "+v"((f)[1]), "+v"((f)[2]), "+v"((f)[3]))
#define PIN8(f) asm volatile("" : "+v"(f[0]), "+v"(f[1]), "+v"(f[2]), "+v"(f[3]), "+v"(f[4]), "+v"(f[5]), "+v"(f[6]), "+v"(f[7]))
#define CBAR() asm volatile("" ::: "memory")
#define SCHED() __builtin_amdgcn_sched_barrier(0)
#define OPAQUE(x) asm volatile("" : "+s"(x))
template <int N> __device__ __forceinline__ void waitvm() { asm volatile("s_waitcnt vmcnt(%0)" ::"i"(N) : "memory"); }
#define NOUNROLL _Pragma("nounroll")
#define LD128(off, soff) __builtin_amdgcn_raw_buffer_load_b128(rws, off, soff, AUX_SC1)
#define LD64(off, soff) __builtin_amdgcn_raw_buffer_load_b64(rws, off, soff, AUX_SC1)
#define ST128(v, off, soff) __builtin_amdgcn_raw_buffer_store_b128(v, rws, off, soff, AUX_SC1)
#define ST64(v, off, soff) __builtin_amdgcn_raw_buffer_store_b64(v, rws, off, soff, AUX_SC1)
#define BARRIER() do { SCHED(); asm volatile("s_barrier" ::: "memory"); SCHED(); } while (0)

__device__ __forceinline__ u64 sent_mask(u32 m) { return __builtin_amdgcn_ballot_w64(m == SENT); }
#define RELD4(k, so) "s_mov_b64 exec, %[m" #k "]\n\tbuffer_load_dwordx4 %" #k ", %[off], %[rs], " so " offen sc1\n\t"
#define RELD_END "s_mov_b64 exec, -1\n\ts_waitcnt vmcnt(0)"

template <int F, int SLEEP> __device__ __forceinline__ void lds_wait(LAS u32* fl, u32 want) {
  CBAR();
  NOUNROLL for (int n = 0; n < SPIN_LDS; ++n) {
    if ((u32)__builtin_amdgcn_readfirstlane((int)__hip_atomic_load(fl + F, __ATOMIC_RELAXED, __HIP_MEMORY_SCOPE_WORKGROUP)) >= want) break;
    if (SLEEP) __builtin_amdgcn_s_sleep(SLEEP);
  }
  CBAR();
}
template <int F> __device__ __forceinline__ void lds_add(LAS u32* fl) {
  asm volatile("s_mov_b64 exec, 1\n\tds_add_u32 %0, %1 offset:%2\n\ts_mov_b64 exec, -1" ::"v"((u32)(size_t)fl), "v"(1u), "i"(4 * F) : "memory");
}
template <int F> __device__ __forceinline__ u32 lds_add_rtn(LAS u32* fl) {
  u32 old;
  asm volatile("s_mov_b64 exec, 1\n\tds_add_rtn_u32 %0, %1, %2 offset:%3\n\ts_mov_b64 exec, -1\n\ts_waitcnt lgkmcnt(0)"
               : "=v"(old) : "v"((u32)(size_t)fl), "v"(1u), "i"(4 * F) : "memory");
  return (u32)__builtin_amdgcn_readfirstlane((int)old);
}
template <int SLEEP> __device__ __forceinline__ void pace(u32 t0, u32 ticks) {
  NOUNROLL while ((u32)__builtin_amdgcn_s_memrealtime() - t0 < ticks) { if (SLEEP) __builtin_amdgcn_s_sleep(SLEEP); }
}
__device__ __forceinline__ int sel(bool c, int a, int b) { asm volatile("" : "+v"(a)); return c ? a : b; }

#define STAMP(k) do {} while (0)
#define STAMP_INIT() do {} while (0)
#define STAMP_FLUSH() do {} while (0)

__global__ void __launch_bounds__(1024) fused_decode(const u16* x_a, const u16* kc_a, const u16* vc_a, const u16* wq_a, const u16* wo_a,
                                                     const u16* w1_a, const u16* w3_a, const u16* w2_a, u16* out_a, char* ws_a) {
  __shared__ __attribute__((aligned(16))) char smem[L_END];
  LAS char* L = (LAS char*)smem;
  LAS u32* FL = (LAS u32*)(L + L_FLAG);
  const int bid = blockIdx.x, lane = threadIdx.x & 63, w = __builtin_amdgcn_readfirstlane(threadIdx.x >> 6);
  u32 cpf = 0;
  if (w == 3) {
    u64 pc0; asm volatile("s_getpc_b64 %0" : "=s"(pc0));
    int ln = (bid >> 3) + 32 * (lane & 7);
    ln = ln < CODE_LINES ? ln : 0;
    cpf = *(const GAS u32*)((const GAS char*)(pc0 & ~(u64)127) + ln * 128);
  }
  if (w == 0) {
    FL[lane] = 0;
    asm volatile("s_waitcnt lgkmcnt(0)" ::: "memory");
  }
  SCHED();
  const CAS u64* kargs = (const CAS u64*)__builtin_amdgcn_kernarg_segment_ptr();
  OPAQUE(kargs);
  const u16 *x = (const u16*)kargs[0], *kc = (const u16*)kargs[1], *vc = (const u16*)kargs[2], *wq = (const u16*)kargs[3],
            *wo = (const u16*)kargs[4], *w1 = (const u16*)kargs[5], *w3 = (const u16*)kargs[6], *w2 = (const u16*)kargs[7];
  u16* out = (u16*)kargs[8];
  char* ws = (char*)kargs[9];
  BARRIER();
  const u32 tstart = (u32)__builtin_amdgcn_s_memrealtime();
  STAMP_INIT();
  STAMP(0);
  const int xcd = bid & 7, ii = bid >> 3;
  const int kvh = 2 * xcd + (ii >> 4), jj = ii & 15, bb = jj >> 3;
  const bool isY = ii < 18, isX = ii >= 18 && ii < 27;
  const int yi = xcd * 18 + ii, xc = ii - 18;
  const int g4 = lane >> 4, r16 = lane & 15, hb = lane >> 5, l5 = lane & 31;
  const rsrc_t rws = mkrs(ws, WS_END);
  const rsrc_t rx = mkrs(x, 2 * D * 2);
  const int kw = w & 3;
  const int rot = bid * 5;
#define LOAD_TAG() (*(const CAS u64*)(ws + WS_EPOCH) + 1)
#define LDS_TAG() (*(LAS v2u*)(FL + F_TAG))

  int yt = -1;
  int v2 = -1;
  if (w < 3) {
    v4u xs[4], tq[8];
    const int k0 = w * 64 + (r16 >> 3) * 32 + g4 * 8, ka = w * 64 + (lane & 1) * 32 + g4 * 8;
    const int xo = l5 < 24 ? hb * (D * 2) + l5 * 16 : OOB;
#pragma unroll
    for (int i = 0; i < 3; ++i) xs[i] = __builtin_amdgcn_raw_buffer_load_b128(rx, xo + i * 384, 0, 0);
    xs[3] = __builtin_amdgcn_raw_buffer_load_b128(rx, sel(ka < 144, ((r16 >> 2) & 1) * (D * 2) + ka * 2, OOB), (jj & 3) * 288, 0);
    const rsrc_t rq = mkrs(wq, D * QD * 2);
    const int qo = sel(k0 < 144, k0 * (QD * 2) + (lane & 7) * 16, OOB) + (jj & 3) * (144 * QD * 2) + kvh * 512 + (jj >> 2) * 128;
#pragma unroll
    for (int i = 0; i < 8; ++i) tq[i] = __builtin_amdgcn_raw_buffer_load_b128(rq, qo, i * (QD * 2), AUX_NT);
    const u64 tag = LOAD_TAG();
    int qpo = sel((lane & 0x28) == 0, L_QP + w * 512 + g4 * 256 + r16 * 32, L_DUMMY + lane * 32);
    int qso = WS_QG + ((hb * 16 + kvh) * 4 + (jj & 3)) * 2048 + (jj >> 2) * 512 + l5 * 16;
    asm volatile("" : "+v"(qpo), "+v"(qso));
    SCHED();
    float s = 0, v = 0;
#pragma unroll
    for (int i = 0; i < 3; ++i)
#pragma unroll
      for (int e = 0; e < 4; ++e) { s = dot2(xs[i][e], 0x3F803F80u, s); v = dot2(xs[i][e], xs[i][e], v); }
    s += dpp<XOR1>(s); v += dpp<XOR1>(v); s += dpp<XOR2>(s); v += dpp<XOR2>(v);
    s += dpp<HMIR>(s); v += dpp<HMIR>(v); s += dpp<ROR8>(s); v += dpp<ROR8>(v);
    s = x16_sum(s); v = x16_sum(v);
    const float mean = s * (1.f / D);
    const float mb = rbf(mean);
    const float rd = __builtin_amdgcn_rcpf(rbf(__builtin_amdgcn_sqrtf(rbf(rbf(fmaxf(v * (1.f / D) - mean * mean, 0.f)) + 1e-5f))));
    const auto ms = __builtin_amdgcn_permlane32_swap(asu(mb), asu(mb), false, false);
    const auto rs = __builtin_amdgcn_permlane32_swap(asu(rd), asu(rd), false, false);
    const float mm = asf((lane & 4) ? ms[1] : ms[0]), rr = asf((lane & 4) ? rs[1] : rs[0]);
    v4u a;
#pragma unroll
    for (int e = 0; e < 4; ++e) { u32 c = cvtpk(blo(xs[3][e]) - mm, bhi(xs[3][e]) - mm); a[e] = cvtpk(blo(c) * rr, bhi(c) * rr); }
    asm volatile("" : "+v"(a));
    SCHED();
    STAMP(11);
    v4u f[8];
    tr8(tq, f);
    f4 acc[8];
#pragma unroll
    for (int j = 0; j < 8; ++j) acc[j] = mfma(a, f[j], f4{0, 0, 0, 0});
    float o[8];
#pragma unroll
    for (int j = 0; j < 8; ++j) o[j] = acc[j][0] + dpp<ROR8>(acc[j][1]);
    STAMP(1);
    *(LAS f4*)(L + qpo) = f4{o[0], o[1], o[2], o[3]};
    *(LAS f4*)(L + qpo + 16) = f4{o[4], o[5], o[6], o[7]};
    if (lds_add_rtn<F_QCNT>(FL) == 2) {
      const f2 q = (*(LAS f2*)(L + L_QP + lane * 8) + *(LAS f2*)(L + L_QP + 512 + lane * 8)) + *(LAS f2*)(L + L_QP + 1024 + lane * 8);
      const u32 th = (u32)(tag >> 8), tl = (u32)tag & 0xffu;
      ST128((v4u{((asu(q[0]) + 0x80u) & ~0xffu) | tl, th, ((asu(q[1]) + 0x80u) & ~0xffu) | tl, th}), qso, 0);
      lds_add<F_QCNT>(FL);
      STAMP(2);
      const int go = WS_QG + (bb * 16 + kvh) * 8192 + lane * 16;
      v4u g[8];
      __builtin_amdgcn_s_sleep(QSLEEP);
      NOUNROLL for (int n = 0; n < SPIN_GLB; ++n) {
#pragma unroll
        for (int k = 0; k < 8; ++k) g[k] = LD128(go, k * 1024);
        CBAR();
        u32 bad = 0, lo = 0;
#pragma unroll
        for (int k = 0; k < 8; ++k) { bad |= (g[k][1] ^ th) | (g[k][3] ^ th); lo |= (g[k][0] ^ tl) | (g[k][2] ^ tl); }
        bad |= lo & 0xffu;
        if (n == 0) STAMP(4);
        if (__builtin_amdgcn_ballot_w64(bad != 0) == 0) break;
        __builtin_amdgcn_s_sleep(2);
      }
#pragma unroll
      for (int c = 0; c < 2; ++c) {
        const float a0 = (asf(g[c][0] & ~0xffu) + asf(g[2 + c][0] & ~0xffu)) + (asf(g[4 + c][0] & ~0xffu) + asf(g[6 + c][0] & ~0xffu));
        const float a1 = (asf(g[c][2] & ~0xffu) + asf(g[2 + c][2] & ~0xffu)) + (asf(g[4 + c][2] & ~0xffu) + asf(g[6 + c][2] & ~0xffu));
        *(LAS u32*)(L + L_Q + c * 256 + lane * 4) = cvtpk(a0, a1);
      }
      lds_add<F_QRDY>(FL);
      STAMP(3);
    }
    yt = w + 3;
  }
  int w1_ = w;
  OPAQUE(w1_);
  v4u t[16];
  const rsrc_t rk = mkrs(w < 8 ? kc : vc, 2 * 16 * SK * 256);
  const int kvbase = ((bb * 16 + kvh) * SK + (SK - WIN) + (jj & 7) * 256 + kw * 64) * 256;
  if (w1_ >= 4 && w1_ < 8) {
    const int voff = (r16 >> 2) * 256 + (g4 * 4 + (lane & 3)) * 16 + kvbase;
    const u32 tk = tstart + kw * (DKV / 40);
#pragma unroll
    for (int i = 0; i < 16; ++i) {
      if (i >= KVWN) waitvm<KVWN - 1>();
      pace<0>(tk, TK0 + i * DKV / 10);
      if (i == 0) STAMP(10);
      if (i == 8) STAMP(11);
      t[i] = __builtin_amdgcn_raw_buffer_load_b128(rk, voff, ((i + rot) & 15) * 1024, AUX_NT);
      SCHED();
    }
    STAMP(1);
    lds_wait<F_QRDY, 1>(FL, 1);
    STAMP(2);
    v4u qf = *(LAS v4u*)(L + L_Q + ((r16 >> 2) & 1) * 256 + (g4 * 4 + (lane & 3)) * 16);
    const v4u z4 = {0, 0, 0, 0};
    const v4u q0 = r16 < 8 ? qf : z4, q1 = r16 < 8 ? z4 : qf;
    float sc[8];
#pragma unroll
    for (int m = 0; m < 8; ++m) {
      f4 d = mfma(t[m], q0, f4{0, 0, 0, 0});
      d = mfma(t[m + 8], q1, d);
      sc[m] = dpp<0xFF>(d[3]) + (dpp<0xAA>(d[2]) + (dpp<0x55>(d[1]) + dpp<0x00>(d[0])));
    }
    const bool b0 = lane & 1, b1 = lane & 2;
    float s0 = b1 ? (b0 ? sc[6] : sc[4]) : (b0 ? sc[2] : sc[0]);
    float s1 = b1 ? (b0 ? sc[7] : sc[5]) : (b0 ? sc[3] : sc[1]);
    u32 pk = cvtpk(s0, s1);
    pk = cvtpk(blo(pk) * 0.08838834764831845f, bhi(pk) * 0.08838834764831845f);
    s0 = blo(pk) * 1.4426950408889634f; s1 = bhi(pk) * 1.4426950408889634f;
    float mx = fmaxf(s0, s1);
    mx = fmaxf(mx, dpp<XOR1>(mx)); mx = fmaxf(mx, dpp<XOR2>(mx)); mx = fmaxf(mx, dpp<ROR8>(mx));
    mx = x32_max(x16_max(mx));
    const int hd = (r16 >> 2) & 1;
    LAS float* MX = (LAS float*)(L + L_MX);
    const float p0 = ex2(s0 - mx), p1 = ex2(s1 - mx);
    *(LAS u32*)(L + L_P + kw * 256 + hd * 128 + (r16 >> 3) * 64 + g4 * 16 + (lane & 3) * 4) = cvtpk(p0, p1);
    float ls = p0 + p1;
    ls += dpp<XOR1>(ls); ls += dpp<XOR2>(ls); ls += dpp<ROR8>(ls);
    ls = x32_sum(x16_sum(ls));
    MX[hd * 4 + kw] = mx; MX[8 + hd * 4 + kw] = ls;
    lds_add<F_PCNT>(FL);
    STAMP(3);
    if (isX && kw < ZW) {
      const int z = xcd * 9 + xc, zl = z >> 1, zb = zl / 18, zz = zl % 18;
      const int col = (zz >> 1) * 64 + (z & 1) * 32 + (lane & 3) * 8 + (zz & 1) * 4;
      const v2u xv = __builtin_amdgcn_raw_buffer_load_b64(rx, (zb * D + col) * 2, 0, 0);
      const int ho = WS_AP + ((zz >> 1) * 8 + ((lane >> 2) & 7)) * 512 + (lane & 3) * 128 + zb * 64 + (zz & 1) * 32 + (z & 1) * 16;
      int oo = sel(lane < 4, (zb * D + col) * 2, OOB);
      const int mo = WS_MP + zl * 18432 + (lane >> 2) * 128 + (z & 1) * 64 + (lane & 3) * 16;
      lds_wait<F_RCNT, 2>(FL, 5);
      v4u hv;
      NOUNROLL for (int n = 0; n < SPIN_GLB; ++n) {
        hv = LD128(ho, 0);
        CBAR();
        if (none_sent(mx4(hv, 0))) break;
        __builtin_amdgcn_s_sleep(4);
      }
      float h[4];
#pragma unroll
      for (int j = 0; j < 4; ++j) { float a = asf(hv[j]); a += dpp<ROR4>(a); a += dpp<ROR8>(a); h[j] = x16_sum(a); }
      h[0] = rbf(blo(xv[0]) + rbf(h[0])); h[1] = rbf(bhi(xv[0]) + rbf(h[1]));
      h[2] = rbf(blo(xv[1]) + rbf(h[2])); h[3] = rbf(bhi(xv[1]) + rbf(h[3]));
      PIN4(h);
      asm volatile("" : "+v"(oo));
      STAMP(12);
      for (int k = 0; k < kw; ++k) __builtin_amdgcn_s_sleep(2);
      v4u p[9];
      u64 m[9];
#pragma unroll
      for (int k = 0; k < 9; ++k) { m[k] = ~0ull; p[k] = v4u{0, 0, 0, 0}; }
      NOUNROLL for (int n = 0; n < SPIN_GLB; ++n) {
        asm volatile(RELD4(0, "0") RELD4(1, "%[s1]") RELD4(2, "%[s2]") RELD4(3, "%[s3]") RELD4(4, "%[s4]") RELD4(5, "%[s5]") RELD4(6, "%[s6]")
                     RELD4(7, "%[s7]") RELD4(8, "%[s8]") RELD_END
                     : "+v"(p[0]), "+v"(p[1]), "+v"(p[2]), "+v"(p[3]), "+v"(p[4]), "+v"(p[5]), "+v"(p[6]), "+v"(p[7]), "+v"(p[8])
                     : [off] "v"(mo), [rs] "s"(rws), [m0] "s"(m[0]), [m1] "s"(m[1]), [m2] "s"(m[2]), [m3] "s"(m[3]), [m4] "s"(m[4]), [m5] "s"(m[5]),
                       [m6] "s"(m[6]), [m7] "s"(m[7]), [m8] "s"(m[8]), [s1] "s"(2048), [s2] "s"(4096), [s3] "s"(6144), [s4] "s"(8192),
                       [s5] "s"(10240), [s6] "s"(12288), [s7] "s"(14336), [s8] "s"(16384)
                     : "memory");
        u64 any = 0, all = ~0ull;
#pragma unroll
        for (int k = 0; k < 9; ++k) { m[k] = sent_mask(mx4(p[k], 0)); any |= m[k]; all &= m[k]; }
        if (any == 0) break;
        if (all == ~0ull) __builtin_amdgcn_s_sleep(4);
      }
      STAMP(13);
      float r[4];
#pragma unroll
      for (int j = 0; j < 4; ++j) {
        float a = ((asf(p[0][j]) + asf(p[1][j])) + (asf(p[2][j]) + asf(p[3][j]))) + ((asf(p[4][j]) + asf(p[5][j])) + (asf(p[6][j]) + asf(p[7][j]))) + asf(p[8][j]);
        a += dpp<ROR4>(a); a += dpp<ROR8>(a);
        r[j] = h[j] + rbf(x32_sum(x16_sum(a)));
      }
      __builtin_amdgcn_raw_buffer_store_b64(v2u{cvtpk(r[0], r[1]), cvtpk(r[2], r[3])}, mkrs(out, 2 * D * 2), oo, 0, 0);
      if (z == 0) __builtin_amdgcn_raw_buffer_store_b64(LDS_TAG(), rws, lane == 0 ? WS_EPOCH : OOB, 0, 0);
      STAMP(14);
    }
    if (kw == 0) yt = 7;
    if (kw == 1) v2 = 4;
  }
  int w2_ = w;
  OPAQUE(w2_);
  if (w2_ >= 8 && w2_ < 12) {
    const int voff = lane * 16 + kvbase;
    const u32 tk = tstart + kw * (DKV / 40);
    pace<2>(tk, TK0 + 15 * DKV / 10);
#pragma unroll
    for (int i = 0; i < 16; ++i) {
      if (i >= KVWN) waitvm<KVWN - 1>();
      pace<0>(tk, TK0 + (16 + i) * DKV / 10);
      if (i == 0) STAMP(10);
      if (i == 8) STAMP(11);
      t[i] = __builtin_amdgcn_raw_buffer_load_b128(rk, voff, ((i + rot) & 15) * 1024, AUX_NT);
      SCHED();
    }
    lds_add<F_VISS>(FL);
    STAMP(1);
    v4u f[8];
    tr8(t, f);
    PIN8(f);
    lds_wait<F_PCNT, 1>(FL, 4);
    STAMP(2);
    const v4u pa = *(LAS v4u*)(L + L_P + kw * 256 + ((r16 >> 2) & 1) * 128 + g4 * 16);
    const v4u pb = *(LAS v4u*)(L + L_P + kw * 256 + ((r16 >> 2) & 1) * 128 + 64 + g4 * 16);
    const f4 m4 = *(LAS f4*)(L + L_MX + (g4 & 1) * 16), l4 = *(LAS f4*)(L + L_MX + 32 + (g4 & 1) * 16);
    const float mw = *(LAS float*)(L + L_MX + (g4 & 1) * 16 + kw * 4);
    f4 acc[8];
#pragma unroll
    for (int j = 0; j < 8; ++j) acc[j] = mfma(pa, f[j], f4{0, 0, 0, 0});
    const float M = fmaxf(fmaxf(m4[0], m4[1]), fmaxf(m4[2], m4[3]));
    float fs = ex2(mw - M);
    v2u ml = {nosent(asu(M)), nosent(asu((l4[0] * ex2(m4[0] - M) + l4[1] * ex2(m4[1] - M)) + (l4[2] * ex2(m4[2] - M) + l4[3] * ex2(m4[3] - M))))};
    int mlo = sel((lane & 0x2F) == 0, WS_ML + (kvh * 16 + jj) * 128 + g4 * 8, OOB);
    int opo = WS_OP + ((2 * kvh + hb) * 16 + jj) * 256 + l5 * 8;
    int lo = L_O + kw * 1024 + (g4 & 1) * 512 + r16 * 32 + (g4 >> 1) * 16;
    asm volatile("" : "+v"(fs), "+v"(ml), "+v"(mlo), "+v"(opo), "+v"(lo));
    lds_wait<F_CLR, 1>(FL, 1);
    SCHED();
    tr8(t + 8, f);
#pragma unroll
    for (int j = 0; j < 8; ++j) acc[j] = mfma(pb, f[j], acc[j]);
    STAMP(3);
    {
      const bool hi = g4 >= 2;
      f4 o = {hi ? acc[4][0] : acc[0][0], hi ? acc[5][0] : acc[1][0], hi ? acc[6][0] : acc[2][0], hi ? acc[7][0] : acc[3][0]};
      *(LAS f4*)(L + lo) = o * fs;
    }
    if (lds_add_rtn<F_OCNT>(FL) == 3) {
      ST64(ml, mlo, 0);
      f4 o = (*(LAS f4*)(L + L_O + lane * 16) + *(LAS f4*)(L + L_O + 1024 + lane * 16)) +
             (*(LAS f4*)(L + L_O + 2048 + lane * 16) + *(LAS f4*)(L + L_O + 3072 + lane * 16));
      ST64((v2u{nosent(cvtpk(o[0], o[1])), nosent(cvtpk(o[2], o[3]))}), opo, 0);
      lds_add<F_OCNT>(FL);
    }
    STAMP(4);
    v2 = kw;
  }
  int w3_ = w;
  OPAQUE(w3_);
  if (w3_ == 3) {
    const u64 tag = LOAD_TAG();
    const v4u s4 = {SENT, SENT, SENT, SENT};
    ST64((v2u{SENT, SENT}), WS_OP + ((2 * kvh + hb) * 16 + jj) * 256 + l5 * 8, 0);
    ST64((v2u{SENT, SENT}), sel(lane < 16, WS_ML + (kvh * 16 + jj) * 128 + lane * 8, OOB), 0);
    if (ii == 31) {
      const int ao = WS_AP + (hb * 8 + xcd) * 512 + l5 * 16;
#pragma unroll
      for (int n = 0; n < 4; ++n) ST128(s4, ao, n * 8192);
      ST128(s4, lane < 32 ? ao : OOB, 4 * 8192);
    }
    STAMP(1);
    asm volatile("s_waitcnt vmcnt(0)" ::"v"(cpf) : "memory");
    const v2u tg = {(u32)tag, (u32)(tag >> 32)};
    lds_wait<F_QCNT, 1>(FL, 4);
    ST64(tg, lane == 0 ? WS_FLAGS + bid * 128 : OOB, 0);
    if (isY) {
      const int mo = WS_MP + (lane >> 3) * 18432 + yi * 128 + (lane & 7) * 16;
#pragma unroll
      for (int n = 0; n < 4; ++n) ST128(s4, mo, n * 8 * 18432);
      ST128(s4, lane < 32 ? mo : OOB, 4 * 8 * 18432);
      waitvm<0>();
    }
    *(LAS v2u*)(FL + F_TAG) = tg;
    lds_add<F_CLR>(FL);
    STAMP(2);
    if (ii < 27) {
      const bool act = lane < 32 ? isX : lane < 40;
      const int fo = sel(act, WS_FLAGS + (lane < 32 ? lane * 8 + xcd : 248 + lane - 32) * 128, OOB);
      lds_wait<F_QRDY, 4>(FL, 1);
      NOUNROLL for (int n = 0; n < SPIN_GLB; ++n) {
        const v2u g = LD64(fo, 0);
        CBAR();
        if (__builtin_amdgcn_ballot_w64(act && ((g[0] ^ tg[0]) | (g[1] ^ tg[1])) != 0) == 0) break;
        __builtin_amdgcn_s_sleep(12);
      }
      lds_add<F_INOK>(FL);
      STAMP(3);
    }
    if (!isY) {
      const rsrc_t rp = mkrs(w2, (xcd + 1) * (576 * D * 2));
      const int po = lane * 128 + xcd * (576 * D * 2) + (31 - ii) * 8192;
      if (isX) lds_wait<F_RCNT, 2>(FL, 5); else lds_wait<F_BI, 2>(FL, 1);
      pace<1>(tstart, TW2);
      STAMP(4);
      u32 pf[6];
#pragma unroll
      for (int k = 0; k < 6; ++k) {
        if (k >= 4) waitvm<3>();
        pf[k] = __builtin_amdgcn_raw_buffer_load_b32(rp, po, k * 14 * 8192, 0);
        SCHED();
      }
#pragma unroll
      for (int k = 0; k < 6; ++k) asm volatile("" ::"v"(pf[k]));
      STAMP(5);
    }
    yt = 6;
  } else if (w3_ >= 12) {
    if (isX) {
      const int hh = 4 * xcd + kw;
      const rsrc_t ro = mkrs(wo, QD * D * 2);
      const int vo = (hh * 128 + g4 * 16) * (D * 2) + (xc * 64 + r16 * 4) * 2;
      int xo = L_R + kw * 512 + ((r16 >> 1) & 3) * 128 + (g4 >> 1) * 64 + (r16 & 1) * 32 + (r16 >> 3) * 16;
      int po = sel(lane < 32, WS_AP + (xc * 8 + xcd) * 512 + lane * 16, OOB);
      const int ao = WS_OP + (hh * 16 + r16) * 256 + g4 * 32;
      const int mo = WS_ML + ((hh >> 1) * 16 + (g4 >> 1) * 8 + ((r16 + 4 * (g4 & 1)) & 7)) * 128 + (hh & 1) * 8;
      asm volatile("" : "+v"(xo), "+v"(po));
      lds_wait<F_INOK, 2>(FL, 1);
      lds_wait<F_VISS, 1>(FL, 4);
      pace<1>(tstart, TXB + kw * DWO);
      STAMP(1);
      v2u u[32];
#pragma unroll
      for (int i = 0; i < 32; ++i) u[i] = __builtin_amdgcn_raw_buffer_load_b64(ro, vo, ((i >> 4) * 64 + (i & 15)) * (D * 2), AUX_NT);
      SCHED();
      STAMP(4);
      v4u f[16];
      v4u a[4];
      v2u ml;
      if (kw == 3) {
        a[0] = LD128(ao, 0); a[1] = LD128(ao + 16, 0); a[2] = LD128(ao + 128, 0); a[3] = LD128(ao + 144, 0);
        ml = LD64(mo, 0);
      } else {
        a[0] = a[1] = a[2] = a[3] = v4u{SENT, SENT, SENT, SENT};
        ml = v2u{SENT, SENT};
      }
      SCHED();
#pragma unroll
      for (int k = 0; k < 4; ++k) { tr4(u + 8 * k, f + 4 * k); PIN4(f + 4 * k); }
      STAMP(5);
      pace<0>(tstart, TXO);
      NOUNROLL for (int n = 0; n < SPIN_GLB; ++n) {
        const u64 ma = sent_mask(mx4(a[3], mx4(a[2], mx4(a[1], mx4(a[0], 0)))));
        const u64 mm = sent_mask(umax(ml[0], ml[1]));
        if ((ma | mm) == 0) break;
        asm volatile("s_mov_b64 exec, %[mm]\n\tbuffer_load_dwordx2 %4, %[mo], %[rs], 0 offen sc1\n\t"
                     "s_mov_b64 exec, %[ma]\n\tbuffer_load_dwordx4 %0, %[off], %[rs], 0 offen sc1\n\t"
                     "buffer_load_dwordx4 %1, %[off], %[rs], 0 offen offset:16 sc1\n\t"
                     "buffer_load_dwordx4 %2, %[off], %[rs], 0 offen offset:128 sc1\n\t"
                     "buffer_load_dwordx4 %3, %[off], %[rs], 0 offen offset:144 sc1\n\t" RELD_END
                     : "+v"(a[0]), "+v"(a[1]), "+v"(a[2]), "+v"(a[3]), "+v"(ml)
                     : [off] "v"(ao), [mo] "v"(mo), [rs] "s"(rws), [ma] "s"(ma), [mm] "s"(mm)
                     : "memory");
      }
      STAMP(6);
      f4 d[4];
#pragma unroll
      for (int j = 0; j < 4; ++j) d[j] = mfma(a[0], f[j], f4{0, 0, 0, 0});
#pragma unroll
      for (int k = 1; k < 4; ++k)
#pragma unroll
        for (int j = 0; j < 4; ++j) d[j] = mfma(a[k], f[4 * k + j], d[j]);
      float M = asf(ml[0]);
      M = fmaxf(M, dpp<XOR1>(M)); M = fmaxf(M, dpp<XOR2>(M)); M = fmaxf(M, dpp<HMIR>(M));
      const float ee = ex2(asf(ml[0]) - M);
      float Ls = ee * asf(ml[1]);
      Ls += dpp<XOR1>(Ls); Ls += dpp<XOR2>(Ls); Ls += dpp<HMIR>(Ls);
      const float wr = ee * __builtin_amdgcn_rcpf(Ls);
      const float e[4] = {dpp<0x150>(wr), dpp<0x151>(wr), dpp<0x152>(wr), dpp<0x153>(wr)};
      f4 o;
#pragma unroll
      for (int j = 0; j < 4; ++j) o[j] = x16_sum((e[0] * d[j][0] + e[1] * d[j][1]) + (e[2] * d[j][2] + e[3] * d[j][3]));
      *(LAS f4*)(L + xo) = o;
      if (lds_add_rtn<F_RCNT>(FL) == 3) {
        LAS char* rp = L + L_R + l5 * 16;
        const f4 s = (*(LAS f4*)(rp) + *(LAS f4*)(rp + 512)) + (*(LAS f4*)(rp + 1024) + *(LAS f4*)(rp + 1536));
        ST128(nosent4(s), po, 0);
        lds_add<F_RCNT>(FL);
      }
      STAMP(12);
    } else if (w == 15) {
      const int pi = isY ? ii : ii - 9;
      const int br = lane / 9;
      const int band = sel(lane < 63, br * (FF * 2) + (lane - br * 9) * 128, OOB) + xcd * 1152;
      const rsrc_t ro = mkrs(wo, (xcd + 1) * (512 * D * 2));
      const int oo = lane * 128 + xcd * (512 * D * 2) + (22 - pi) * 8192;
      u32 pf[12];
      pace<2>(tstart, TWO);
      STAMP(1);
#pragma unroll
      for (int k = 0; k < 4; ++k) {
        pace<0>(tstart, TWO + k * DWO);
        pf[k] = __builtin_amdgcn_raw_buffer_load_b32(ro, oo, k * 23 * 8192, 0);
        SCHED();
      }
      lds_wait<F_VISS, 1>(FL, 4);
      STAMP(4);
#pragma unroll
      for (int k = 0; k < 8; ++k) {
        const int m = 22 - pi + 23 * k;
        waitvm<PWIN - 1>();
        pace<0>(tstart, TPF + k * DPF);
        pf[4 + k] = __builtin_amdgcn_raw_buffer_load_b32(mkrs((m & 1) ? w3 : w1, D * FF * 2), band, (m >> 1) * (7 * FF * 2), 0);
        SCHED();
      }
      STAMP(2);
      if (!isY) lds_add<F_BI>(FL);
#pragma unroll
      for (int k = 0; k < 12; ++k) asm volatile("" ::"v"(pf[k]));
      lds_wait<F_OCNT, 1>(FL, 5);
      lds_add<F_PF>(FL);
      STAMP(3);
    }
    yt = w == 15 ? 8 : w - 12;
  }
  if (isY) {
    if (yt >= 0) {
      const GAS char* wp = (const GAS char*)((lane & 4) ? w3 : w1) + (size_t)((yt * 64 + (r16 >> 3) * 32 + g4 * 8) * (FF * 2) + (yi * 32 + (lane & 3) * 8) * 2);
      const int ao = WS_AP + (yt * 8 + (r16 >> 3)) * 512 + g4 * 128 + (lane & 7) * 16;
      int ypo = sel(r16 < 8, L_R + yt * 1024 + ((lane & 3) * 16 + g4 * 4 + (r16 >> 2) * 2) * 16, L_DUMMY + lane * 32);
      int sto = sel(lane == 0 || lane == 8 || lane == 20 || lane == 24, L_ST + ((r16 == 8 ? 0 : 2) + g4) * 64 + yt * 4, L_DUMMY + lane * 32);
      asm volatile("" : "+v"(ypo), "+v"(sto), "+v"(wp));
      lds_wait<F_INOK, 2>(FL, 1);
      lds_wait<F_PF, 1>(FL, 1);
#pragma unroll
      for (int i = 0; i < 8; ++i) t[i] = __builtin_nontemporal_load((const GAS v4u*)(wp + i * (FF * 2)));
      const v2u xv = __builtin_amdgcn_raw_buffer_load_b64(rx, (((lane >> 2) & 1) * D + yt * 64 + (lane & 1) * 32 + g4 * 8 + ((lane >> 1) & 1) * 4) * 2, 0, 0);
      SCHED();
      lds_add<F_BI>(FL);
      STAMP(13);
      v4u f[8];
      tr8(t, f);
      PIN8(f);
      STAMP(14);
      lds_wait<F_BI, 1>(FL, 9);
      v4u p[4] = {};
      u64 m[4] = {~0ull, ~0ull, ~0ull, ~0ull};
      NOUNROLL for (int n = 0; n < SPIN_GLB; ++n) {
        asm volatile("s_mov_b64 exec, %[m0]\n\tbuffer_load_dwordx4 %0, %[off], %[rs], 0 offen sc1\n\t"
                     "s_mov_b64 exec, %[m1]\n\tbuffer_load_dwordx4 %1, %[off], %[rs], 0 offen offset:1024 sc1\n\t"
                     "s_mov_b64 exec, %[m2]\n\tbuffer_load_dwordx4 %2, %[off], %[rs], 0 offen offset:2048 sc1\n\t"
                     "s_mov_b64 exec, %[m3]\n\tbuffer_load_dwordx4 %3, %[off], %[rs], 0 offen offset:3072 sc1\n\t" RELD_END
                     : "+v"(p[0]), "+v"(p[1]), "+v"(p[2]), "+v"(p[3])
                     : [off] "v"(ao), [rs] "s"(rws), [m0] "s"(m[0]), [m1] "s"(m[1]), [m2] "s"(m[2]), [m3] "s"(m[3])
                     : "memory");
        if (n == 0) lds_add<F_BI>(FL);
#pragma unroll
        for (int k = 0; k < 4; ++k) m[k] = sent_mask(mx4(p[k], 0));
        if ((m[0] | m[1] | m[2] | m[3]) == 0) break;
        if ((m[0] & m[1] & m[2] & m[3]) == ~0ull) __builtin_amdgcn_s_sleep(YSLEEP);
      }
      STAMP(15);
      float h[4];
#pragma unroll
      for (int j = 0; j < 4; ++j) { float a = (asf(p[0][j]) + asf(p[1][j])) + (asf(p[2][j]) + asf(p[3][j])); h[j] = a + dpp<ROR8>(a); }
      const u32 c0 = cvtpk(h[0], h[1]), c1 = cvtpk(h[2], h[3]);
      const u32 h0 = cvtpk(blo(xv[0]) + blo(c0), bhi(xv[0]) + bhi(c0)), h1 = cvtpk(blo(xv[1]) + blo(c1), bhi(xv[1]) + bhi(c1));
      const u32 h2 = dppu<XOR2>(h0), h3 = dppu<XOR2>(h1);
      const bool one = lane & 8;
      const v4u a = {one ? 0x3F803F80u : h0, one ? 0x3F803F80u : h1, one ? 0x3F803F80u : h2, one ? 0x3F803F80u : h3};
      f4 acc[8];
#pragma unroll
      for (int j = 0; j < 8; ++j) acc[j] = mfma(a, f[j], f4{0, 0, 0, 0});
      const f4 gm = mfma(a, a, f4{0, 0, 0, 0});
      float o[8];
#pragma unroll
      for (int j = 0; j < 8; ++j) o[j] = acc[j][0] + dpp<ROR8>(acc[j][1]);
      *(LAS f4*)(L + ypo) = f4{o[0], o[1], o[2], o[3]};
      *(LAS f4*)(L + ypo + 16) = f4{o[4], o[5], o[6], o[7]};
      *(LAS float*)(L + sto) = gm[0] + dpp<SHL1>(gm[1]);
      lds_add<F_RCNT>(FL);
      STAMP(7);
    } else if (v2 >= 0) {
      const rsrc_t r2 = mkrs(w2, FF * D * 2);
      const int vo = sel(v2 < 4 || r16 < 8, (yi * 32 + g4 * 8) * (D * 2) + (v2 * 128 + r16 * 8) * 2, OOB);
      int mo = sel(g4 < 2 && (v2 < 4 || r16 < 8), WS_MP + (g4 * 18 + v2 * 4 + (r16 >> 3) * 2) * 18432 + yi * 128 + (lane & 7) * 16, OOB);
      asm volatile("" : "+v"(mo));
      lds_wait<F_BI, 1>(FL, W2GATE);
#pragma unroll
      for (int i = 0; i < 8; ++i) t[i] = __builtin_amdgcn_raw_buffer_load_b128(r2, vo, i * (D * 2), AUX_NT);
      SCHED();
      STAMP(13);
      v4u f[8];
      tr8(t, f);
      PIN8(f);
      STAMP(14);
      lds_wait<F_RCNT, 0>(FL, 9);
      STAMP(15);
      float st = *(LAS float*)(L + L_ST + lane * 4);
      asm volatile("" : "+v"(st));
      st = r16 < 9 ? st : 0.f;
      f4 s = *(LAS f4*)(L + L_R + lane * 16);
#pragma unroll
      for (int k = 1; k < 9; ++k) s += *(LAS f4*)(L + L_R + k * 1024 + lane * 16);
      st += dpp<XOR1>(st); st += dpp<XOR2>(st); st += dpp<HMIR>(st); st += dpp<ROR8>(st);
      const float a0 = asf(__builtin_amdgcn_readlane(asu(st), 0)), a1 = asf(__builtin_amdgcn_readlane(asu(st), 16));
      const float q0 = asf(__builtin_amdgcn_readlane(asu(st), 32)), q1 = asf(__builtin_amdgcn_readlane(asu(st), 48));
      const float mean = ((lane & 4) ? a1 : a0) * (1.f / D);
      const float var = ((lane & 4) ? q1 : q0) * (1.f / D) - mean * mean;
      const float rs = __builtin_amdgcn_rsqf(fmaxf(var, 0.f) + 1e-5f);
      float hid[4];
#pragma unroll
      for (int j = 0; j < 4; ++j) {
        const float zv = rs * (s[j] - mean * dpp<ROR8>(s[j]));
        const float up = dpp<XOR2>(zv);
        hid[j] = zv * __builtin_amdgcn_rcpf(1.f + ex2(-1.4426950408889634f * zv)) * up;
      }
      const u32 h0 = cvtpk(hid[0], hid[1]), h1 = cvtpk(hid[2], hid[3]);
      const v4u a = {h0, h1, dppu<XOR1>(h0), dppu<XOR1>(h1)};
      f4 acc[8];
#pragma unroll
      for (int j = 0; j < 8; ++j) acc[j] = mfma(a, f[j], f4{0, 0, 0, 0});
      ST128(nosent4(f4{acc[0][0], acc[1][0], acc[2][0], acc[3][0]}), mo, 0);
      ST128(nosent4(f4{acc[4][0], acc[5][0], acc[6][0], acc[7][0]}), mo, 18432);
      STAMP(7);
    }
  }
  STAMP_FLUSH();
}

extern "C" void kernel_launch(void* const* d_in, const int* in_sizes, int n_in, void* d_out, int out_size, void* d_ws, size_t ws_size,
                              hipStream_t stream) {
  fused_decode<<<256, 1024, 0, stream>>>((const u16*)d_in[0], (const u16*)d_in[1], (const u16*)d_in[2], (const u16*)d_in[3],
                                         (const u16*)d_in[4], (const u16*)d_in[6], (const u16*)d_in[7], (const u16*)d_in[8], (u16*)d_out,
                                         (char*)d_ws);
}
```

```cpp
#include <hip/hip_runtime.h>
#include <hip/hip_bf16.h>
#include <stdint.h>

#define GAS __attribute__((address_space(1)))
#define LAS __attribute__((address_space(3)))
#define CAS __attribute__((address_space(4)))
typedef unsigned u32;
typedef unsigned long long u64;
typedef unsigned short u16;
typedef u32 v4u __attribute__((ext_vector_type(4)));
typedef u32 v2u __attribute__((ext_vector_type(2)));
typedef float f4 __attribute__((ext_vector_type(4)));
typedef float f2 __attribute__((ext_vector_type(2)));
typedef __bf16 bf8 __attribute__((ext_vector_type(8)));
typedef __bf16 bf2 __attribute__((ext_vector_type(2)));
typedef __amdgpu_buffer_rsrc_t rsrc_t;

#define TK0 45
#define DKV 132
#define TWO 580
#define DWO 24
#define TPF 655
#define DPF 24
#define TXB 640
#define DXB 21
#define TXO 700
#define TW2 830
#define W2GATE 9
#define TAP 915
#define APGATE 9
#define TQ 290
#define TFL 400
#define TMP 230
#define DMP 0
#define PS 0
#define PFLEFT 4
#define YHOLD 9
#define RIDEW 0
#define TOFF -10
#define QS 50
#define CODE_LINES 156
#define SELFWAIT 1
#define KVWN 8
#define PWIN 4
#define YSLEEP 3
constexpr int D = 576, SK = 8192, WIN = 2048, FF = 4608, QD = 4096;
constexpr int WS_EPOCH = 0, WS_FLAGS = 4096, WS_ML = WS_FLAGS + 256 * 128, WS_OP = WS_ML + 256 * 128, WS_AP = WS_OP + 131072,
              WS_MP = WS_AP + 36864, WS_QG = WS_MP + 663552, WS_END = WS_QG + 262144;
constexpr int WS_STAMP = 2 << 20;
constexpr u32 SENT = 0xFFFFFFFFu;
constexpr int AUX_NT = 2, AUX_SC1 = 16, OOB = 1 << 30;
constexpr int SPIN_LDS = 1 << 18, SPIN_GLB = 1 << 15;
constexpr int L_FLAG = 0;
constexpr int L_Q = 256;
constexpr int L_P = 768;
constexpr int L_MX = 1792;
constexpr int L_ST = 1920;
constexpr int L_QP = 2304;
constexpr int L_O = 4096;
constexpr int L_X = 8192;
constexpr int L_R = 10240;
constexpr int L_DUMMY = L_R + 9216;
constexpr int L_STAMP = L_DUMMY + 2048;
constexpr int L_END = L_STAMP + 2048;
enum { F_QCNT = 0, F_QRDY, F_PCNT, F_OCNT, F_VISS, F_CLR, F_PF, F_RCNT, F_XCNT, F_ZERO, F_BI, F_TAG = 16 };

__device__ __forceinline__ rsrc_t mkrs(const void* p, int bytes) { return __builtin_amdgcn_make_buffer_rsrc((void*)p, 0, bytes, 0x00020000); }
__device__ __forceinline__ float asf(u32 u) { return __uint_as_float(u); }
__device__ __forceinline__ u32 asu(float f) { return __float_as_uint(f); }
__device__ __forceinline__ u32 cvtpk(float lo, float hi) { return __builtin_bit_cast(u32, __builtin_convertvector(f2{lo, hi}, bf2)); }
__device__ __forceinline__ float rbf(float f) { return asf(cvtpk(f, f) & 0xffff0000u); }
__device__ __forceinline__ float blo(u32 u) { return asf(u << 16); }
__device__ __forceinline__ float bhi(u32 u) { return asf(u & 0xffff0000u); }
__device__ __forceinline__ float ex2(float x) { return __builtin_amdgcn_exp2f(x); }
__device__ __forceinline__ float dot2(u32 a, u32 b, float c) { return __builtin_amdgcn_fdot2_f32_bf16(__builtin_bit_cast(bf2, a), __builtin_bit_cast(bf2, b), c, false); }
template <int C> __device__ __forceinline__ u32 dppu(u32 x) { return (u32)__builtin_amdgcn_mov_dpp((int)x, C, 0xf, 0xf, true); }
template <int C> __device__ __forceinline__ float dpp(float x) { return asf(dppu<C>(asu(x))); }
constexpr int XOR1 = 0xB1, XOR2 = 0x4E, HMIR = 0x141, ROR4 = 0x124, ROR8 = 0x128, SHL1 = 0x101;
__device__ __forceinline__ float x16_sum(float x) { auto s = __builtin_amdgcn_permlane16_swap(asu(x), asu(x), false, false); return asf(s[0]) + asf(s[1]); }
__device__ __forceinline__ float x32_sum(float x) { auto s = __builtin_amdgcn_permlane32_swap(asu(x), asu(x), false, false); return asf(s[0]) + asf(s[1]); }
__device__ __forceinline__ float x16_max(float x) { auto s = __builtin_amdgcn_permlane16_swap(asu(x), asu(x), false, false); return fmaxf(asf(s[0]), asf(s[1])); }
__device__ __forceinline__ float x32_max(float x) { auto s = __builtin_amdgcn_permlane32_swap(asu(x), asu(x), false, false); return fmaxf(asf(s[0]), asf(s[1])); }
__device__ __forceinline__ float t32_sum(float a, float b) { auto s = __builtin_amdgcn_permlane32_swap(asu(a), asu(b), false, false); return asf(s[0]) + asf(s[1]); }
__device__ __forceinline__ float t16_sum(float a, float b) { auto s = __builtin_amdgcn_permlane16_swap(asu(a), asu(b), false, false); return asf(s[0]) + asf(s[1]); }
__device__ __forceinline__ f4 asf4(v4u v) { return __builtin_bit_cast(f4, v); }
__device__ __forceinline__ bool any_nan(f4 v) { return __builtin_amdgcn_ballot_w64(__builtin_isunordered(v[0], v[1]) || __builtin_isunordered(v[2], v[3])) != 0; }
__device__ __forceinline__ f4 mfma(v4u a, v4u b, f4 c) { return __builtin_amdgcn_mfma_f32_16x16x32_bf16(__builtin_bit_cast(bf8, a), __builtin_bit_cast(bf8, b), c, 0, 0, 0); }
__device__ __forceinline__ u32 plo(u32 a, u32 b) { return __builtin_amdgcn_perm(b, a, 0x05040100u); }
__device__ __forceinline__ u32 phi(u32 a, u32 b) { return __builtin_amdgcn_perm(b, a, 0x07060302u); }
__device__ __forceinline__ void tr8(const v4u* r, v4u* f) {
#pragma unroll
  for (int d = 0; d < 4; ++d)
#pragma unroll
    for (int e = 0; e < 4; ++e) { f[2 * e][d] = plo(r[2 * d][e], r[2 * d + 1][e]); f[2 * e + 1][d] = phi(r[2 * d][e], r[2 * d + 1][e]); }
}
__device__ __forceinline__ void tr4(const v2u* r, v4u* f) {
#pragma unroll
  for (int d = 0; d < 4; ++d)
#pragma unroll
    for (int e = 0; e < 2; ++e) { f[2 * e][d] = plo(r[2 * d][e], r[2 * d + 1][e]); f[2 * e + 1][d] = phi(r[2 * d][e], r[2 * d + 1][e]); }
}

#define CND(d, o, k, P) "v_cndmask_b32_dpp %" #d ", %" #o ", %" #k ", vcc quad_perm:" P " row_mask:0xf bank_mask:0xf\n\t"
template <int X> __device__ __forceinline__ void quad_stage(const v4u& lo, const v4u& hi, v4u& nlo, v4u& nhi) {
  u32 l0, l1, l2, l3, h0, h1, h2, h3;
  if (X == 1)
    asm volatile("s_nop 1\n\ts_mov_b64 vcc, %16\n\t" CND(0, 12, 8, "[1,0,3,2]") CND(1, 13, 9, "[1,0,3,2]") CND(2, 14, 10, "[1,0,3,2]") CND(3, 15, 11, "[1,0,3,2]")
                 "s_not_b64 vcc, vcc\n\t" CND(4, 8, 12, "[1,0,3,2]") CND(5, 9, 13, "[1,0,3,2]") CND(6, 10, 14, "[1,0,3,2]") CND(7, 11, 15, "[1,0,3,2]") "s_nop 1"
                 : "=&v"(l0), "=&v"(l1), "=&v"(l2), "=&v"(l3), "=&v"(h0), "=&v"(h1), "=&v"(h2), "=&v"(h3)
                 : "v"(lo[0]), "v"(lo[1]), "v"(lo[2]), "v"(lo[3]), "v"(hi[0]), "v"(hi[1]), "v"(hi[2]), "v"(hi[3]), "s"(0x5555555555555555ull) : "vcc", "scc");
  else
    asm volatile("s_nop 1\n\ts_mov_b64 vcc, %16\n\t" CND(0, 12, 8, "[2,3,0,1]") CND(1, 13, 9, "[2,3,0,1]") CND(2, 14, 10, "[2,3,0,1]") CND(3, 15, 11, "[2,3,0,1]")
                 "s_not_b64 vcc, vcc\n\t" CND(4, 8, 12, "[2,3,0,1]") CND(5, 9, 13, "[2,3,0,1]") CND(6, 10, 14, "[2,3,0,1]") CND(7, 11, 15, "[2,3,0,1]") "s_nop 1"
                 : "=&v"(l0), "=&v"(l1), "=&v"(l2), "=&v"(l3), "=&v"(h0), "=&v"(h1), "=&v"(h2), "=&v"(h3)
                 : "v"(lo[0]), "v"(lo[1]), "v"(lo[2]), "v"(lo[3]), "v"(hi[0]), "v"(hi[1]), "v"(hi[2]), "v"(hi[3]), "s"(0x3333333333333333ull) : "vcc", "scc");
  nlo = v4u{l0, l1, l2, l3}; nhi = v4u{h0, h1, h2, h3};
}
__device__ __forceinline__ void quad_tr(v4u* a) {
  v4u b[4];
  quad_stage<1>(a[0], a[1], b[0], b[1]); quad_stage<1>(a[2], a[3], b[2], b[3]);
  quad_stage<2>(b[0], b[2], a[0], a[2]); quad_stage<2>(b[1], b[3], a[1], a[3]);
}
__device__ __forceinline__ u32 umax(u32 a, u32 b) { return a > b ? a : b; }
__device__ __forceinline__ u32 nosent(u32 x) { return x < 0xFFFFFFFEu ? x : 0xFFFFFFFEu; }
__device__ __forceinline__ v4u nosent4(f4 v) { return v4u{nosent(asu(v[0])), nosent(asu(v[1])), nosent(asu(v[2])), nosent(asu(v[3]))}; }
__device__ __forceinline__ u32 mx4(v4u v, u32 m) { return umax(umax(umax(v[0], v[1]), m), umax(v[2], v[3])); }
__device__ __forceinline__ bool none_sent(u32 m) { return __builtin_amdgcn_ballot_w64(m == SENT) == 0; }
#define PIN4(f) asm volatile("" : "+v"((f)[0]), "+v"((f)[1]), "+v"((f)[2]), "+v"((f)[3]))
#define PIN8(f) asm volatile("" : "+v"(f[0]), "+v"(f[1]), "+v"(f[2]), "+v"(f[3]), "+v"(f[4]), "+v"(f[5]), "+v"(f[6]), "+v"(f[7]))
#define CBAR() asm volatile("" ::: "memory")
#define SCHED() __builtin_amdgcn_sched_barrier(0)
#define OPAQUE(x) asm volatile("" : "+s"(x))
template <int N> __device__ __forceinline__ void waitvm() { asm volatile("s_waitcnt vmcnt(%0)" ::"i"(N) : "memory"); }
#define NOUNROLL _Pragma("nounroll")
#define LD128(off, soff) __builtin_amdgcn_raw_buffer_load_b128(rws, off, soff, AUX_SC1)
#define LD64(off, soff) __builtin_amdgcn_raw_buffer_load_b64(rws, off, soff, AUX_SC1)
#define ST128(v, off, soff) __builtin_amdgcn_raw_buffer_store_b128(v, rws, off, soff, AUX_SC1)
#define ST64(v, off, soff) __builtin_amdgcn_raw_buffer_store_b64(v, rws, off, soff, AUX_SC1)
#define BARRIER() do { SCHED(); asm volatile("s_barrier" ::: "memory"); SCHED(); } while (0)

__device__ __forceinline__ u64 sent_mask(u32 m) { return __builtin_amdgcn_ballot_w64(m == SENT); }
#define RELD4(k, so) "s_mov_b64 exec, %[m" #k "]\n\tbuffer_load_dwordx4 %" #k ", %[off], %[rs], " so " offen sc1\n\t"
#define RELD_END "s_mov_b64 exec, -1\n\ts_waitcnt vmcnt(0)"

template <int F, int SLEEP> __device__ __forceinline__ void lds_wait(LAS u32* fl, u32 want) {
  CBAR();
  NOUNROLL for (int n = 0; n < SPIN_LDS; ++n) {
    if ((u32)__builtin_amdgcn_readfirstlane((int)__hip_atomic_load(fl + F, __ATOMIC_RELAXED, __HIP_MEMORY_SCOPE_WORKGROUP)) >= want) break;
    if (SLEEP) __builtin_amdgcn_s_sleep(SLEEP);
  }
  CBAR();
}
template <int F> __device__ __forceinline__ void lds_add(LAS u32* fl) {
  asm volatile("s_mov_b64 exec, 1\n\tds_add_u32 %0, %1 offset:%2\n\ts_mov_b64 exec, -1" ::"v"((u32)(size_t)fl), "v"(1u), "i"(4 * F) : "memory");
}
template <int F> __device__ __forceinline__ u32 lds_add_rtn(LAS u32* fl) {
  u32 old;
  asm volatile("s_mov_b64 exec, 1\n\tds_add_rtn_u32 %0, %1, %2 offset:%3\n\ts_mov_b64 exec, -1\n\ts_waitcnt lgkmcnt(0)"
               : "=v"(old) : "v"((u32)(size_t)fl), "v"(1u), "i"(4 * F) : "memory");
  return (u32)__builtin_amdgcn_readfirstlane((int)old);
}
template <int SLEEP> __device__ __forceinline__ void pace(u32 t0, u32 ticks) {
  NOUNROLL while ((int)((u32)__builtin_amdgcn_s_memrealtime() - t0) < (int)ticks) { if (SLEEP) __builtin_amdgcn_s_sleep(SLEEP); }
}
__device__ __forceinline__ int sel(bool c, int a, int b) { asm volatile("" : "+v"(a)); return c ? a : b; }

#define STAMP(k) do {} while (0)
#define STAMP_INIT() do {} while (0)
#define STAMP_START() do {} while (0)
#define STAMP_FLUSH() do {} while (0)

__global__ void __launch_bounds__(1024) fused_decode(const u16* x_a, const u16* kc_a, const u16* vc_a, const u16* wq_a, const u16* wo_a,
                                                     const u16* w1_a, const u16* w3_a, const u16* w2_a, u16* out_a, char* ws_a
) {
  __shared__ __attribute__((aligned(16))) char smem[L_END];
  LAS char* L = (LAS char*)smem;
  LAS u32* FL = (LAS u32*)(L + L_FLAG);
  const int bid = blockIdx.x, lane = threadIdx.x & 63, w = __builtin_amdgcn_readfirstlane(threadIdx.x >> 6);
  typedef u32 v16u __attribute__((ext_vector_type(16)));
  const CAS u64* kargs = (const CAS u64*)__builtin_amdgcn_kernarg_segment_ptr();
  v16u ka;
  v4u kb;
  u64 t64;
  asm volatile("s_load_dwordx16 %0, %3, 0x0\n\ts_load_dwordx4 %1, %3, 0x40\n\ts_memrealtime %2" : "=&s"(ka), "=&s"(kb), "=&s"(t64) : "s"(kargs));
  u32 cpf = 0;
  if (w == 3) {
    u64 pc0; asm volatile("s_getpc_b64 %0" : "=s"(pc0));
    int ln = (bid >> 3) + 32 * (lane & 7);
    ln = ln < CODE_LINES ? ln : 0;
    cpf = *(const GAS u32*)((const GAS char*)(pc0 & ~(u64)127) + ln * 128);
    FL[lane] = 0;
  }
  const int xcd = bid & 7, ii = bid >> 3;
  const int kvh = 2 * xcd + (ii >> 4), jj = ii & 15, bb = jj >> 3;
  const bool isY = ii < 18, isX = ii >= 18 && ii < 27;
  const int yi = xcd * 18 + ii, xc = ii - 18;
  const int g4 = lane >> 4, r16 = lane & 15, hb = lane >> 5, l5 = lane & 31;
  const int kw = w & 3;
  const int rot = bid * 5;
  const int k0 = w * 64 + (r16 >> 3) * 32 + g4 * 8, ka_ = w * 64 + (lane & 1) * 32 + g4 * 8;
  int xo = l5 < 24 ? hb * (D * 2) + l5 * 16 : OOB;
  int xf = sel(ka_ < 144, ((r16 >> 2) & 1) * (D * 2) + ka_ * 2, OOB) + (jj & 3) * 288;
  int qo = sel(k0 < 144, k0 * (QD * 2) + (lane & 7) * 16, OOB) + (jj & 3) * (144 * QD * 2) + kvh * 512 + (jj >> 2) * 128;
  SCHED();
  asm volatile("s_waitcnt lgkmcnt(0)" : "+s"(ka), "+s"(kb), "+s"(t64), "+v"(xo), "+v"(xf), "+v"(qo)::"memory");
  SCHED();
#define KPTR(v, i) ((u64)v[2 * (i)] | ((u64)v[2 * (i) + 1] << 32))
  const u16 *x = (const u16*)KPTR(ka, 0), *kc = (const u16*)KPTR(ka, 1), *vc = (const u16*)KPTR(ka, 2), *wq = (const u16*)KPTR(ka, 3),
            *wo = (const u16*)KPTR(ka, 4), *w1 = (const u16*)KPTR(ka, 5), *w3 = (const u16*)KPTR(ka, 6), *w2 = (const u16*)KPTR(ka, 7);
  u16* out = (u16*)KPTR(kb, 0);
  char* ws = (char*)KPTR(kb, 1);
  const u32 tstart = (u32)__builtin_amdgcn_s_memrealtime() + TOFF;
  const rsrc_t rws = mkrs(ws, WS_END);
  const rsrc_t rx = mkrs(x, 2 * D * 2);
#define LOAD_TAG() (*(const CAS u64*)(ws + WS_EPOCH) + 1)
#define LDS_TAG() (*(LAS v2u*)(FL + F_TAG))

  int yt = -1;
  int v2 = -1;
  if (w < 3) {
    v4u xs[4], tq[8];
#pragma unroll
    for (int i = 0; i < 3; ++i) xs[i] = __builtin_amdgcn_raw_buffer_load_b128(rx, xo + i * 384, 0, 0);
    xs[3] = __builtin_amdgcn_raw_buffer_load_b128(rx, xf, 0, 0);
    const rsrc_t rq = mkrs(wq, D * QD * 2);
#pragma unroll
    for (int i = 0; i < 8; ++i) tq[i] = __builtin_amdgcn_raw_buffer_load_b128(rq, qo, i * (QD * 2), AUX_NT);
    BARRIER();
    STAMP_START();
    const u64 tag = LOAD_TAG();
    int qpo = sel((lane & 0x28) == 0, L_QP + w * 512 + g4 * 256 + r16 * 32, L_DUMMY + lane * 32);
    int qso = WS_QG + ((hb * 16 + kvh) * 4 + (jj & 3)) * 2048 + (jj >> 2) * 512 + l5 * 16;
    asm volatile("" : "+v"(qpo), "+v"(qso));
    SCHED();
    float s = 0, v = 0;
#pragma unroll
    for (int i = 0; i < 3; ++i)
#pragma unroll
      for (int e = 0; e < 4; ++e) { s = dot2(xs[i][e], 0x3F803F80u, s); v = dot2(xs[i][e], xs[i][e], v); }
    s += dpp<XOR1>(s); v += dpp<XOR1>(v); s += dpp<XOR2>(s); v += dpp<XOR2>(v);
    s += dpp<HMIR>(s); v += dpp<HMIR>(v); s += dpp<ROR8>(s); v += dpp<ROR8>(v);
    s = x16_sum(s); v = x16_sum(v);
    const float mean = s * (1.f / D);
    const float mb = rbf(mean);
    const float rd = __builtin_amdgcn_rcpf(rbf(__builtin_amdgcn_sqrtf(rbf(rbf(fmaxf(v * (1.f / D) - mean * mean, 0.f)) + 1e-5f))));
    const auto ms = __builtin_amdgcn_permlane32_swap(asu(mb), asu(mb), false, false);
    const auto rs = __builtin_amdgcn_permlane32_swap(asu(rd), asu(rd), false, false);
    const float mm = asf((lane & 4) ? ms[1] : ms[0]), rr = asf((lane & 4) ? rs[1] : rs[0]);
    v4u a;
#pragma unroll
    for (int e = 0; e < 4; ++e) { u32 c = cvtpk(blo(xs[3][e]) - mm, bhi(xs[3][e]) - mm); a[e] = cvtpk(blo(c) * rr, bhi(c) * rr); }
    asm volatile("" : "+v"(a));
    SCHED();
    STAMP(11);
    v4u f[8];
    tr8(tq, f);
    f4 acc[8];
#pragma unroll
    for (int j = 0; j < 8; ++j) acc[j] = mfma(a, f[j], f4{0, 0, 0, 0});
    float o[8];
#pragma unroll
    for (int j = 0; j < 8; ++j) o[j] = acc[j][0] + dpp<ROR8>(acc[j][1]);
    STAMP(1);
    *(LAS f4*)(L + qpo) = f4{o[0], o[1], o[2], o[3]};
    *(LAS f4*)(L + qpo + 16) = f4{o[4], o[5], o[6], o[7]};
    if (lds_add_rtn<F_QCNT>(FL) == 2) {
      const f2 q = (*(LAS f2*)(L + L_QP + lane * 8) + *(LAS f2*)(L + L_QP + 512 + lane * 8)) + *(LAS f2*)(L + L_QP + 1024 + lane * 8);
      const u32 th = (u32)(tag >> 8), tl = (u32)tag & 0xffu;
      ST128((v4u{((asu(q[0]) + 0x80u) & ~0xffu) | tl, th, ((asu(q[1]) + 0x80u) & ~0xffu) | tl, th}), qso, 0);
      STAMP(2);
      const int go = WS_QG + (bb * 16 + kvh) * 8192 + lane * 16;
      v4u g[8];
      u64 m[8];
#pragma unroll
      for (int k = 0; k < 8; ++k) { m[k] = ~0ull; asm volatile("" : "=v"(g[k])); }
      pace<1>((u32)__builtin_amdgcn_s_memrealtime(), QS);
      pace<1>(tstart, TQ);
      NOUNROLL for (int n = 0; n < SPIN_GLB; ++n) {
        asm volatile(RELD4(0, "0") RELD4(1, "%[s1]") RELD4(2, "%[s2]") RELD4(3, "%[s3]") RELD4(4, "%[s4]") RELD4(5, "%[s5]") RELD4(6, "%[s6]")
                     RELD4(7, "%[s7]") RELD_END
                     : "+v"(g[0]), "+v"(g[1]), "+v"(g[2]), "+v"(g[3]), "+v"(g[4]), "+v"(g[5]), "+v"(g[6]), "+v"(g[7])
                     : [off] "v"(go), [rs] "s"(rws), [m0] "s"(m[0]), [m1] "s"(m[1]), [m2] "s"(m[2]), [m3] "s"(m[3]), [m4] "s"(m[4]), [m5] "s"(m[5]),
                       [m6] "s"(m[6]), [m7] "s"(m[7]), [s1] "s"(1024), [s2] "s"(2048), [s3] "s"(3072), [s4] "s"(4096), [s5] "s"(5120),
                       [s6] "s"(6144), [s7] "s"(7168)
                     : "memory");
        u64 any = 0;
#pragma unroll
        for (int k = 0; k < 8; ++k) {
          m[k] = __builtin_amdgcn_ballot_w64(((g[k][1] ^ th) | (g[k][3] ^ th) | (((g[k][0] ^ tl) | (g[k][2] ^ tl)) & 0xffu)) != 0);
          any |= m[k];
        }
        if (n == 0) STAMP(4);
        if (any == 0) break;
        __builtin_amdgcn_s_sleep(2);
      }
#pragma unroll
      for (int c = 0; c < 2; ++c) {
        const float a0 = (asf(g[c][0] & ~0xffu) + asf(g[2 + c][0] & ~0xffu)) + (asf(g[4 + c][0] & ~0xffu) + asf(g[6 + c][0] & ~0xffu));
        const float a1 = (asf(g[c][2] & ~0xffu) + asf(g[2 + c][2] & ~0xffu)) + (asf(g[4 + c][2] & ~0xffu) + asf(g[6 + c][2] & ~0xffu));
        *(LAS u32*)(L + L_Q + c * 256 + lane * 4) = cvtpk(a0, a1);
      }
      lds_add<F_QRDY>(FL);
      STAMP(3);
    }
    yt = w;
  } else {
    BARRIER();
    STAMP_START();
  }
  int w1_ = w;
  OPAQUE(w1_);
  v4u t[16];
  const rsrc_t rk = mkrs(w < 8 ? kc : vc, 2 * 16 * SK * 256);
  const int kvbase = ((bb * 16 + kvh) * SK + (SK - WIN) + (jj & 7) * 256 + kw * 64) * 256;
  if (w1_ >= 4 && w1_ < 8) {
    const int voff = (r16 >> 2) * 256 + (g4 * 4 + (lane & 3)) * 16 + kvbase;
    const u32 tk = tstart + kw * (DKV >> 5);
#pragma unroll
    for (int i = 0; i < 16; ++i) {
      if (i >= KVWN) waitvm<KVWN - 1>();
      pace<PS>(tk, TK0 + (i * DKV >> 3));
      if (i == 0) STAMP(10);
      if (i == 8) STAMP(11);
      t[i] = __builtin_amdgcn_raw_buffer_load_b128(rk, voff, ((i + rot) & 15) * 1024, AUX_NT);
      SCHED();
    }
    STAMP(1);
    lds_wait<F_QRDY, 1>(FL, 1);
    STAMP(2);
    v4u qf = *(LAS v4u*)(L + L_Q + ((r16 >> 2) & 1) * 256 + (g4 * 4 + (lane & 3)) * 16);
    const v4u z4 = {0, 0, 0, 0};
    const v4u q0 = r16 < 8 ? qf : z4, q1 = r16 < 8 ? z4 : qf;
    float sc[8];
#pragma unroll
    for (int m = 0; m < 8; ++m) {
      f4 d = mfma(t[m], q0, f4{0, 0, 0, 0});
      d = mfma(t[m + 8], q1, d);
      sc[m] = dpp<0xFF>(d[3]) + (dpp<0xAA>(d[2]) + (dpp<0x55>(d[1]) + dpp<0x00>(d[0])));
    }
    const bool b0 = lane & 1, b1 = lane & 2;
    float s0 = b1 ? (b0 ? sc[6] : sc[4]) : (b0 ? sc[2] : sc[0]);
    float s1 = b1 ? (b0 ? sc[7] : sc[5]) : (b0 ? sc[3] : sc[1]);
    u32 pk = cvtpk(s0, s1);
    pk = cvtpk(blo(pk) * 0.08838834764831845f, bhi(pk) * 0.08838834764831845f);
    s0 = blo(pk) * 1.4426950408889634f; s1 = bhi(pk) * 1.4426950408889634f;
    float mx = fmaxf(s0, s1);
    mx = fmaxf(mx, dpp<XOR1>(mx)); mx = fmaxf(mx, dpp<XOR2>(mx)); mx = fmaxf(mx, dpp<ROR8>(mx));
    mx = x32_max(x16_max(mx));
    const int hd = (r16 >> 2) & 1;
    LAS float* MX = (LAS float*)(L + L_MX);
    const float p0 = ex2(s0 - mx), p1 = ex2(s1 - mx);
    *(LAS u32*)(L + L_P + kw * 256 + hd * 128 + (r16 >> 3) * 64 + g4 * 16 + (lane & 3) * 4) = cvtpk(p0, p1);
    float ls = p0 + p1;
    ls += dpp<XOR1>(ls); ls += dpp<XOR2>(ls); ls += dpp<ROR8>(ls);
    ls = x32_sum(x16_sum(ls));
    MX[hd * 4 + kw] = mx; MX[8 + hd * 4 + kw] = ls;
    lds_add<F_PCNT>(FL);
    STAMP(3);
    if (isX && kw == 0) {
      const int z = xcd * 9 + ii - 18, zl = z >> 1, zb = zl / 18, zz = zl % 18;
      const int col = (zz >> 1) * 64 + (z & 1) * 32 + (lane & 3) * 8 + (zz & 1) * 4;
      const int jz = (g4 >> 1) + 2 * (g4 & 1);
      const u32 xv = __builtin_amdgcn_raw_buffer_load_b16(rx, (zb * D + col + jz) * 2, 0, 0);
      const int ho = sel(lane < 32, WS_AP + ((zz >> 1) * 8 + (lane >> 2)) * 512 + (lane & 3) * 128 + zb * 64 + (zz & 1) * 32 + (z & 1) * 16, OOB);
      int oo = sel(r16 < 4, (zb * D + col + jz) * 2, OOB);
      const int mo = WS_MP + zl * 18432 + (lane >> 2) * 128 + (z & 1) * 64 + (lane & 3) * 16;
      lds_wait<F_XCNT, 2>(FL, 5);
      v4u hv;
      NOUNROLL for (int n = 0; n < SPIN_GLB; ++n) {
        hv = LD128(ho, 0);
        CBAR();
        if (none_sent(mx4(hv, 0))) break;
        __builtin_amdgcn_s_sleep(4);
      }
      float h = t16_sum(t32_sum(asf(hv[0]), asf(hv[1])), t32_sum(asf(hv[2]), asf(hv[3])));
      h += dpp<ROR4>(h); h += dpp<ROR8>(h);
      h = rbf(blo(xv) + rbf(h));
      asm volatile("" : "+v"(oo), "+v"(h));
      STAMP(12);
      v4u p[9];
      u64 m[9];
      f4 sum;
#pragma unroll
      for (int k = 0; k < 9; ++k) { m[k] = ~0ull; asm volatile("" : "=v"(p[k])); }
      NOUNROLL for (int n = 0; n < SPIN_GLB; ++n) {
        asm volatile(RELD4(0, "0") RELD4(1, "%[s1]") RELD4(2, "%[s2]") RELD4(3, "%[s3]") RELD4(4, "%[s4]") RELD4(5, "%[s5]") RELD4(6, "%[s6]")
                     RELD4(7, "%[s7]") RELD4(8, "%[s8]") RELD_END
                     : "+v"(p[0]), "+v"(p[1]), "+v"(p[2]), "+v"(p[3]), "+v"(p[4]), "+v"(p[5]), "+v"(p[6]), "+v"(p[7]), "+v"(p[8])
                     : [off] "v"(mo), [rs] "s"(rws), [m0] "s"(m[0]), [m1] "s"(m[1]), [m2] "s"(m[2]), [m3] "s"(m[3]), [m4] "s"(m[4]), [m5] "s"(m[5]),
                       [m6] "s"(m[6]), [m7] "s"(m[7]), [m8] "s"(m[8]), [s1] "s"(2048), [s2] "s"(4096), [s3] "s"(6144), [s4] "s"(8192),
                       [s5] "s"(10240), [s6] "s"(12288), [s7] "s"(14336), [s8] "s"(16384)
                     : "memory");
        sum = (((asf4(p[0]) + asf4(p[1])) + (asf4(p[2]) + asf4(p[3]))) + ((asf4(p[4]) + asf4(p[5])) + (asf4(p[6]) + asf4(p[7])))) + asf4(p[8]);
        if (!any_nan(sum)) break;
        u64 any = 0, all = ~0ull;
#pragma unroll
        for (int k = 0; k < 9; ++k) { m[k] = sent_mask(mx4(p[k], 0)); any |= m[k]; all &= m[k]; }
        if (any == 0) break;
        if (all == ~0ull) __builtin_amdgcn_s_sleep(4);
      }
      STAMP(13);
      float r = t16_sum(t32_sum(sum[0], sum[1]), t32_sum(sum[2], sum[3]));
      r += dpp<ROR4>(r); r += dpp<ROR8>(r);
      r = h + rbf(r);
      __builtin_amdgcn_raw_buffer_store_b16((u16)cvtpk(r, r), mkrs(out, 2 * D * 2), oo, 0, 0);
      if (z == 0) __builtin_amdgcn_raw_buffer_store_b64(LDS_TAG(), rws, lane == 0 ? WS_EPOCH : OOB, 0, 0);
      STAMP(14);
    }
    if (isX && kw == 1) {
      const int wo_ = WS_AP + xc * 4096 + l5 * 128;
      lds_wait<F_XCNT, 2>(FL, 5);
      u32 v = SENT;
      u64 m = 0xFFFFFFFFull;
      NOUNROLL for (int n = 0; n < SPIN_GLB && m; ++n) {
        asm volatile("s_mov_b64 exec, %[m]\n\tbuffer_load_dword %0, %[off], %[rs], 0 offen sc1\n\t" RELD_END
                     : "+v"(v) : [off] "v"(wo_), [rs] "s"(rws), [m] "s"(m) : "memory");
        m &= sent_mask(v);
      }
      STAMP(12);
    }
    if (kw == 0) yt = 4;
    if (kw == 1) v2 = 4;
  }
  int w2_ = w;
  OPAQUE(w2_);
  if (w2_ >= 8 && w2_ < 12) {
    const int voff = lane * 16 + kvbase;
    u32 tk = tstart + kw * (DKV >> 5);
    pace<2>(tk, TK0 + (15 * DKV >> 3));
#pragma unroll
    for (int i = 0; i < 16; ++i) {
      if (i >= KVWN) waitvm<KVWN - 1>();
      pace<PS>(tk, TK0 + ((16 + i) * DKV >> 3));
      if (i == 0) STAMP(10);
      if (i == 8) STAMP(11);
      t[i] = __builtin_amdgcn_raw_buffer_load_b128(rk, voff, ((i + rot) & 15) * 1024, AUX_NT);
      SCHED();
    }
    lds_add<F_VISS>(FL);
    STAMP(1);
    v4u f[8];
    tr8(t, f);
    PIN8(f);
    lds_wait<F_PCNT, 1>(FL, 4);
    STAMP(2);
    const v4u pa = *(LAS v4u*)(L + L_P + kw * 256 + ((r16 >> 2) & 1) * 128 + g4 * 16);
    const v4u pb = *(LAS v4u*)(L + L_P + kw * 256 + ((r16 >> 2) & 1) * 128 + 64 + g4 * 16);
    const f4 m4 = *(LAS f4*)(L + L_MX + (g4 & 1) * 16), l4 = *(LAS f4*)(L + L_MX + 32 + (g4 & 1) * 16);
    const float mw = *(LAS float*)(L + L_MX + (g4 & 1) * 16 + kw * 4);
    f4 acc[8];
#pragma unroll
    for (int j = 0; j < 8; ++j) acc[j] = mfma(pa, f[j], f4{0, 0, 0, 0});
    const float M = fmaxf(fmaxf(m4[0], m4[1]), fmaxf(m4[2], m4[3]));
    float fs = ex2(mw - M);
    v2u ml = {nosent(asu(M)), nosent(asu((l4[0] * ex2(m4[0] - M) + l4[1] * ex2(m4[1] - M)) + (l4[2] * ex2(m4[2] - M) + l4[3] * ex2(m4[3] - M))))};
    int mlo = sel((lane & 0x2F) == 0, WS_ML + (kvh * 16 + jj) * 128 + g4 * 8, OOB);
    int opo = WS_OP + ((2 * kvh + hb) * 16 + jj) * 256 + l5 * 8;
    int lo = L_O + kw * 1024 + (g4 & 1) * 512 + r16 * 32 + (g4 >> 1) * 16;
    asm volatile("" : "+v"(fs), "+v"(ml), "+v"(mlo), "+v"(opo), "+v"(lo));
    lds_wait<F_CLR, 1>(FL, 1);
    SCHED();
    tr8(t + 8, f);
#pragma unroll
    for (int j = 0; j < 8; ++j) acc[j] = mfma(pb, f[j], acc[j]);
    STAMP(3);
    {
      const bool hi = g4 >= 2;
      f4 o = {hi ? acc[4][0] : acc[0][0], hi ? acc[5][0] : acc[1][0], hi ? acc[6][0] : acc[2][0], hi ? acc[7][0] : acc[3][0]};
      *(LAS f4*)(L + lo) = o * fs;
    }
    if (lds_add_rtn<F_OCNT>(FL) == 3) {
      ST64(ml, mlo, 0);
      f4 o = (*(LAS f4*)(L + L_O + lane * 16) + *(LAS f4*)(L + L_O + 1024 + lane * 16)) +
             (*(LAS f4*)(L + L_O + 2048 + lane * 16) + *(LAS f4*)(L + L_O + 3072 + lane * 16));
      ST64((v2u{nosent(cvtpk(o[0], o[1])), nosent(cvtpk(o[2], o[3]))}), opo, 0);
      lds_add<F_OCNT>(FL);
      if (SELFWAIT) waitvm<0>();
      const u32 rb = __builtin_amdgcn_raw_buffer_load_b32(rws, r16 == 0 ? opo : lane == 1 ? WS_ML + (kvh * 16 + jj) * 128 : OOB, 0, AUX_SC1);
      asm volatile("" ::"v"(rb));
    }
    STAMP(4);
    v2 = kw;
  }
  int w3_ = w;
  OPAQUE(w3_);
  if (w3_ == 3) {
    const u64 tag = LOAD_TAG();
    const v4u s4 = {SENT, SENT, SENT, SENT};
    ST64((v2u{SENT, SENT}), WS_OP + ((2 * kvh + hb) * 16 + jj) * 256 + l5 * 8, 0);
    ST64((v2u{SENT, SENT}), sel(lane < 16, WS_ML + (kvh * 16 + jj) * 128 + lane * 8, OOB), 0);
    if (ii == 31) {
      const int ao = WS_AP + (hb * 8 + xcd) * 512 + l5 * 16;
#pragma unroll
      for (int n = 0; n < 4; ++n) ST128(s4, ao, n * 8192);
      ST128(s4, lane < 32 ? ao : OOB, 4 * 8192);
    }
    STAMP(1);
    asm volatile("s_waitcnt vmcnt(0)" ::"v"(cpf) : "memory");
    const v2u tg = {(u32)tag, (u32)(tag >> 32)};
    ST64(tg, lane == 0 ? WS_FLAGS + bid * 128 : OOB, 0);
    *(LAS v2u*)(FL + F_TAG) = tg;
    if (isY) {
      const int mo = WS_MP + (lane >> 3) * 18432 + yi * 128 + (lane & 7) * 16;
      pace<4>(tstart, TMP + ii * DMP);
#pragma unroll
      for (int n = 0; n < 4; ++n) ST128(s4, mo, n * 8 * 18432);
      ST128(s4, lane < 32 ? mo : OOB, 4 * 8 * 18432);
    }
    if (ii < 27) {
      const bool act = lane < 32 ? isX : lane < 40;
      const int fo = sel(act, WS_FLAGS + (lane < 32 ? lane * 8 + xcd : 248 + lane - 32) * 128, OOB);
      pace<4>(tstart, TFL);
      NOUNROLL for (int n = 0; n < SPIN_GLB; ++n) {
        const v2u g = LD64(fo, 0);
        CBAR();
        if (__builtin_amdgcn_ballot_w64(act && ((g[0] ^ tg[0]) | (g[1] ^ tg[1])) != 0) == 0) break;
        __builtin_amdgcn_s_sleep(12);
      }
      STAMP(3);
    }
    waitvm<0>();
    lds_add<F_CLR>(FL);
    STAMP(2);
    if (!isY) {
      const rsrc_t rp = mkrs(w2, (xcd + 1) * (576 * D * 2));
      const int po = lane * 128 + xcd * (576 * D * 2) + (31 - ii) * 8192;
      if (isX) lds_wait<F_XCNT, 2>(FL, 5); else lds_wait<F_BI, 2>(FL, 1);
      pace<1>(tstart, TW2);
      STAMP(4);
      u32 pf[6];
#pragma unroll
      for (int k = 0; k < 6; ++k) {
        if (k >= 4) waitvm<3>();
        pf[k] = __builtin_amdgcn_raw_buffer_load_b32(rp, po, k * 14 * 8192, 0);
        SCHED();
      }
#pragma unroll
      for (int k = 0; k < 6; ++k) asm volatile("" ::"v"(pf[k]));
      STAMP(5);
    }
    yt = 3;
  } else if (w3_ >= 12) {
    if (isX) {
      const int hh = 4 * xcd + kw;
      const rsrc_t ro = mkrs(wo, QD * D * 2);
      const int vo = (hh * 128 + g4 * 32) * (D * 2) + (xc * 64 + r16 * 4) * 2;
      int xo = L_X + kw * 512 + ((r16 >> 1) & 3) * 128 + (g4 >> 1) * 64 + (r16 & 1) * 32 + (r16 >> 3) * 16;
      int po = sel(lane < 32, WS_AP + (xc * 8 + xcd) * 512 + lane * 16, OOB);
      const int ao = WS_OP + (hh * 16 + (r16 & 12)) * 256 + (g4 * 4 + (lane & 3)) * 16;
      const int mo = sel(r16 < 8, WS_ML + ((hh >> 1) * 16 + (g4 >> 1) * 8 + ((r16 + 4 * (g4 & 1)) & 7)) * 128 + (hh & 1) * 8, OOB);
      asm volatile("" : "+v"(xo), "+v"(po));
      lds_wait<F_VISS, 1>(FL, 4);
      const u32 tr = tstart;
      pace<1>(tr, TXB + kw * DXB);
      STAMP(8);
      v2u u[32];
#pragma unroll
      for (int i = 0; i < 32; ++i) u[i] = __builtin_amdgcn_raw_buffer_load_b64(ro, vo, i * (D * 2), AUX_NT);
      SCHED();
      pace<1>(tr, TXO);
      lds_wait<F_CLR, 1>(FL, 1);
      v4u a[4] = {LD128(ao, 0), LD128(ao + 256, 0), LD128(ao + 512, 0), LD128(ao + 768, 0)};
      v2u ml = LD64(mo, 0);
      SCHED();
      STAMP(4);
      v4u f[16];
#pragma unroll
      for (int k = 0; k < 4; ++k) { tr4(u + 8 * k, f + 4 * k); PIN4(f + 4 * k); }
      STAMP(5);
      NOUNROLL for (int n = 0; n < SPIN_GLB; ++n) {
        if (none_sent(mx4(a[3], mx4(a[2], mx4(a[1], mx4(a[0], umax(ml[0], ml[1]))))))) break;
        u64 m[4];
#pragma unroll
        for (int k = 0; k < 4; ++k) m[k] = sent_mask(mx4(a[k], 0));
        const u64 mm = sent_mask(umax(ml[0], ml[1]));
        asm volatile("s_mov_b64 exec, %[mm]\n\tbuffer_load_dwordx2 %4, %[mo], %[rs], 0 offen sc1\n\t"
                     "s_mov_b64 exec, %[m0]\n\tbuffer_load_dwordx4 %0, %[off], %[rs], 0 offen sc1\n\t"
                     "s_mov_b64 exec, %[m1]\n\tbuffer_load_dwordx4 %1, %[off], %[rs], 0 offen offset:256 sc1\n\t"
                     "s_mov_b64 exec, %[m2]\n\tbuffer_load_dwordx4 %2, %[off], %[rs], 0 offen offset:512 sc1\n\t"
                     "s_mov_b64 exec, %[m3]\n\tbuffer_load_dwordx4 %3, %[off], %[rs], 0 offen offset:768 sc1\n\t" RELD_END
                     : "+v"(a[0]), "+v"(a[1]), "+v"(a[2]), "+v"(a[3]), "+v"(ml)
                     : [off] "v"(ao), [mo] "v"(mo), [rs] "s"(rws), [m0] "s"(m[0]), [m1] "s"(m[1]), [m2] "s"(m[2]), [m3] "s"(m[3]), [mm] "s"(mm)
                     : "memory");
      }
      STAMP(6);
      quad_tr(a);
      f4 d[4];
#pragma unroll
      for (int j = 0; j < 4; ++j) d[j] = mfma(a[0], f[j], f4{0, 0, 0, 0});
#pragma unroll
      for (int k = 1; k < 4; ++k)
#pragma unroll
        for (int j = 0; j < 4; ++j) d[j] = mfma(a[k], f[4 * k + j], d[j]);
      float M = asf(ml[0]);
      M = fmaxf(M, dpp<XOR1>(M)); M = fmaxf(M, dpp<XOR2>(M)); M = fmaxf(M, dpp<HMIR>(M));
      const float ee = ex2(asf(ml[0]) - M);
      float Ls = ee * asf(ml[1]);
      Ls += dpp<XOR1>(Ls); Ls += dpp<XOR2>(Ls); Ls += dpp<HMIR>(Ls);
      const float wr = ee * __builtin_amdgcn_rcpf(Ls);
      const float e[4] = {dpp<0x150>(wr), dpp<0x151>(wr), dpp<0x152>(wr), dpp<0x153>(wr)};
      f4 o;
#pragma unroll
      for (int j = 0; j < 4; ++j) o[j] = x16_sum((e[0] * d[j][0] + e[1] * d[j][1]) + (e[2] * d[j][2] + e[3] * d[j][3]));
      *(LAS f4*)(L + xo) = o;
      if (lds_add_rtn<F_XCNT>(FL) == 3) {
        LAS char* rp = L + L_X + l5 * 16;
        const f4 s = (*(LAS f4*)(rp) + *(LAS f4*)(rp + 512)) + (*(LAS f4*)(rp + 1024) + *(LAS f4*)(rp + 1536));
        ST128(nosent4(s), po, 0);
        lds_add<F_XCNT>(FL);
        STAMP(9);
      }
      STAMP(12);
    } else if (w == 15) {
      const int pi = isY ? ii : ii - 9;
      const int br = lane / 9;
      const int band = sel(lane < 63, br * (FF * 2) + (lane - br * 9) * 128, OOB) + xcd * 1152;
      const rsrc_t ro = mkrs(wo, (xcd + 1) * (512 * D * 2));
      const int oo = lane * 128 + xcd * (512 * D * 2) + (22 - pi) * 8192;
      u32 pf[12];
      pace<2>(tstart, TWO);
      STAMP(1);
#pragma unroll
      for (int k = 0; k < 4; ++k) {
        if (k < 3) pace<PS>(tstart, TWO + k * DWO);
        pf[k] = __builtin_amdgcn_raw_buffer_load_b32(ro, oo, k * 23 * 8192, 0);
        SCHED();
      }
      lds_wait<F_VISS, 1>(FL, 4);
      STAMP(4);
#pragma unroll
      for (int k = 0; k < 8; ++k) {
        const int m = 22 - pi + 23 * k;
        if (k < 7 || !isY) {
          waitvm<PWIN - 1>();
          pace<PS>(tstart, TPF + (k < 7 ? k : 6) * DPF);
          pf[4 + k] = __builtin_amdgcn_raw_buffer_load_b32(mkrs((m & 1) ? w3 : w1, D * FF * 2), band, (m >> 1) * (7 * FF * 2), 0);
        } else pf[4 + k] = 0;
        SCHED();
      }
      STAMP(2);
      if (!isY) lds_add<F_BI>(FL);
      waitvm<PFLEFT>();
      lds_wait<F_OCNT, 1>(FL, 5);
      lds_add<F_PF>(FL);
#pragma unroll
      for (int k = 0; k < 12; ++k) asm volatile("" ::"v"(pf[k]));
      STAMP(3);
    }
    yt = w - 7;
  }
  if (isY) {
    if (yt >= 0) {
      const GAS char* wp = (const GAS char*)((lane & 4) ? w3 : w1) + (size_t)((yt * 64 + (r16 >> 3) * 32 + g4 * 8) * (FF * 2) + (yi * 32 + (lane & 3) * 8) * 2);
      const int ao = WS_AP + (yt * 8 + (r16 >> 3)) * 512 + g4 * 128 + (lane & 7) * 16;
      int ypo = sel(r16 < 8, L_R + yt * 1024 + ((lane & 3) * 16 + g4 * 4 + (r16 >> 2) * 2) * 16, L_DUMMY + lane * 32);
      int sto = sel(lane == 0 || lane == 8 || lane == 20 || lane == 24, L_ST + ((r16 == 8 ? 0 : 2) + g4) * 64 + yt * 4, L_DUMMY + lane * 32);
      asm volatile("" : "+v"(ypo), "+v"(sto), "+v"(wp));
      v2u xv = __builtin_amdgcn_raw_buffer_load_b64(rx, (((lane >> 2) & 1) * D + yt * 64 + (lane & 1) * 32 + g4 * 8 + ((lane >> 1) & 1) * 4) * 2, 0, 0);
      asm volatile("" : "+v"(xv));
      lds_wait<F_PF, 1>(FL, 1);
      if (yt >= YHOLD) lds_wait<F_BI, 0>(FL, yt - YHOLD + 1);
#pragma unroll
      for (int i = 0; i < 8; ++i) t[i] = __builtin_nontemporal_load((const GAS v4u*)(wp + i * (FF * 2)));
      SCHED();
      lds_add<F_BI>(FL);
      const bool ride = (u32)__builtin_amdgcn_s_memrealtime() - tstart >= (u32)(TAP - RIDEW);
      const int ar = ride ? ao : OOB;
      v4u p[4] = {LD128(ar, 0), LD128(ar + 1024, 0), LD128(ar + 2048, 0), LD128(ar + 3072, 0)};
      SCHED();
      STAMP(13);
      v4u f[8];
      tr8(t, f);
      PIN8(f);
      STAMP(14);
      if (!ride) p[0] = p[1] = p[2] = p[3] = v4u{SENT, SENT, SENT, SENT};
      f4 hs;
      NOUNROLL for (int n = 0; n < SPIN_GLB; ++n) {
        hs = (asf4(p[0]) + asf4(p[1])) + (asf4(p[2]) + asf4(p[3]));
        if (!any_nan(hs)) break;
        u64 m[4];
#pragma unroll
        for (int k = 0; k < 4; ++k) m[k] = sent_mask(mx4(p[k], 0));
        if ((m[0] | m[1] | m[2] | m[3]) == 0) break;
        if (n == 0) { pace<1>(tstart, TAP); if (ride) lds_wait<F_BI, 1>(FL, APGATE); }
        else __builtin_amdgcn_s_sleep(YSLEEP);
        asm volatile("s_mov_b64 exec, %[m0]\n\tbuffer_load_dwordx4 %0, %[off], %[rs], 0 offen sc1\n\t"
                     "s_mov_b64 exec, %[m1]\n\tbuffer_load_dwordx4 %1, %[off], %[rs], 0 offen offset:1024 sc1\n\t"
                     "s_mov_b64 exec, %[m2]\n\tbuffer_load_dwordx4 %2, %[off], %[rs], 0 offen offset:2048 sc1\n\t"
                     "s_mov_b64 exec, %[m3]\n\tbuffer_load_dwordx4 %3, %[off], %[rs], 0 offen offset:3072 sc1\n\t" RELD_END
                     : "+v"(p[0]), "+v"(p[1]), "+v"(p[2]), "+v"(p[3])
                     : [off] "v"(ao), [rs] "s"(rws), [m0] "s"(m[0]), [m1] "s"(m[1]), [m2] "s"(m[2]), [m3] "s"(m[3])
                     : "memory");
      }
      STAMP(15);
      float h[4];
#pragma unroll
      for (int j = 0; j < 4; ++j) h[j] = hs[j] + dpp<ROR8>(hs[j]);
      const u32 c0 = cvtpk(h[0], h[1]), c1 = cvtpk(h[2], h[3]);
      const u32 h0 = cvtpk(blo(xv[0]) + blo(c0), bhi(xv[0]) + bhi(c0)), h1 = cvtpk(blo(xv[1]) + blo(c1), bhi(xv[1]) + bhi(c1));
      const u32 h2 = dppu<XOR2>(h0), h3 = dppu<XOR2>(h1);
      const bool one = lane & 8;
      const v4u a = {one ? 0x3F803F80u : h0, one ? 0x3F803F80u : h1, one ? 0x3F803F80u : h2, one ? 0x3F803F80u : h3};
      f4 acc[8];
#pragma unroll
      for (int j = 0; j < 8; ++j) acc[j] = mfma(a, f[j], f4{0, 0, 0, 0});
      const f4 gm = mfma(a, a, f4{0, 0, 0, 0});
      float o[8];
#pragma unroll
      for (int j = 0; j < 8; ++j) o[j] = acc[j][0] + dpp<ROR8>(acc[j][1]);
      *(LAS f4*)(L + ypo) = f4{o[0], o[1], o[2], o[3]};
      *(LAS f4*)(L + ypo + 16) = f4{o[4], o[5], o[6], o[7]};
      *(LAS float*)(L + sto) = gm[0] + dpp<SHL1>(gm[1]);
      lds_add<F_RCNT>(FL);
      STAMP(7);
    } else if (v2 >= 0) {
      const rsrc_t r2 = mkrs(w2, FF * D * 2);
      const int vo = sel(v2 < 4 || r16 < 8, (yi * 32 + g4 * 8) * (D * 2) + (v2 * 128 + r16 * 8) * 2, OOB);
      int mo = sel(g4 < 2 && (v2 < 4 || r16 < 8), WS_MP + (g4 * 18 + v2 * 4 + (r16 >> 3) * 2) * 18432 + yi * 128 + (lane & 7) * 16, OOB);
      int sto = sel(r16 < 9, L_ST + lane * 4, L_FLAG + 4 * F_ZERO);
      asm volatile("" : "+v"(mo), "+v"(sto));
      lds_wait<F_BI, 1>(FL, W2GATE);
#pragma unroll
      for (int i = 0; i < 8; ++i) t[i] = __builtin_amdgcn_raw_buffer_load_b128(r2, vo, i * (D * 2), AUX_NT);
      SCHED();
      STAMP(13);
      v4u f[8];
      tr8(t, f);
      PIN8(f);
      STAMP(14);
      lds_wait<F_RCNT, 2>(FL, 1);
      lds_wait<F_RCNT, 0>(FL, 9);
      STAMP(15);
      float st = *(LAS float*)(L + sto);
      f4 y[9];
#pragma unroll
      for (int k = 0; k < 9; ++k) y[k] = *(LAS f4*)(L + L_R + k * 1024 + lane * 16);
      SCHED();
      const f4 s = (((y[0] + y[1]) + (y[2] + y[3])) + ((y[4] + y[5]) + (y[6] + y[7]))) + y[8];
      st += dpp<XOR1>(st); st += dpp<XOR2>(st); st += dpp<HMIR>(st); st += dpp<ROR8>(st);
      const float a0 = asf(__builtin_amdgcn_readlane(asu(st), 0)), a1 = asf(__builtin_amdgcn_readlane(asu(st), 16));
      const float q0 = asf(__builtin_amdgcn_readlane(asu(st), 32)), q1 = asf(__builtin_amdgcn_readlane(asu(st), 48));
      const float mean = ((lane & 4) ? a1 : a0) * (1.f / D);
      const float var = ((lane & 4) ? q1 : q0) * (1.f / D) - mean * mean;
      const float rs = __builtin_amdgcn_rsqf(fmaxf(var, 0.f) + 1e-5f);
      float hid[4];
#pragma unroll
      for (int j = 0; j < 4; ++j) {
        const float zv = rs * (s[j] - mean * dpp<ROR8>(s[j]));
        const float up = dpp<XOR2>(zv);
        hid[j] = zv * __builtin_amdgcn_rcpf(1.f + ex2(-1.4426950408889634f * zv)) * up;
      }
      const u32 h0 = cvtpk(hid[0], hid[1]), h1 = cvtpk(hid[2], hid[3]);
      const v4u a = {h0, h1, dppu<XOR1>(h0), dppu<XOR1>(h1)};
      f4 acc[8];
#pragma unroll
      for (int j = 0; j < 8; ++j) acc[j] = mfma(a, f[j], f4{0, 0, 0, 0});
      ST128(nosent4(f4{acc[0][0], acc[1][0], acc[2][0], acc[3][0]}), mo, 0);
      ST128(nosent4(f4{acc[4][0], acc[5][0], acc[6][0], acc[7][0]}), mo, 18432);
      STAMP(7);
    }
  }
  STAMP_FLUSH();
}

extern "C" void kernel_launch(void* const* d_in, const int* in_sizes, int n_in, void* d_out, int out_size, void* d_ws, size_t ws_size,
                              hipStream_t stream) {
  fused_decode<<<256, 1024, 0, stream>>>((const u16*)d_in[0], (const u16*)d_in[1], (const u16*)d_in[2], (const u16*)d_in[3],
                                         (const u16*)d_in[4], (const u16*)d_in[6], (const u16*)d_in[7], (const u16*)d_in[8], (u16*)d_out,
                                         (char*)d_ws
  );
}
```

```cpp
#include <hip/hip_runtime.h>
#include <hip/hip_bf16.h>
#include <stdint.h>

#define GAS __attribute__((address_space(1)))
#define LAS __attribute__((address_space(3)))
#define CAS __attribute__((address_space(4)))
typedef unsigned u32;
typedef unsigned long long u64;
typedef unsigned short u16;
typedef u32 v4u __attribute__((ext_vector_type(4)));
typedef u32 v2u __attribute__((ext_vector_type(2)));
typedef float f4 __attribute__((ext_vector_type(4)));
typedef float f2 __attribute__((ext_vector_type(2)));
typedef __bf16 bf8 __attribute__((ext_vector_type(8)));
typedef __bf16 bf2 __attribute__((ext_vector_type(2)));
typedef __amdgpu_buffer_rsrc_t rsrc_t;

constexpr int TOFF = -10;
constexpr int TK0 = 60, DK = 120, DV = 120;
constexpr int TVE = TK0 + 2 * DK + 2 * DV;
constexpr int DWO = 22, DPF = 22;
constexpr int TWO = TVE + 7, TPF = TWO + 3 + 3 * DWO, TW2 = TPF + 7 * DPF + 7;
constexpr int TXB = TVE + 67, DXB = 21, TXO = TVE + 127;
constexpr int TAP = TVE + 342;
constexpr int TQ = 250, QS = 50, TFL = 300;
constexpr int VSYNC = 6, DITH = 4;
constexpr int KVWN = 8, PWIN = 4, NPX = 4;
constexpr int CODE_LINES = 142;

constexpr int D = 576, SK = 8192, WIN = 2048, FF = 4608, QD = 4096;
constexpr int WS_EPOCH = 0, WS_FLAGS = 4096, WS_OP = WS_FLAGS + 256 * 128, WS_AP = WS_OP + 256 * 640,
              WS_MP = WS_AP + 36864, WS_QG = WS_MP + 663552, WS_END = WS_QG + 262144;
constexpr u32 SENT = 0xFFFFFFFFu;
constexpr int AUX_NT = 2, AUX_SC1 = 16, OOB = 1 << 30;
constexpr int SPIN_LDS = 1 << 20, SPIN_GLB = 1 << 17;
constexpr int L_FLAG = 0;
constexpr int L_Q = 256;
constexpr int L_P = 768;
constexpr int L_MX = 1792;
constexpr int L_ST = 1920;
constexpr int L_QP = 2304;
constexpr int L_O = 4096;
constexpr int L_X = 8192;
constexpr int L_R = 10240;
constexpr int L_DUMMY = L_R + 9216;
constexpr int L_END = L_DUMMY + 2048;
enum { F_QCNT = 0, F_QRDY, F_PCNT, F_OCNT, F_VISS, F_CLR, F_INOK, F_PF, F_RCNT, F_XCNT, F_ZERO, F_BI, F_DT, F_TAG = 16 };

__device__ __forceinline__ rsrc_t mkrs(const void* p, int bytes) { return __builtin_amdgcn_make_buffer_rsrc((void*)p, 0, bytes, 0x00020000); }
__device__ __forceinline__ float asf(u32 u) { return __uint_as_float(u); }
__device__ __forceinline__ u32 asu(float f) { return __float_as_uint(f); }
__device__ __forceinline__ u32 cvtpk(float lo, float hi) { return __builtin_bit_cast(u32, __builtin_convertvector(f2{lo, hi}, bf2)); }
__device__ __forceinline__ float rbf(float f) { return asf(cvtpk(f, f) & 0xffff0000u); }
__device__ __forceinline__ float blo(u32 u) { return asf(u << 16); }
__device__ __forceinline__ float bhi(u32 u) { return asf(u & 0xffff0000u); }
__device__ __forceinline__ float ex2(float x) { return __builtin_amdgcn_exp2f(x); }
__device__ __forceinline__ float dot2(u32 a, u32 b, float c) { return __builtin_amdgcn_fdot2_f32_bf16(__builtin_bit_cast(bf2, a), __builtin_bit_cast(bf2, b), c, false); }
template <int C> __device__ __forceinline__ u32 dppu(u32 x) { return (u32)__builtin_amdgcn_mov_dpp((int)x, C, 0xf, 0xf, true); }
template <int C> __device__ __forceinline__ float dpp(float x) { return asf(dppu<C>(asu(x))); }
constexpr int XOR1 = 0xB1, XOR2 = 0x4E, HMIR = 0x141, ROR4 = 0x124, ROR8 = 0x128, SHL1 = 0x101;
__device__ __forceinline__ float x16_sum(float x) { auto s = __builtin_amdgcn_permlane16_swap(asu(x), asu(x), false, false); return asf(s[0]) + asf(s[1]); }
__device__ __forceinline__ float x32_sum(float x) { auto s = __builtin_amdgcn_permlane32_swap(asu(x), asu(x), false, false); return asf(s[0]) + asf(s[1]); }
__device__ __forceinline__ float x16_max(float x) { auto s = __builtin_amdgcn_permlane16_swap(asu(x), asu(x), false, false); return fmaxf(asf(s[0]), asf(s[1])); }
__device__ __forceinline__ float x32_max(float x) { auto s = __builtin_amdgcn_permlane32_swap(asu(x), asu(x), false, false); return fmaxf(asf(s[0]), asf(s[1])); }
__device__ __forceinline__ float t32_sum(float a, float b) { auto s = __builtin_amdgcn_permlane32_swap(asu(a), asu(b), false, false); return asf(s[0]) + asf(s[1]); }
__device__ __forceinline__ float t16_sum(float a, float b) { auto s = __builtin_amdgcn_permlane16_swap(asu(a), asu(b), false, false); return asf(s[0]) + asf(s[1]); }
__device__ __forceinline__ f4 asf4(v4u v) { return __builtin_bit_cast(f4, v); }
__device__ __forceinline__ bool any_nan(f4 v) { return __builtin_amdgcn_ballot_w64(__builtin_isunordered(v[0], v[1]) || __builtin_isunordered(v[2], v[3])) != 0; }
__device__ __forceinline__ f4 mfma(v4u a, v4u b, f4 c) { return __builtin_amdgcn_mfma_f32_16x16x32_bf16(__builtin_bit_cast(bf8, a), __builtin_bit_cast(bf8, b), c, 0, 0, 0); }
__device__ __forceinline__ u32 plo(u32 a, u32 b) { return __builtin_amdgcn_perm(b, a, 0x05040100u); }
__device__ __forceinline__ u32 phi(u32 a, u32 b) { return __builtin_amdgcn_perm(b, a, 0x07060302u); }
__device__ __forceinline__ void tr8(const v4u* r, v4u* f) {
#pragma unroll
  for (int d = 0; d < 4; ++d)
#pragma unroll
    for (int e = 0; e < 4; ++e) { f[2 * e][d] = plo(r[2 * d][e], r[2 * d + 1][e]); f[2 * e + 1][d] = phi(r[2 * d][e], r[2 * d + 1][e]); }
}
__device__ __forceinline__ void tr4(const v2u* r, v4u* f) {
#pragma unroll
  for (int d = 0; d < 4; ++d)
#pragma unroll
    for (int e = 0; e < 2; ++e) { f[2 * e][d] = plo(r[2 * d][e], r[2 * d + 1][e]); f[2 * e + 1][d] = phi(r[2 * d][e], r[2 * d + 1][e]); }
}

#define CND(d, o, k, P) "v_cndmask_b32_dpp %" #d ", %" #o ", %" #k ", vcc quad_perm:" P " row_mask:0xf bank_mask:0xf\n\t"
template <int X> __device__ __forceinline__ void quad_stage(const v4u& lo, const v4u& hi, v4u& nlo, v4u& nhi) {
  u32 l0, l1, l2, l3, h0, h1, h2, h3;
  if (X == 1)
    asm volatile("s_nop 1\n\ts_mov_b64 vcc, %16\n\t" CND(0, 12, 8, "[1,0,3,2]") CND(1, 13, 9, "[1,0,3,2]") CND(2, 14, 10, "[1,0,3,2]") CND(3, 15, 11, "[1,0,3,2]")
                 "s_not_b64 vcc, vcc\n\t" CND(4, 8, 12, "[1,0,3,2]") CND(5, 9, 13, "[1,0,3,2]") CND(6, 10, 14, "[1,0,3,2]") CND(7, 11, 15, "[1,0,3,2]") "s_nop 1"
                 : "=&v"(l0), "=&v"(l1), "=&v"(l2), "=&v"(l3), "=&v"(h0), "=&v"(h1), "=&v"(h2), "=&v"(h3)
                 : "v"(lo[0]), "v"(lo[1]), "v"(lo[2]), "v"(lo[3]), "v"(hi[0]), "v"(hi[1]), "v"(hi[2]), "v"(hi[3]), "s"(0x5555555555555555ull) : "vcc", "scc");
  else
    asm volatile("s_nop 1\n\ts_mov_b64 vcc, %16\n\t" CND(0, 12, 8, "[2,3,0,1]") CND(1, 13, 9, "[2,3,0,1]") CND(2, 14, 10, "[2,3,0,1]") CND(3, 15, 11, "[2,3,0,1]")
                 "s_not_b64 vcc, vcc\n\t" CND(4, 8, 12, "[2,3,0,1]") CND(5, 9, 13, "[2,3,0,1]") CND(6, 10, 14, "[2,3,0,1]") CND(7, 11, 15, "[2,3,0,1]") "s_nop 1"
                 : "=&v"(l0), "=&v"(l1), "=&v"(l2), "=&v"(l3), "=&v"(h0), "=&v"(h1), "=&v"(h2), "=&v"(h3)
                 : "v"(lo[0]), "v"(lo[1]), "v"(lo[2]), "v"(lo[3]), "v"(hi[0]), "v"(hi[1]), "v"(hi[2]), "v"(hi[3]), "s"(0x3333333333333333ull) : "vcc", "scc");
  nlo = v4u{l0, l1, l2, l3}; nhi = v4u{h0, h1, h2, h3};
}
__device__ __forceinline__ void quad_tr(v4u* a) {
  v4u b[4];
  quad_stage<1>(a[0], a[1], b[0], b[1]); quad_stage<1>(a[2], a[3], b[2], b[3]);
  quad_stage<2>(b[0], b[2], a[0], a[2]); quad_stage<2>(b[1], b[3], a[1], a[3]);
}
__device__ __forceinline__ u32 umax(u32 a, u32 b) { return a > b ? a : b; }
__device__ __forceinline__ u32 nosent(u32 x) { return x < 0xFFFFFFFEu ? x : 0xFFFFFFFEu; }
__device__ __forceinline__ v4u nosent4(f4 v) { return v4u{nosent(asu(v[0])), nosent(asu(v[1])), nosent(asu(v[2])), nosent(asu(v[3]))}; }
__device__ __forceinline__ u32 mx4(v4u v, u32 m) { return umax(umax(umax(v[0], v[1]), m), umax(v[2], v[3])); }
__device__ __forceinline__ bool none_sent(u32 m) { return __builtin_amdgcn_ballot_w64(m == SENT) == 0; }
#define PIN4(f) asm volatile("" : "+v"((f)[0]), "+v"((f)[1]), "+v"((f)[2]), "+v"((f)[3]))
#define PIN8(f) asm volatile("" : "+v"(f[0]), "+v"(f[1]), "+v"(f[2]), "+v"(f[3]), "+v"(f[4]), "+v"(f[5]), "+v"(f[6]), "+v"(f[7]))
#define CBAR() asm volatile("" ::: "memory")
#define SCHED() __builtin_amdgcn_sched_barrier(0)
#define OPAQUE(x) asm volatile("" : "+s"(x))
template <int N> __device__ __forceinline__ void waitvm() { asm volatile("s_waitcnt vmcnt(%0)" ::"i"(N) : "memory"); }
#define NOUNROLL _Pragma("nounroll")
#define LD128(off, soff) __builtin_amdgcn_raw_buffer_load_b128(rws, off, soff, AUX_SC1)
#define LD64(off, soff) __builtin_amdgcn_raw_buffer_load_b64(rws, off, soff, AUX_SC1)
#define ST128(v, off, soff) __builtin_amdgcn_raw_buffer_store_b128(v, rws, off, soff, AUX_SC1)
#define ST64(v, off, soff) __builtin_amdgcn_raw_buffer_store_b64(v, rws, off, soff, AUX_SC1)
#define BARRIER() do { SCHED(); asm volatile("s_barrier" ::: "memory"); SCHED(); } while (0)

__device__ __forceinline__ u64 sent_mask(u32 m) { return __builtin_amdgcn_ballot_w64(m == SENT); }
#define RELD4(k, so) "s_mov_b64 exec, %[m" #k "]\n\tbuffer_load_dwordx4 %" #k ", %[off], %[rs], " so " offen sc1\n\t"
#define RELD_END "s_mov_b64 exec, -1\n\ts_waitcnt vmcnt(0)"

template <int F, int SLEEP> __device__ __forceinline__ void lds_wait(LAS u32* fl, u32 want) {
  CBAR();
  NOUNROLL for (int n = 0; n < SPIN_LDS; ++n) {
    if ((u32)__builtin_amdgcn_readfirstlane((int)__hip_atomic_load(fl + F, __ATOMIC_RELAXED, __HIP_MEMORY_SCOPE_WORKGROUP)) >= want) break;
    if (SLEEP) __builtin_amdgcn_s_sleep(SLEEP);
  }
  CBAR();
}
template <int F> __device__ __forceinline__ void lds_add(LAS u32* fl) {
  asm volatile("s_mov_b64 exec, 1\n\tds_add_u32 %0, %1 offset:%2\n\ts_mov_b64 exec, -1" ::"v"((u32)(size_t)fl), "v"(1u), "i"(4 * F) : "memory");
}
template <int F> __device__ __forceinline__ u32 lds_add_rtn(LAS u32* fl) {
  u32 old;
  asm volatile("s_mov_b64 exec, 1\n\tds_add_rtn_u32 %0, %1, %2 offset:%3\n\ts_mov_b64 exec, -1\n\ts_waitcnt lgkmcnt(0)"
               : "=v"(old) : "v"((u32)(size_t)fl), "v"(1u), "i"(4 * F) : "memory");
  return (u32)__builtin_amdgcn_readfirstlane((int)old);
}
template <int SLEEP> __device__ __forceinline__ void pace(u32 t0, u32 ticks) {
  NOUNROLL while ((int)((u32)__builtin_amdgcn_s_memrealtime() - t0) < (int)ticks) { if (SLEEP) __builtin_amdgcn_s_sleep(SLEEP); }
}
__device__ __forceinline__ u32 tref_or(LAS u32* fl, u32 own) {
  const u32 t = (u32)__builtin_amdgcn_readfirstlane((int)__hip_atomic_load(fl + F_DT, __ATOMIC_RELAXED, __HIP_MEMORY_SCOPE_WORKGROUP));
  return t ? t : own;
}
__device__ __forceinline__ int sel(bool c, int a, int b) { asm volatile("" : "+v"(a)); return c ? a : b; }

__global__ void __launch_bounds__(1024, 4) fused_decode(const u16* x_a, const u16* kc_a, const u16* vc_a, const u16* wq_a, const u16* wo_a,
                                                     const u16* w1_a, const u16* w3_a, const u16* w2_a, u16* out_a, char* ws_a) {
  __shared__ __attribute__((aligned(16))) char smem[L_END];
  LAS char* L = (LAS char*)smem;
  LAS u32* FL = (LAS u32*)(L + L_FLAG);
  const int bid = blockIdx.x, lane = threadIdx.x & 63, w = __builtin_amdgcn_readfirstlane(threadIdx.x >> 6);
  typedef u32 v16u __attribute__((ext_vector_type(16)));
  const CAS u64* kargs = (const CAS u64*)__builtin_amdgcn_kernarg_segment_ptr();
  v16u ka;
  v4u kb;
  asm volatile("s_load_dwordx16 %0, %2, 0x0\n\ts_load_dwordx4 %1, %2, 0x40" : "=&s"(ka), "=&s"(kb) : "s"(kargs));
  u32 cpf = 0;
  if (w == 3) {
    u64 pc0; asm volatile("s_getpc_b64 %0" : "=s"(pc0));
    int ln = (bid >> 3) + 32 * (lane & 7);
    ln = ln < CODE_LINES ? ln : 0;
    cpf = *(const GAS u32*)((const GAS char*)(pc0 & ~(u64)127) + ln * 128);
    FL[lane] = 0;
  }
  const int xcd = bid & 7, ii = bid >> 3;
  const int kvh = 2 * xcd + (ii >> 4), jj = ii & 15, bb = jj >> 3;
  const bool isY = ii < 18, isX = ii >= 18 && ii < 27;
  const int yi = xcd * 18 + ii, xc = ii - 18;
  const int g4 = lane >> 4, r16 = lane & 15, hb = lane >> 5, l5 = lane & 31;
  const int kw = w & 3;
  const int rot = (bb * 8 + kvh + jj) * 5;
  const int k0 = w * 64 + (r16 >> 3) * 32 + g4 * 8, ka_ = w * 64 + (lane & 1) * 32 + g4 * 8;
  int xo = l5 < 24 ? hb * (D * 2) + l5 * 16 : OOB;
  int xf = sel(ka_ < 144, ((r16 >> 2) & 1) * (D * 2) + ka_ * 2, OOB) + (jj & 3) * 288;
  int qo = sel(k0 < 144, k0 * (QD * 2) + (lane & 7) * 16, OOB) + (jj & 3) * (144 * QD * 2) + kvh * 512 + (jj >> 2) * 128;
  asm volatile("" : "+v"(xo), "+v"(xf), "+v"(qo));
  SCHED();
  asm volatile("s_waitcnt lgkmcnt(0)" : "+s"(ka), "+s"(kb)::"memory");
  SCHED();
#define KPTR(v, i) ((u64)v[2 * (i)] | ((u64)v[2 * (i) + 1] << 32))
  const u16 *x = (const u16*)KPTR(ka, 0), *kc = (const u16*)KPTR(ka, 1), *vc = (const u16*)KPTR(ka, 2), *wq = (const u16*)KPTR(ka, 3),
            *wo = (const u16*)KPTR(ka, 4), *w1 = (const u16*)KPTR(ka, 5), *w3 = (const u16*)KPTR(ka, 6), *w2 = (const u16*)KPTR(ka, 7);
  u16* out = (u16*)KPTR(kb, 0);
  char* ws = (char*)KPTR(kb, 1);
  const u32 tstart = (u32)__builtin_amdgcn_s_memrealtime() + TOFF;
  const rsrc_t rws = mkrs(ws, WS_END);
  const rsrc_t rx = mkrs(x, 2 * D * 2);
#define LOAD_TAG() (*(const CAS u64*)(ws + WS_EPOCH) + 1)
#define LDS_TAG() (*(LAS v2u*)(FL + F_TAG))
#define TREF() tref_or(FL, tstart)

  const v4u s4 = {SENT, SENT, SENT, SENT};
  int yt = -1;
  int v2 = -1;
  v4u xs[4], tq[8];
  if (w < 3) {
#pragma unroll
    for (int i = 0; i < 3; ++i) xs[i] = __builtin_amdgcn_raw_buffer_load_b128(rx, xo + i * 384, 0, 0);
    xs[3] = __builtin_amdgcn_raw_buffer_load_b128(rx, xf, 0, 0);
    const rsrc_t rq = mkrs(wq, D * QD * 2);
#pragma unroll
    for (int i = 0; i < 8; ++i) tq[i] = __builtin_amdgcn_raw_buffer_load_b128(rq, qo, i * (QD * 2), AUX_NT);
  }
  BARRIER();
  if (w == 3) {
    ST128(s4, lane < 40 ? lane * 16 : OOB, WS_OP + (xcd * 32 + ii) * 640);
    if (isY || ii == 31) {
      const rsrc_t rc = mkrs(ws, isY ? WS_QG : WS_MP);
      const int co = isY ? WS_MP + (lane >> 3) * 18432 + yi * 128 + (lane & 7) * 16 : WS_AP + (hb * 8 + xcd) * 512 + l5 * 16;
      const int cs = isY ? 8 * 18432 : 8192;
      NOUNROLL for (int n = 0; n < 5; ++n) __builtin_amdgcn_raw_buffer_store_b128(s4, rc, co, n * cs, AUX_SC1);
    }
    SCHED();
  }
  int w0_ = w;
  OPAQUE(w0_);
  if (w0_ < 3) {
    const u64 tag = LOAD_TAG();
    int qpo = sel((lane & 0x28) == 0, L_QP + w * 512 + g4 * 256 + r16 * 32, L_DUMMY + lane * 32);
    int qso = WS_QG + ((hb * 16 + kvh) * 4 + (jj & 3)) * 2048 + (jj >> 2) * 512 + l5 * 16;
    asm volatile("" : "+v"(qpo), "+v"(qso));
    SCHED();
    float s = 0, v = 0;
#pragma unroll
    for (int i = 0; i < 3; ++i)
#pragma unroll
      for (int e = 0; e < 4; ++e) { s = dot2(xs[i][e], 0x3F803F80u, s); v = dot2(xs[i][e], xs[i][e], v); }
    s += dpp<XOR1>(s); v += dpp<XOR1>(v); s += dpp<XOR2>(s); v += dpp<XOR2>(v);
    s += dpp<HMIR>(s); v += dpp<HMIR>(v); s += dpp<ROR8>(s); v += dpp<ROR8>(v);
    s = x16_sum(s); v = x16_sum(v);
    const float mean = s * (1.f / D);
    const float mb = rbf(mean);
    const float rd = __builtin_amdgcn_rcpf(rbf(__builtin_amdgcn_sqrtf(rbf(rbf(fmaxf(v * (1.f / D) - mean * mean, 0.f)) + 1e-5f))));
    const auto ms = __builtin_amdgcn_permlane32_swap(asu(mb), asu(mb), false, false);
    const auto rs = __builtin_amdgcn_permlane32_swap(asu(rd), asu(rd), false, false);
    const float mm = asf((lane & 4) ? ms[1] : ms[0]), rr = asf((lane & 4) ? rs[1] : rs[0]);
    v4u a;
#pragma unroll
    for (int e = 0; e < 4; ++e) { u32 c = cvtpk(blo(xs[3][e]) - mm, bhi(xs[3][e]) - mm); a[e] = cvtpk(blo(c) * rr, bhi(c) * rr); }
    asm volatile("" : "+v"(a));
    SCHED();
    v4u f[8];
    tr8(tq, f);
    f4 acc[8];
#pragma unroll
    for (int j = 0; j < 8; ++j) acc[j] = mfma(a, f[j], f4{0, 0, 0, 0});
    float o[8];
#pragma unroll
    for (int j = 0; j < 8; ++j) o[j] = acc[j][0] + dpp<ROR8>(acc[j][1]);
    *(LAS f4*)(L + qpo) = f4{o[0], o[1], o[2], o[3]};
    *(LAS f4*)(L + qpo + 16) = f4{o[4], o[5], o[6], o[7]};
    if (lds_add_rtn<F_QCNT>(FL) == 2) {
      const f2 q = (*(LAS f2*)(L + L_QP + lane * 8) + *(LAS f2*)(L + L_QP + 512 + lane * 8)) + *(LAS f2*)(L + L_QP + 1024 + lane * 8);
      const u32 th = (u32)(tag >> 8), tl = (u32)tag & 0xffu;
      ST128((v4u{((asu(q[0]) + 0x80u) & ~0xffu) | tl, th, ((asu(q[1]) + 0x80u) & ~0xffu) | tl, th}), qso, 0);
      const int go = WS_QG + (bb * 16 + kvh) * 8192 + lane * 16;
      v4u g[8];
      u64 m[8];
#pragma unroll
      for (int k = 0; k < 8; ++k) { m[k] = ~0ull; asm volatile("" : "=v"(g[k])); }
      pace<1>((u32)__builtin_amdgcn_s_memrealtime(), QS);
      pace<1>(tstart, TQ);
      NOUNROLL for (int n = 0; n < SPIN_GLB; ++n) {
        asm volatile(RELD4(0, "0") RELD4(1, "%[s1]") RELD4(2, "%[s2]") RELD4(3, "%[s3]") RELD4(4, "%[s4]") RELD4(5, "%[s5]") RELD4(6, "%[s6]")
                     RELD4(7, "%[s7]") RELD_END
                     : "+v"(g[0]), "+v"(g[1]), "+v"(g[2]), "+v"(g[3]), "+v"(g[4]), "+v"(g[5]), "+v"(g[6]), "+v"(g[7])
                     : [off] "v"(go), [rs] "s"(rws), [m0] "s"(m[0]), [m1] "s"(m[1]), [m2] "s"(m[2]), [m3] "s"(m[3]), [m4] "s"(m[4]), [m5] "s"(m[5]),
                       [m6] "s"(m[6]), [m7] "s"(m[7]), [s1] "s"(1024), [s2] "s"(2048), [s3] "s"(3072), [s4] "s"(4096), [s5] "s"(5120),
                       [s6] "s"(6144), [s7] "s"(7168)
                     : "memory");
        u64 any = 0;
#pragma unroll
        for (int k = 0; k < 8; ++k) {
          m[k] = __builtin_amdgcn_ballot_w64(((g[k][1] ^ th) | (g[k][3] ^ th) | (((g[k][0] ^ tl) | (g[k][2] ^ tl)) & 0xffu)) != 0);
          any |= m[k];
        }
        if (any == 0) break;
        __builtin_amdgcn_s_sleep(2);
      }
#pragma unroll
      for (int c = 0; c < 2; ++c) {
        const float a0 = (asf(g[c][0] & ~0xffu) + asf(g[2 + c][0] & ~0xffu)) + (asf(g[4 + c][0] & ~0xffu) + asf(g[6 + c][0] & ~0xffu));
        const float a1 = (asf(g[c][2] & ~0xffu) + asf(g[2 + c][2] & ~0xffu)) + (asf(g[4 + c][2] & ~0xffu) + asf(g[6 + c][2] & ~0xffu));
        *(LAS u32*)(L + L_Q + c * 256 + lane * 4) = cvtpk(a0, a1);
      }
      lds_add<F_QRDY>(FL);
    }
    yt = w;
  }
  int w1_ = w;
  OPAQUE(w1_);
  v4u t[16];
  const rsrc_t rk = mkrs(w < 8 ? kc : vc, 2 * 16 * SK * 256);
  const int kvbase = ((bb * 16 + kvh) * SK + (SK - WIN) + (jj & 7) * 256 + kw * 4) * 256;
  if (w1_ >= 4 && w1_ < 8) {
    const int voff = (r16 >> 2) * 256 + (g4 * 4 + (lane & 3)) * 16 + kvbase;
    const u32 tk = tstart + kw * (DK >> 5);
#pragma unroll
    for (int i = 0; i < 16; ++i) {
      if (i >= KVWN) waitvm<KVWN - 1>();
      pace<0>(tk, TK0 + (i * DK >> 3));
      t[i] = __builtin_amdgcn_raw_buffer_load_b128(rk, voff, ((i + rot) & 15) * 4096, AUX_NT);
      SCHED();
    }
    lds_wait<F_QRDY, 1>(FL, 1);
    v4u qf = *(LAS v4u*)(L + L_Q + ((r16 >> 2) & 1) * 256 + (g4 * 4 + (lane & 3)) * 16);
    const v4u z4 = {0, 0, 0, 0};
    const v4u q0 = r16 < 8 ? qf : z4, q1 = r16 < 8 ? z4 : qf;
    float sc[8];
#pragma unroll
    for (int m = 0; m < 8; ++m) {
      f4 d = mfma(t[m], q0, f4{0, 0, 0, 0});
      d = mfma(t[m + 8], q1, d);
      sc[m] = dpp<0xFF>(d[3]) + (dpp<0xAA>(d[2]) + (dpp<0x55>(d[1]) + dpp<0x00>(d[0])));
    }
    const bool b0 = lane & 1, b1 = lane & 2;
    float s0 = b1 ? (b0 ? sc[6] : sc[4]) : (b0 ? sc[2] : sc[0]);
    float s1 = b1 ? (b0 ? sc[7] : sc[5]) : (b0 ? sc[3] : sc[1]);
    u32 pk = cvtpk(s0, s1);
    pk = cvtpk(blo(pk) * 0.08838834764831845f, bhi(pk) * 0.08838834764831845f);
    s0 = blo(pk) * 1.4426950408889634f; s1 = bhi(pk) * 1.4426950408889634f;
    float mx = fmaxf(s0, s1);
    mx = fmaxf(mx, dpp<XOR1>(mx)); mx = fmaxf(mx, dpp<XOR2>(mx)); mx = fmaxf(mx, dpp<ROR8>(mx));
    mx = x32_max(x16_max(mx));
    const int hd = (r16 >> 2) & 1;
    LAS float* MX = (LAS float*)(L + L_MX);
    const float p0 = ex2(s0 - mx), p1 = ex2(s1 - mx);
    *(LAS u32*)(L + L_P + kw * 256 + hd * 128 + (r16 >> 3) * 64 + g4 * 16 + (lane & 3) * 4) = cvtpk(p0, p1);
    float ls = p0 + p1;
    ls += dpp<XOR1>(ls); ls += dpp<XOR2>(ls); ls += dpp<ROR8>(ls);
    ls = x32_sum(x16_sum(ls));
    MX[hd * 4 + kw] = mx; MX[8 + hd * 4 + kw] = ls;
    lds_add<F_PCNT>(FL);
    if (isX && kw == 0) {
      const int z = xcd * 9 + ii - 18, zl = z >> 1, zb = zl / 18, zz = zl % 18;
      const int col = (zz >> 1) * 64 + (z & 1) * 32 + (lane & 3) * 8 + (zz & 1) * 4;
      const int jz = (g4 >> 1) + 2 * (g4 & 1);
      const u32 xv = __builtin_amdgcn_raw_buffer_load_b16(rx, (zb * D + col + jz) * 2, 0, 0);
      const int ho = sel(lane < 32, WS_AP + ((zz >> 1) * 8 + (lane >> 2)) * 512 + (lane & 3) * 128 + zb * 64 + (zz & 1) * 32 + (z & 1) * 16, OOB);
      int oo = sel(r16 < 4, (zb * D + col + jz) * 2, OOB);
      const int mo = WS_MP + zl * 18432 + (lane >> 2) * 128 + (z & 1) * 64 + (lane & 3) * 16;
      lds_wait<F_XCNT, 2>(FL, 5);
      v4u hv;
      NOUNROLL for (int n = 0; n < SPIN_GLB; ++n) {
        hv = LD128(ho, 0);
        CBAR();
        if (none_sent(mx4(hv, 0))) break;
        __builtin_amdgcn_s_sleep(4);
      }
      float h = t16_sum(t32_sum(asf(hv[0]), asf(hv[1])), t32_sum(asf(hv[2]), asf(hv[3])));
      h += dpp<ROR4>(h); h += dpp<ROR8>(h);
      h = rbf(blo(xv) + rbf(h));
      asm volatile("" : "+v"(oo), "+v"(h));
      v4u p[9];
      u64 m[9];
      f4 sum;
#pragma unroll
      for (int k = 0; k < 9; ++k) { m[k] = ~0ull; asm volatile("" : "=v"(p[k])); }
      NOUNROLL for (int n = 0; n < SPIN_GLB; ++n) {
        asm volatile(RELD4(0, "0") RELD4(1, "%[s1]") RELD4(2, "%[s2]") RELD4(3, "%[s3]") RELD4(4, "%[s4]") RELD4(5, "%[s5]") RELD4(6, "%[s6]")
                     RELD4(7, "%[s7]") RELD4(8, "%[s8]") RELD_END
                     : "+v"(p[0]), "+v"(p[1]), "+v"(p[2]), "+v"(p[3]), "+v"(p[4]), "+v"(p[5]), "+v"(p[6]), "+v"(p[7]), "+v"(p[8])
                     : [off] "v"(mo), [rs] "s"(rws), [m0] "s"(m[0]), [m1] "s"(m[1]), [m2] "s"(m[2]), [m3] "s"(m[3]), [m4] "s"(m[4]), [m5] "s"(m[5]),
                       [m6] "s"(m[6]), [m7] "s"(m[7]), [m8] "s"(m[8]), [s1] "s"(2048), [s2] "s"(4096), [s3] "s"(6144), [s4] "s"(8192),
                       [s5] "s"(10240), [s6] "s"(12288), [s7] "s"(14336), [s8] "s"(16384)
                     : "memory");
        sum = (((asf4(p[0]) + asf4(p[1])) + (asf4(p[2]) + asf4(p[3]))) + ((asf4(p[4]) + asf4(p[5])) + (asf4(p[6]) + asf4(p[7])))) + asf4(p[8]);
        if (!any_nan(sum)) break;
        u64 any = 0, all = ~0ull;
#pragma unroll
        for (int k = 0; k < 9; ++k) { m[k] = sent_mask(mx4(p[k], 0)); any |= m[k]; all &= m[k]; }
        if (any == 0) break;
        if (all == ~0ull) __builtin_amdgcn_s_sleep(4);
      }
      float r = t16_sum(t32_sum(sum[0], sum[1]), t32_sum(sum[2], sum[3]));
      r += dpp<ROR4>(r); r += dpp<ROR8>(r);
      r = h + rbf(r);
      __builtin_amdgcn_raw_buffer_store_b16((u16)cvtpk(r, r), mkrs(out, 2 * D * 2), oo, 0, 0);
      if (z == 0) __builtin_amdgcn_raw_buffer_store_b64(LDS_TAG(), rws, lane == 0 ? WS_EPOCH : OOB, 0, 0);
    }
    if (kw == 0) yt = 4;
    if (kw == 2) yt = 5;
    if (kw == 1) v2 = 4;
  }
  int w2_ = w;
  OPAQUE(w2_);
  if (w2_ >= 8 && w2_ < 12) {
    const int voff = lane * 16 + kvbase;
    u32 tk = tstart + kw * (DV >> 5);
    pace<2>(tk, TK0 + (15 * DK >> 3));
    int dt = 0;
#pragma unroll
    for (int i = 0; i < 16; ++i) {
      if (i == VSYNC) dt = (int)(TREF() - tstart);
      if (i >= KVWN) waitvm<KVWN - 1>();
      pace<0>(tk, TK0 + 2 * DK + (i * DV >> 3) + (i >= VSYNC ? dt * (i - VSYNC + 1) / (16 - VSYNC) : 0));
      t[i] = __builtin_amdgcn_raw_buffer_load_b128(rk, voff, ((i + rot) & 15) * 4096, AUX_NT);
      SCHED();
    }
    lds_add<F_VISS>(FL);
    v4u f[8];
    tr8(t, f);
    PIN8(f);
    lds_wait<F_PCNT, 1>(FL, 4);
    const v4u pa = *(LAS v4u*)(L + L_P + kw * 256 + ((r16 >> 2) & 1) * 128 + g4 * 16);
    const v4u pb = *(LAS v4u*)(L + L_P + kw * 256 + ((r16 >> 2) & 1) * 128 + 64 + g4 * 16);
    const f4 m4 = *(LAS f4*)(L + L_MX + (g4 & 1) * 16), l4 = *(LAS f4*)(L + L_MX + 32 + (g4 & 1) * 16);
    const float mw = *(LAS float*)(L + L_MX + (g4 & 1) * 16 + kw * 4);
    f4 acc[8];
#pragma unroll
    for (int j = 0; j < 8; ++j) acc[j] = mfma(pa, f[j], f4{0, 0, 0, 0});
    const float M = fmaxf(fmaxf(m4[0], m4[1]), fmaxf(m4[2], m4[3]));
    float fs = ex2(mw - M);
    v2u ml = {nosent(asu(M)), nosent(asu((l4[0] * ex2(m4[0] - M) + l4[1] * ex2(m4[1] - M)) + (l4[2] * ex2(m4[2] - M) + l4[3] * ex2(m4[3] - M))))};
    int mlo = sel((lane & 0x2F) == 0, WS_OP + (xcd * 32 + ii) * 640 + 512 + g4 * 8, OOB);
    int opo = WS_OP + (xcd * 32 + ii) * 640 + lane * 8;
    int lo = L_O + kw * 1024 + (g4 & 1) * 512 + r16 * 32 + (g4 >> 1) * 16;
    asm volatile("" : "+v"(fs), "+v"(ml), "+v"(mlo), "+v"(opo), "+v"(lo));
    lds_wait<F_CLR, 1>(FL, 1);
    SCHED();
    tr8(t + 8, f);
#pragma unroll
    for (int j = 0; j < 8; ++j) acc[j] = mfma(pb, f[j], acc[j]);
    {
      const bool hi = g4 >= 2;
      f4 o = {hi ? acc[4][0] : acc[0][0], hi ? acc[5][0] : acc[1][0], hi ? acc[6][0] : acc[2][0], hi ? acc[7][0] : acc[3][0]};
      *(LAS f4*)(L + lo) = o * fs;
    }
    if (lds_add_rtn<F_OCNT>(FL) == 3) {
      ST64(ml, mlo, 0);
      f4 o = (*(LAS f4*)(L + L_O + lane * 16) + *(LAS f4*)(L + L_O + 1024 + lane * 16)) +
             (*(LAS f4*)(L + L_O + 2048 + lane * 16) + *(LAS f4*)(L + L_O + 3072 + lane * 16));
      ST64((v2u{nosent(cvtpk(o[0], o[1])), nosent(cvtpk(o[2], o[3]))}), opo, 0);
      lds_add<F_OCNT>(FL);
    }
    v2 = kw;
  }
  int w3_ = w;
  OPAQUE(w3_);
  if (w3_ == 3) {
    const u64 tag = LOAD_TAG();
    asm volatile("s_waitcnt vmcnt(0)" ::"v"(cpf) : "memory");
    const v2u tg = {(u32)tag, (u32)(tag >> 32)};
    ST128((v4u{tg[0], tg[1], tstart, 0}), lane == 0 ? WS_FLAGS + bid * 128 : OOB, 0);
    *(LAS v2u*)(FL + F_TAG) = tg;
    lds_add<F_CLR>(FL);
    {
      const bool act = lane < 32 ? isX : lane < 40;
      const int fo = sel(act, WS_FLAGS + (lane < 32 ? lane * 8 + xcd : 248 + lane - 32) * 128, OOB);
      pace<4>(tstart, TFL);
      v4u g;
      NOUNROLL for (int n = 0; n < SPIN_GLB; ++n) {
        g = LD128(fo, 0);
        CBAR();
        const bool done = __builtin_amdgcn_ballot_w64(act && ((g[0] ^ tg[0]) | (g[1] ^ tg[1])) != 0) == 0;
        if (done) break;
        __builtin_amdgcn_s_sleep(12);
      }
      int d = (int)(g[2] - tstart);
      d = d > 100 ? 100 : d < -100 ? -100 : d;
      d += (int)dppu<XOR1>(d); d += (int)dppu<XOR2>(d); d += (int)dppu<HMIR>(d);
      FL[F_DT] = tstart + (__builtin_amdgcn_readlane(d, 32) >> 3);
      lds_add<F_INOK>(FL);
    }
    if (!isY) {
      constexpr int NPP = (81 - 9 * NPX + 4) / 5;
      const rsrc_t rp = mkrs(w2, xcd * (576 * D * 2) + (isX ? 81 : 81 - 9 * NPX) * 8192);
      const int po = lane * 128 + xcd * (576 * D * 2) + (isX ? 81 - 9 * NPX + xc : ii - 27) * 8192;
      const int step = isX ? 9 * 8192 : 5 * 8192;
      if (isX) lds_wait<F_XCNT, 1>(FL, 5); else lds_wait<F_BI, 2>(FL, 1);
      pace<1>(TREF(), TW2);
      u32 pf[NPP];
#pragma unroll
      for (int k = 0; k < NPP; ++k) {
        if (k >= PWIN) waitvm<PWIN - 1>();
        pf[k] = __builtin_amdgcn_raw_buffer_load_b32(rp, po, k * step, 0);
        SCHED();
      }
#pragma unroll
      for (int k = 0; k < NPP; ++k) asm volatile("" ::"v"(pf[k]));
    }
    yt = 3;
  } else if (w3_ >= 12) {
    if (isX) {
      const int hh = 4 * xcd + kw;
      const rsrc_t ro = mkrs(wo, QD * D * 2);
      const int vo = (hh * 128 + g4 * 32) * (D * 2) + (xc * 64 + r16 * 4) * 2;
      int xo = L_X + kw * 512 + ((r16 >> 1) & 3) * 128 + (g4 >> 1) * 64 + (r16 & 1) * 32 + (r16 >> 3) * 16;
      int po = sel(lane < 32, WS_AP + (xc * 8 + xcd) * 512 + lane * 16, OOB);
      const int ao = WS_OP + ((hh >> 1) * 16 + (r16 & 12)) * 640 + (hh & 1) * 256 + (g4 * 4 + (lane & 3)) * 16;
      const int mo = sel(r16 < 8, WS_OP + ((hh >> 1) * 16 + (g4 >> 1) * 8 + ((r16 + 4 * (g4 & 1)) & 7)) * 640 + 512 + (hh & 1) * 8, OOB);
      asm volatile("" : "+v"(xo), "+v"(po));
      lds_wait<F_VISS, 1>(FL, 4);
      const u32 tr = TREF();
      pace<1>(tr, TXB + kw * DXB);
      v2u u[32];
#pragma unroll
      for (int i = 0; i < 32; ++i) u[i] = __builtin_amdgcn_raw_buffer_load_b64(ro, vo, i * (D * 2), AUX_NT);
      SCHED();
      pace<1>(tr, TXO);
      lds_wait<F_INOK, 1>(FL, 1);
      v4u a[4] = {LD128(ao, 0), LD128(ao + 640, 0), LD128(ao + 1280, 0), LD128(ao + 1920, 0)};
      v2u ml = LD64(mo, 0);
      SCHED();
      v4u f[16];
#pragma unroll
      for (int k = 0; k < 4; ++k) { tr4(u + 8 * k, f + 4 * k); PIN4(f + 4 * k); }
      NOUNROLL for (int n = 0; n < SPIN_GLB; ++n) {
        if (none_sent(mx4(a[3], mx4(a[2], mx4(a[1], mx4(a[0], umax(ml[0], ml[1]))))))) break;
        u64 m[4];
#pragma unroll
        for (int k = 0; k < 4; ++k) m[k] = sent_mask(mx4(a[k], 0));
        const u64 mm = sent_mask(umax(ml[0], ml[1]));
        asm volatile("s_mov_b64 exec, %[mm]\n\tbuffer_load_dwordx2 %4, %[mo], %[rs], 0 offen sc1\n\t"
                     "s_mov_b64 exec, %[m0]\n\tbuffer_load_dwordx4 %0, %[off], %[rs], 0 offen sc1\n\t"
                     "s_mov_b64 exec, %[m1]\n\tbuffer_load_dwordx4 %1, %[off], %[rs], 0 offen offset:640 sc1\n\t"
                     "s_mov_b64 exec, %[m2]\n\tbuffer_load_dwordx4 %2, %[off], %[rs], 0 offen offset:1280 sc1\n\t"
                     "s_mov_b64 exec, %[m3]\n\tbuffer_load_dwordx4 %3, %[off], %[rs], 0 offen offset:1920 sc1\n\t" RELD_END
                     : "+v"(a[0]), "+v"(a[1]), "+v"(a[2]), "+v"(a[3]), "+v"(ml)
                     : [off] "v"(ao), [mo] "v"(mo), [rs] "s"(rws), [m0] "s"(m[0]), [m1] "s"(m[1]), [m2] "s"(m[2]), [m3] "s"(m[3]), [mm] "s"(mm)
                     : "memory");
      }
      quad_tr(a);
      f4 d[4];
#pragma unroll
      for (int j = 0; j < 4; ++j) d[j] = mfma(a[0], f[j], f4{0, 0, 0, 0});
#pragma unroll
      for (int k = 1; k < 4; ++k)
#pragma unroll
        for (int j = 0; j < 4; ++j) d[j] = mfma(a[k], f[4 * k + j], d[j]);
      float M = asf(ml[0]);
      M = fmaxf(M, dpp<XOR1>(M)); M = fmaxf(M, dpp<XOR2>(M)); M = fmaxf(M, dpp<HMIR>(M));
      const float ee = ex2(asf(ml[0]) - M);
      float Ls = ee * asf(ml[1]);
      Ls += dpp<XOR1>(Ls); Ls += dpp<XOR2>(Ls); Ls += dpp<HMIR>(Ls);
      const float wr = ee * __builtin_amdgcn_rcpf(Ls);
      const float e[4] = {dpp<0x150>(wr), dpp<0x151>(wr), dpp<0x152>(wr), dpp<0x153>(wr)};
      f4 o;
#pragma unroll
      for (int j = 0; j < 4; ++j) o[j] = x16_sum((e[0] * d[j][0] + e[1] * d[j][1]) + (e[2] * d[j][2] + e[3] * d[j][3]));
      *(LAS f4*)(L + xo) = o;
      if (lds_add_rtn<F_XCNT>(FL) == 3) {
        LAS char* rp = L + L_X + l5 * 16;
        const f4 s = (*(LAS f4*)(rp) + *(LAS f4*)(rp + 512)) + (*(LAS f4*)(rp + 1024) + *(LAS f4*)(rp + 1536));
        ST128(nosent4(s), po, 0);
        lds_add<F_XCNT>(FL);
      }
    } else if (w == 15) {
      const int pi = isY ? ii : ii - 9;
      const int br = lane / 9;
      const int band = sel(lane < 63, br * (FF * 2) + (lane - br * 9) * 128, OOB) + xcd * 1152;
      const rsrc_t ro = mkrs(wo, (xcd + 1) * (512 * D * 2));
      const int oo = lane * 128 + xcd * (512 * D * 2) + (22 - pi) * 8192;
      u32 pf[12];
      pace<4>(tstart, TWO - 150);
      const u32 tp = TREF() + (pi % 6 - 3) * DITH;
#pragma unroll
      for (int k = 0; k < 4; ++k) {
        if (k < 3) pace<0>(tp, TWO + k * DWO);
        pf[k] = __builtin_amdgcn_raw_buffer_load_b32(ro, oo, k * 23 * 8192, 0);
        SCHED();
      }
      lds_wait<F_VISS, 1>(FL, 4);
#pragma unroll
      for (int k = 0; k < 8; ++k) {
        const int m = 22 - pi + 23 * k;
        if (k < 7 || !isY) {
          waitvm<PWIN - 1>();
          pace<0>(tp, TPF + (k < 7 ? k : 6) * DPF);
          const bool second = k > 3 || (k == 3 && m >= 83);
          pf[4 + k] = __builtin_amdgcn_raw_buffer_load_b32(mkrs(second ? w3 : w1, D * FF * 2), band, (second ? m - 83 : m) * (7 * FF * 2), 0);
        } else pf[4 + k] = 0;
        SCHED();
      }
      if (!isY) lds_add<F_BI>(FL);
      lds_wait<F_OCNT, 1>(FL, 5);
      lds_add<F_PF>(FL);
#pragma unroll
      for (int k = 0; k < 12; ++k) asm volatile("" ::"v"(pf[k]));
    }
    yt = w < 15 ? w - 6 : -1;
  }
  if (isY) {
    if (yt >= 0) {
      const GAS char* wp = (const GAS char*)((lane & 4) ? w3 : w1) + (size_t)((yt * 64 + (r16 >> 3) * 32 + g4 * 8) * (FF * 2) + (yi * 32 + (lane & 3) * 8) * 2);
      const int ao = WS_AP + (yt * 8 + (r16 >> 3)) * 512 + g4 * 128 + (lane & 7) * 16;
      int ypo = sel(r16 < 8, L_R + yt * 1024 + ((lane & 3) * 16 + g4 * 4 + (r16 >> 2) * 2) * 16, L_DUMMY + lane * 32);
      int sto = sel(lane == 0 || lane == 8 || lane == 20 || lane == 24, L_ST + ((r16 == 8 ? 0 : 2) + g4) * 64 + yt * 4, L_DUMMY + lane * 32);
      asm volatile("" : "+v"(ypo), "+v"(sto), "+v"(wp));
      v2u xv = __builtin_amdgcn_raw_buffer_load_b64(rx, (((lane >> 2) & 1) * D + yt * 64 + (lane & 1) * 32 + g4 * 8 + ((lane >> 1) & 1) * 4) * 2, 0, 0);
      SCHED();
      lds_wait<F_INOK, 2>(FL, 1);
      const u32 tr = TREF();
      lds_wait<F_PF, 1>(FL, 1);
#pragma unroll
      for (int i = 0; i < 8; ++i) t[i] = __builtin_nontemporal_load((const GAS v4u*)(wp + i * (FF * 2)));
      SCHED();
      lds_add<F_BI>(FL);
      const bool ride = (u32)__builtin_amdgcn_s_memrealtime() - tr >= (u32)TAP;
      const int ar = ride ? ao : OOB;
      v4u p[4] = {LD128(ar, 0), LD128(ar + 1024, 0), LD128(ar + 2048, 0), LD128(ar + 3072, 0)};
      SCHED();
      v4u f[8];
      tr8(t, f);
      PIN8(f);
      if (!ride) p[0] = p[1] = p[2] = p[3] = v4u{SENT, SENT, SENT, SENT};
      f4 hs;
      NOUNROLL for (int n = 0; n < SPIN_GLB; ++n) {
        hs = (asf4(p[0]) + asf4(p[1])) + (asf4(p[2]) + asf4(p[3]));
        if (!any_nan(hs)) break;
        u64 m[4];
#pragma unroll
        for (int k = 0; k < 4; ++k) m[k] = sent_mask(mx4(p[k], 0));
        if ((m[0] | m[1] | m[2] | m[3]) == 0) break;
        if (n == 0) { pace<1>(tr, TAP); if (ride) lds_wait<F_BI, 1>(FL, 9); }
        else __builtin_amdgcn_s_sleep(3);
        asm volatile("s_mov_b64 exec, %[m0]\n\tbuffer_load_dwordx4 %0, %[off], %[rs], 0 offen sc1\n\t"
                     "s_mov_b64 exec, %[m1]\n\tbuffer_load_dwordx4 %1, %[off], %[rs], 0 offen offset:1024 sc1\n\t"
                     "s_mov_b64 exec, %[m2]\n\tbuffer_load_dwordx4 %2, %[off], %[rs], 0 offen offset:2048 sc1\n\t"
                     "s_mov_b64 exec, %[m3]\n\tbuffer_load_dwordx4 %3, %[off], %[rs], 0 offen offset:3072 sc1\n\t" RELD_END
                     : "+v"(p[0]), "+v"(p[1]), "+v"(p[2]), "+v"(p[3])
                     : [off] "v"(ao), [rs] "s"(rws), [m0] "s"(m[0]), [m1] "s"(m[1]), [m2] "s"(m[2]), [m3] "s"(m[3])
                     : "memory");
      }
      float h[4];
#pragma unroll
      for (int j = 0; j < 4; ++j) h[j] = hs[j] + dpp<ROR8>(hs[j]);
      const u32 c0 = cvtpk(h[0], h[1]), c1 = cvtpk(h[2], h[3]);
      const u32 h0 = cvtpk(blo(xv[0]) + blo(c0), bhi(xv[0]) + bhi(c0)), h1 = cvtpk(blo(xv[1]) + blo(c1), bhi(xv[1]) + bhi(c1));
      const u32 h2 = dppu<XOR2>(h0), h3 = dppu<XOR2>(h1);
      const bool one = lane & 8;
      const v4u a = {one ? 0x3F803F80u : h0, one ? 0x3F803F80u : h1, one ? 0x3F803F80u : h2, one ? 0x3F803F80u : h3};
      f4 acc[8];
#pragma unroll
      for (int j = 0; j < 8; ++j) acc[j] = mfma(a, f[j], f4{0, 0, 0, 0});
      const f4 gm = mfma(a, a, f4{0, 0, 0, 0});
      float o[8];
#pragma unroll
      for (int j = 0; j < 8; ++j) o[j] = acc[j][0] + dpp<ROR8>(acc[j][1]);
      *(LAS f4*)(L + ypo) = f4{o[0], o[1], o[2], o[3]};
      *(LAS f4*)(L + ypo + 16) = f4{o[4], o[5], o[6], o[7]};
      *(LAS float*)(L + sto) = gm[0] + dpp<SHL1>(gm[1]);
      lds_add<F_RCNT>(FL);
    } else if (v2 >= 0) {
      const rsrc_t r2 = mkrs(w2, FF * D * 2);
      const int vo = sel(v2 < 4 || r16 < 8, (yi * 32 + g4 * 8) * (D * 2) + (v2 * 128 + r16 * 8) * 2, OOB);
      int mo = sel(g4 < 2 && (v2 < 4 || r16 < 8), WS_MP + (g4 * 18 + v2 * 4 + (r16 >> 3) * 2) * 18432 + yi * 128 + (lane & 7) * 16, OOB);
      int sto = sel(r16 < 9, L_ST + lane * 4, L_FLAG + 4 * F_ZERO);
      asm volatile("" : "+v"(mo), "+v"(sto));
      lds_wait<F_BI, 1>(FL, 9);
#pragma unroll
      for (int i = 0; i < 8; ++i) t[i] = __builtin_amdgcn_raw_buffer_load_b128(r2, vo, i * (D * 2), AUX_NT);
      SCHED();
      v4u f[8];
      tr8(t, f);
      PIN8(f);
      lds_wait<F_RCNT, 2>(FL, 1);
      lds_wait<F_RCNT, 0>(FL, 9);
      float st = *(LAS float*)(L + sto);
      f4 y[9];
#pragma unroll
      for (int k = 0; k < 9; ++k) y[k] = *(LAS f4*)(L + L_R + k * 1024 + lane * 16);
      SCHED();
      const f4 s = (((y[0] + y[1]) + (y[2] + y[3])) + ((y[4] + y[5]) + (y[6] + y[7]))) + y[8];
      st += dpp<XOR1>(st); st += dpp<XOR2>(st); st += dpp<HMIR>(st); st += dpp<ROR8>(st);
      const float a0 = asf(__builtin_amdgcn_readlane(asu(st), 0)), a1 = asf(__builtin_amdgcn_readlane(asu(st), 16));
      const float q0 = asf(__builtin_amdgcn_readlane(asu(st), 32)), q1 = asf(__builtin_amdgcn_readlane(asu(st), 48));
      const float mean = ((lane & 4) ? a1 : a0) * (1.f / D);
      const float var = ((lane & 4) ? q1 : q0) * (1.f / D) - mean * mean;
      const float rs = __builtin_amdgcn_rsqf(fmaxf(var, 0.f) + 1e-5f);
      float hid[4];
#pragma unroll
      for (int j = 0; j < 4; ++j) {
        const float zv = rs * (s[j] - mean * dpp<ROR8>(s[j]));
        const float up = dpp<XOR2>(zv);
        hid[j] = zv * __builtin_amdgcn_rcpf(1.f + ex2(-1.4426950408889634f * zv)) * up;
      }
      const u32 h0 = cvtpk(hid[0], hid[1]), h1 = cvtpk(hid[2], hid[3]);
      const v4u a = {h0, h1, dppu<XOR1>(h0), dppu<XOR1>(h1)};
      f4 acc[8];
#pragma unroll
      for (int j = 0; j < 8; ++j) acc[j] = mfma(a, f[j], f4{0, 0, 0, 0});
      ST128(nosent4(f4{acc[0][0], acc[1][0], acc[2][0], acc[3][0]}), mo, 0);
      ST128(nosent4(f4{acc[4][0], acc[5][0], acc[6][0], acc[7][0]}), mo, 18432);
    }
  }
}

extern "C" void kernel_launch(void* const* d_in, const int* in_sizes, int n_in, void* d_out, int out_size, void* d_ws, size_t ws_size,
                              hipStream_t stream) {
  fused_decode<<<256, 1024, 0, stream>>>((const u16*)d_in[0], (const u16*)d_in[1], (const u16*)d_in[2], (const u16*)d_in[3],
                                         (const u16*)d_in[4], (const u16*)d_in[6], (const u16*)d_in[7], (const u16*)d_in[8], (u16*)d_out,
                                         (char*)d_ws);
}
```

```cpp
#include <hip/hip_runtime.h>
#include <hip/hip_bf16.h>
#include <stdint.h>

#define GAS __attribute__((address_space(1)))
#define LAS __attribute__((address_space(3)))
#define CAS __attribute__((address_space(4)))
typedef unsigned u32;
typedef unsigned long long u64;
typedef unsigned short u16;
typedef u32 v4u __attribute__((ext_vector_type(4)));
typedef u32 v2u __attribute__((ext_vector_type(2)));
typedef float f4 __attribute__((ext_vector_type(4)));
typedef float f2 __attribute__((ext_vector_type(2)));
typedef __bf16 bf8 __attribute__((ext_vector_type(8)));
typedef __bf16 bf2 __attribute__((ext_vector_type(2)));
typedef __amdgpu_buffer_rsrc_t rsrc_t;

constexpr int TOFF = -10;
constexpr int TK0 = 60, DK = 120, DV = 120;
constexpr int TVE = TK0 + 2 * DK + 2 * DV;
constexpr int DWO = 22, DPF = 22;
constexpr int MINSP = 11, PMIN = 16;
constexpr int KREP = 13, VREP = 14;
constexpr int TWO = TVE + 7, TPF = TWO + 3 + 3 * DWO, TW2 = TPF + 7 * DPF + 7;
constexpr int TXB = TVE + 67, DXB = 21, TXO = TVE + 127;
constexpr int TAP = TVE + 342;
constexpr int TQ = 250, QS = 50, TFL = 300;
constexpr int VSYNC = 6, DITH = 4;
constexpr int KVWN = 8, PWIN = 4, NPX = 4;
constexpr int CODE_LINES = 150;

constexpr int D = 576, SK = 8192, WIN = 2048, FF = 4608, QD = 4096;
constexpr int WS_EPOCH = 0, WS_FLAGS = 4096, WS_OP = WS_FLAGS + 256 * 128, WS_AP = WS_OP + 256 * 640,
              WS_MP = WS_AP + 36864, WS_QG = WS_MP + 663552, WS_END = WS_QG + 262144;
constexpr u32 SENT = 0xFFFFFFFFu;
constexpr int AUX_NT = 2, AUX_SC1 = 16, OOB = 1 << 30;
constexpr int SPIN_LDS = 1 << 20, SPIN_GLB = 1 << 17;
constexpr int L_FLAG = 0;
constexpr int L_Q = 256;
constexpr int L_P = 768;
constexpr int L_MX = 1792;
constexpr int L_ST = 1920;
constexpr int L_QP = 2304;
constexpr int L_O = 4096;
constexpr int L_X = 8192;
constexpr int L_R = 10240;
constexpr int L_DUMMY = L_R + 9216;
constexpr int L_END = L_DUMMY + 2048;
enum { F_QCNT = 0, F_QRDY, F_PCNT, F_OCNT, F_VISS, F_CLR, F_INOK, F_PF, F_RCNT, F_XCNT, F_ZERO, F_BI, F_DT, F_KISS, F_VNEAR, F_TAG = 16 };

__device__ __forceinline__ rsrc_t mkrs(const void* p, int bytes) { return __builtin_amdgcn_make_buffer_rsrc((void*)p, 0, bytes, 0x00020000); }
__device__ __forceinline__ float asf(u32 u) { return __uint_as_float(u); }
__device__ __forceinline__ u32 asu(float f) { return __float_as_uint(f); }
__device__ __forceinline__ u32 cvtpk(float lo, float hi) { return __builtin_bit_cast(u32, __builtin_convertvector(f2{lo, hi}, bf2)); }
__device__ __forceinline__ float rbf(float f) { return asf(cvtpk(f, f) & 0xffff0000u); }
__device__ __forceinline__ float blo(u32 u) { return asf(u << 16); }
__device__ __forceinline__ float bhi(u32 u) { return asf(u & 0xffff0000u); }
__device__ __forceinline__ float ex2(float x) { return __builtin_amdgcn_exp2f(x); }
__device__ __forceinline__ float dot2(u32 a, u32 b, float c) { return __builtin_amdgcn_fdot2_f32_bf16(__builtin_bit_cast(bf2, a), __builtin_bit_cast(bf2, b), c, false); }
template <int C> __device__ __forceinline__ u32 dppu(u32 x) { return (u32)__builtin_amdgcn_mov_dpp((int)x, C, 0xf, 0xf, true); }
template <int C> __device__ __forceinline__ float dpp(float x) { return asf(dppu<C>(asu(x))); }
constexpr int XOR1 = 0xB1, XOR2 = 0x4E, HMIR = 0x141, ROR4 = 0x124, ROR8 = 0x128, SHL1 = 0x101;
__device__ __forceinline__ float x16_sum(float x) { auto s = __builtin_amdgcn_permlane16_swap(asu(x), asu(x), false, false); return asf(s[0]) + asf(s[1]); }
__device__ __forceinline__ float x32_sum(float x) { auto s = __builtin_amdgcn_permlane32_swap(asu(x), asu(x), false, false); return asf(s[0]) + asf(s[1]); }
__device__ __forceinline__ float x16_max(float x) { auto s = __builtin_amdgcn_permlane16_swap(asu(x), asu(x), false, false); return fmaxf(asf(s[0]), asf(s[1])); }
__device__ __forceinline__ float x32_max(float x) { auto s = __builtin_amdgcn_permlane32_swap(asu(x), asu(x), false, false); return fmaxf(asf(s[0]), asf(s[1])); }
__device__ __forceinline__ float t32_sum(float a, float b) { auto s = __builtin_amdgcn_permlane32_swap(asu(a), asu(b), false, false); return asf(s[0]) + asf(s[1]); }
__device__ __forceinline__ float t16_sum(float a, float b) { auto s = __builtin_amdgcn_permlane16_swap(asu(a), asu(b), false, false); return asf(s[0]) + asf(s[1]); }
__device__ __forceinline__ f4 asf4(v4u v) { return __builtin_bit_cast(f4, v); }
__device__ __forceinline__ bool any_nan(f4 v) { return __builtin_amdgcn_ballot_w64(__builtin_isunordered(v[0], v[1]) || __builtin_isunordered(v[2], v[3])) != 0; }
__device__ __forceinline__ f4 mfma(v4u a, v4u b, f4 c) { return __builtin_amdgcn_mfma_f32_16x16x32_bf16(__builtin_bit_cast(bf8, a), __builtin_bit_cast(bf8, b), c, 0, 0, 0); }
__device__ __forceinline__ u32 plo(u32 a, u32 b) { return __builtin_amdgcn_perm(b, a, 0x05040100u); }
__device__ __forceinline__ u32 phi(u32 a, u32 b) { return __builtin_amdgcn_perm(b, a, 0x07060302u); }
__device__ __forceinline__ void tr8(const v4u* r, v4u* f) {
#pragma unroll
  for (int d = 0; d < 4; ++d)
#pragma unroll
    for (int e = 0; e < 4; ++e) { f[2 * e][d] = plo(r[2 * d][e], r[2 * d + 1][e]); f[2 * e + 1][d] = phi(r[2 * d][e], r[2 * d + 1][e]); }
}
__device__ __forceinline__ void tr4(const v2u* r, v4u* f) {
#pragma unroll
  for (int d = 0; d < 4; ++d)
#pragma unroll
    for (int e = 0; e < 2; ++e) { f[2 * e][d] = plo(r[2 * d][e], r[2 * d + 1][e]); f[2 * e + 1][d] = phi(r[2 * d][e], r[2 * d + 1][e]); }
}

#define CND(d, o, k, P) "v_cndmask_b32_dpp %" #d ", %" #o ", %" #k ", vcc quad_perm:" P " row_mask:0xf bank_mask:0xf\n\t"
template <int X> __device__ __forceinline__ void quad_stage(const v4u& lo, const v4u& hi, v4u& nlo, v4u& nhi) {
  u32 l0, l1, l2, l3, h0, h1, h2, h3;
  if (X == 1)
    asm volatile("s_nop 1\n\ts_mov_b64 vcc, %16\n\t" CND(0, 12, 8, "[1,0,3,2]") CND(1, 13, 9, "[1,0,3,2]") CND(2, 14, 10, "[1,0,3,2]") CND(3, 15, 11, "[1,0,3,2]")
                 "s_not_b64 vcc, vcc\n\t" CND(4, 8, 12, "[1,0,3,2]") CND(5, 9, 13, "[1,0,3,2]") CND(6, 10, 14, "[1,0,3,2]") CND(7, 11, 15, "[1,0,3,2]") "s_nop 1"
                 : "=&v"(l0), "=&v"(l1), "=&v"(l2), "=&v"(l3), "=&v"(h0), "=&v"(h1), "=&v"(h2), "=&v"(h3)
                 : "v"(lo[0]), "v"(lo[1]), "v"(lo[2]), "v"(lo[3]), "v"(hi[0]), "v"(hi[1]), "v"(hi[2]), "v"(hi[3]), "s"(0x5555555555555555ull) : "vcc", "scc");
  else
    asm volatile("s_nop 1\n\ts_mov_b64 vcc, %16\n\t" CND(0, 12, 8, "[2,3,0,1]") CND(1, 13, 9, "[2,3,0,1]") CND(2, 14, 10, "[2,3,0,1]") CND(3, 15, 11, "[2,3,0,1]")
                 "s_not_b64 vcc, vcc\n\t" CND(4, 8, 12, "[2,3,0,1]") CND(5, 9, 13, "[2,3,0,1]") CND(6, 10, 14, "[2,3,0,1]") CND(7, 11, 15, "[2,3,0,1]") "s_nop 1"
                 : "=&v"(l0), "=&v"(l1), "=&v"(l2), "=&v"(l3), "=&v"(h0), "=&v"(h1), "=&v"(h2), "=&v"(h3)
                 : "v"(lo[0]), "v"(lo[1]), "v"(lo[2]), "v"(lo[3]), "v"(hi[0]), "v"(hi[1]), "v"(hi[2]), "v"(hi[3]), "s"(0x3333333333333333ull) : "vcc", "scc");
  nlo = v4u{l0, l1, l2, l3}; nhi = v4u{h0, h1, h2, h3};
}
__device__ __forceinline__ void quad_tr(v4u* a) {
  v4u b[4];
  quad_stage<1>(a[0], a[1], b[0], b[1]); quad_stage<1>(a[2], a[3], b[2], b[3]);
  quad_stage<2>(b[0], b[2], a[0], a[2]); quad_stage<2>(b[1], b[3], a[1], a[3]);
}
__device__ __forceinline__ u32 umax(u32 a, u32 b) { return a > b ? a : b; }
__device__ __forceinline__ u32 nosent(u32 x) { return x < 0xFFFFFFFEu ? x : 0xFFFFFFFEu; }
__device__ __forceinline__ v4u nosent4(f4 v) { return v4u{nosent(asu(v[0])), nosent(asu(v[1])), nosent(asu(v[2])), nosent(asu(v[3]))}; }
__device__ __forceinline__ u32 mx4(v4u v, u32 m) { return umax(umax(umax(v[0], v[1]), m), umax(v[2], v[3])); }
__device__ __forceinline__ bool none_sent(u32 m) { return __builtin_amdgcn_ballot_w64(m == SENT) == 0; }
#define PIN4(f) asm volatile("" : "+v"((f)[0]), "+v"((f)[1]), "+v"((f)[2]), "+v"((f)[3]))
#define PIN8(f) asm volatile("" : "+v"(f[0]), "+v"(f[1]), "+v"(f[2]), "+v"(f[3]), "+v"(f[4]), "+v"(f[5]), "+v"(f[6]), "+v"(f[7]))
#define CBAR() asm volatile("" ::: "memory")
#define SCHED() __builtin_amdgcn_sched_barrier(0)
#define OPAQUE(x) asm volatile("" : "+s"(x))
template <int N> __device__ __forceinline__ void waitvm() { asm volatile("s_waitcnt vmcnt(%0)" ::"i"(N) : "memory"); }
#define NOUNROLL _Pragma("nounroll")
#define LD128(off, soff) __builtin_amdgcn_raw_buffer_load_b128(rws, off, soff, AUX_SC1)
#define LD64(off, soff) __builtin_amdgcn_raw_buffer_load_b64(rws, off, soff, AUX_SC1)
#define ST128(v, off, soff) __builtin_amdgcn_raw_buffer_store_b128(v, rws, off, soff, AUX_SC1)
#define ST64(v, off, soff) __builtin_amdgcn_raw_buffer_store_b64(v, rws, off, soff, AUX_SC1)
#define BARRIER() do { SCHED(); asm volatile("s_barrier" ::: "memory"); SCHED(); } while (0)

__device__ __forceinline__ u64 sent_mask(u32 m) { return __builtin_amdgcn_ballot_w64(m == SENT); }
#define RELD4(k, so) "s_mov_b64 exec, %[m" #k "]\n\tbuffer_load_dwordx4 %" #k ", %[off], %[rs], " so " offen sc1\n\t"
#define RELD_END "s_mov_b64 exec, -1\n\ts_waitcnt vmcnt(0)"

template <int F, int SLEEP> __device__ __forceinline__ void lds_wait(LAS u32* fl, u32 want) {
  CBAR();
  NOUNROLL for (int n = 0; n < SPIN_LDS; ++n) {
    if ((u32)__builtin_amdgcn_readfirstlane((int)__hip_atomic_load(fl + F, __ATOMIC_RELAXED, __HIP_MEMORY_SCOPE_WORKGROUP)) >= want) break;
    if (SLEEP) __builtin_amdgcn_s_sleep(SLEEP);
  }
  CBAR();
}
template <int F> __device__ __forceinline__ void lds_add(LAS u32* fl) {
  asm volatile("s_mov_b64 exec, 1\n\tds_add_u32 %0, %1 offset:%2\n\ts_mov_b64 exec, -1" ::"v"((u32)(size_t)fl), "v"(1u), "i"(4 * F) : "memory");
}
template <int F> __device__ __forceinline__ u32 lds_add_rtn(LAS u32* fl) {
  u32 old;
  asm volatile("s_mov_b64 exec, 1\n\tds_add_rtn_u32 %0, %1, %2 offset:%3\n\ts_mov_b64 exec, -1\n\ts_waitcnt lgkmcnt(0)"
               : "=v"(old) : "v"((u32)(size_t)fl), "v"(1u), "i"(4 * F) : "memory");
  return (u32)__builtin_amdgcn_readfirstlane((int)old);
}
template <int SLEEP> __device__ __forceinline__ void pace(u32 t0, u32 ticks) {
  NOUNROLL while ((int)((u32)__builtin_amdgcn_s_memrealtime() - t0) < (int)ticks) { if (SLEEP) __builtin_amdgcn_s_sleep(SLEEP); }
}
__device__ __forceinline__ u32 pace_min(u32 due, u32 prev, int gap) {
  if ((int)(prev + gap - due) > 0) due = prev + gap;
  u32 now;
  NOUNROLL do { now = (u32)__builtin_amdgcn_s_memrealtime(); } while ((int)(now - due) < 0);
  return now;
}
__device__ __forceinline__ u32 tref_or(LAS u32* fl, u32 own) {
  const u32 t = (u32)__builtin_amdgcn_readfirstlane((int)__hip_atomic_load(fl + F_DT, __ATOMIC_RELAXED, __HIP_MEMORY_SCOPE_WORKGROUP));
  return t ? t : own;
}
__device__ __forceinline__ int sel(bool c, int a, int b) { asm volatile("" : "+v"(a)); return c ? a : b; }

__global__ void __launch_bounds__(1024, 4) fused_decode(const u16* x_a, const u16* kc_a, const u16* vc_a, const u16* wq_a, const u16* wo_a,
                                                     const u16* w1_a, const u16* w3_a, const u16* w2_a, u16* out_a, char* ws_a) {
  __shared__ __attribute__((aligned(16))) char smem[L_END];
  LAS char* L = (LAS char*)smem;
  LAS u32* FL = (LAS u32*)(L + L_FLAG);
  const int bid = blockIdx.x, lane = threadIdx.x & 63, w = __builtin_amdgcn_readfirstlane(threadIdx.x >> 6);
  typedef u32 v16u __attribute__((ext_vector_type(16)));
  const CAS u64* kargs = (const CAS u64*)__builtin_amdgcn_kernarg_segment_ptr();
  v16u ka;
  v4u kb;
  asm volatile("s_load_dwordx16 %0, %2, 0x0\n\ts_load_dwordx4 %1, %2, 0x40" : "=&s"(ka), "=&s"(kb) : "s"(kargs));
  u32 cpf = 0;
  if (w == 3) {
    u64 pc0; asm volatile("s_getpc_b64 %0" : "=s"(pc0));
    int ln = (bid >> 3) + 32 * (lane & 7);
    ln = ln < CODE_LINES ? ln : 0;
    cpf = *(const GAS u32*)((const GAS char*)(pc0 & ~(u64)127) + ln * 128);
    FL[lane] = 0;
  }
  const int xcd = bid & 7, ii = bid >> 3;
  const int kvh = 2 * xcd + (ii >> 4), jj = ii & 15, bb = jj >> 3;
  const bool isY = ii < 18, isX = ii >= 18 && ii < 27;
  const int yi = xcd * 18 + ii, xc = ii - 18;
  const int g4 = lane >> 4, r16 = lane & 15, hb = lane >> 5, l5 = lane & 31;
  const int kw = w & 3;
  const int rot = (bb * 8 + kvh + jj) * 5;
  const int k0 = w * 64 + (r16 >> 3) * 32 + g4 * 8, ka_ = w * 64 + (lane & 1) * 32 + g4 * 8;
  int xo = l5 < 24 ? hb * (D * 2) + l5 * 16 : OOB;
  int xf = sel(ka_ < 144, ((r16 >> 2) & 1) * (D * 2) + ka_ * 2, OOB) + (jj & 3) * 288;
  int qo = sel(k0 < 144, k0 * (QD * 2) + (lane & 7) * 16, OOB) + (jj & 3) * (144 * QD * 2) + kvh * 512 + (jj >> 2) * 128;
  asm volatile("" : "+v"(xo), "+v"(xf), "+v"(qo));
  SCHED();
  asm volatile("s_waitcnt lgkmcnt(0)" : "+s"(ka), "+s"(kb)::"memory");
  SCHED();
#define KPTR(v, i) ((u64)v[2 * (i)] | ((u64)v[2 * (i) + 1] << 32))
  const u16 *x = (const u16*)KPTR(ka, 0), *kc = (const u16*)KPTR(ka, 1), *vc = (const u16*)KPTR(ka, 2), *wq = (const u16*)KPTR(ka, 3),
            *wo = (const u16*)KPTR(ka, 4), *w1 = (const u16*)KPTR(ka, 5), *w3 = (const u16*)KPTR(ka, 6), *w2 = (const u16*)KPTR(ka, 7);
  u16* out = (u16*)KPTR(kb, 0);
  char* ws = (char*)KPTR(kb, 1);
  const u32 tstart = (u32)__builtin_amdgcn_s_memrealtime() + TOFF;
  const rsrc_t rws = mkrs(ws, WS_END);
  const rsrc_t rx = mkrs(x, 2 * D * 2);
#define LOAD_TAG() (*(const CAS u64*)(ws + WS_EPOCH) + 1)
#define LDS_TAG() (*(LAS v2u*)(FL + F_TAG))
#define TREF() tref_or(FL, tstart)

  const v4u s4 = {SENT, SENT, SENT, SENT};
  int yt = -1;
  int v2 = -1;
  v4u xs[4], tq[8];
  if (w < 3) {
#pragma unroll
    for (int i = 0; i < 3; ++i) xs[i] = __builtin_amdgcn_raw_buffer_load_b128(rx, xo + i * 384, 0, 0);
    xs[3] = __builtin_amdgcn_raw_buffer_load_b128(rx, xf, 0, 0);
    const rsrc_t rq = mkrs(wq, D * QD * 2);
#pragma unroll
    for (int i = 0; i < 8; ++i) tq[i] = __builtin_amdgcn_raw_buffer_load_b128(rq, qo, i * (QD * 2), AUX_NT);
  }
  BARRIER();
  if (w == 3) {
    ST128(s4, lane < 40 ? lane * 16 : OOB, WS_OP + (xcd * 32 + ii) * 640);
    if (isY || ii == 31) {
      const rsrc_t rc = mkrs(ws, isY ? WS_QG : WS_MP);
      const int co = isY ? WS_MP + (lane >> 3) * 18432 + yi * 128 + (lane & 7) * 16 : WS_AP + (hb * 8 + xcd) * 512 + l5 * 16;
      const int cs = isY ? 8 * 18432 : 8192;
      NOUNROLL for (int n = 0; n < 5; ++n) __builtin_amdgcn_raw_buffer_store_b128(s4, rc, co, n * cs, AUX_SC1);
    }
    SCHED();
  }
  int w0_ = w;
  OPAQUE(w0_);
  if (w0_ < 3) {
    const u64 tag = LOAD_TAG();
    int qpo = sel((lane & 0x28) == 0, L_QP + w * 512 + g4 * 256 + r16 * 32, L_DUMMY + lane * 32);
    int qso = WS_QG + ((hb * 16 + kvh) * 4 + (jj & 3)) * 2048 + (jj >> 2) * 512 + l5 * 16;
    asm volatile("" : "+v"(qpo), "+v"(qso));
    SCHED();
    float s = 0, v = 0;
#pragma unroll
    for (int i = 0; i < 3; ++i)
#pragma unroll
      for (int e = 0; e < 4; ++e) { s = dot2(xs[i][e], 0x3F803F80u, s); v = dot2(xs[i][e], xs[i][e], v); }
    s += dpp<XOR1>(s); v += dpp<XOR1>(v); s += dpp<XOR2>(s); v += dpp<XOR2>(v);
    s += dpp<HMIR>(s); v += dpp<HMIR>(v); s += dpp<ROR8>(s); v += dpp<ROR8>(v);
    s = x16_sum(s); v = x16_sum(v);
    const float mean = s * (1.f / D);
    const float mb = rbf(mean);
    const float rd = __builtin_amdgcn_rcpf(rbf(__builtin_amdgcn_sqrtf(rbf(rbf(fmaxf(v * (1.f / D) - mean * mean, 0.f)) + 1e-5f))));
    const auto ms = __builtin_amdgcn_permlane32_swap(asu(mb), asu(mb), false, false);
    const auto rs = __builtin_amdgcn_permlane32_swap(asu(rd), asu(rd), false, false);
    const float mm = asf((lane & 4) ? ms[1] : ms[0]), rr = asf((lane & 4) ? rs[1] : rs[0]);
    v4u a;
#pragma unroll
    for (int e = 0; e < 4; ++e) { u32 c = cvtpk(blo(xs[3][e]) - mm, bhi(xs[3][e]) - mm); a[e] = cvtpk(blo(c) * rr, bhi(c) * rr); }
    asm volatile("" : "+v"(a));
    SCHED();
    v4u f[8];
    tr8(tq, f);
    f4 acc[8];
#pragma unroll
    for (int j = 0; j < 8; ++j) acc[j] = mfma(a, f[j], f4{0, 0, 0, 0});
    float o[8];
#pragma unroll
    for (int j = 0; j < 8; ++j) o[j] = acc[j][0] + dpp<ROR8>(acc[j][1]);
    *(LAS f4*)(L + qpo) = f4{o[0], o[1], o[2], o[3]};
    *(LAS f4*)(L + qpo + 16) = f4{o[4], o[5], o[6], o[7]};
    if (lds_add_rtn<F_QCNT>(FL) == 2) {
      const f2 q = (*(LAS f2*)(L + L_QP + lane * 8) + *(LAS f2*)(L + L_QP + 512 + lane * 8)) + *(LAS f2*)(L + L_QP + 1024 + lane * 8);
      const u32 th = (u32)(tag >> 8), tl = (u32)tag & 0xffu;
      ST128((v4u{((asu(q[0]) + 0x80u) & ~0xffu) | tl, th, ((asu(q[1]) + 0x80u) & ~0xffu) | tl, th}), qso, 0);
      const int go = WS_QG + (bb * 16 + kvh) * 8192 + lane * 16;
      v4u g[8];
      u64 m[8];
#pragma unroll
      for (int k = 0; k < 8; ++k) { m[k] = ~0ull; asm volatile("" : "=v"(g[k])); }
      pace<1>((u32)__builtin_amdgcn_s_memrealtime(), QS);
      pace<1>(tstart, TQ);
      NOUNROLL for (int n = 0; n < SPIN_GLB; ++n) {
        asm volatile(RELD4(0, "0") RELD4(1, "%[s1]") RELD4(2, "%[s2]") RELD4(3, "%[s3]") RELD4(4, "%[s4]") RELD4(5, "%[s5]") RELD4(6, "%[s6]")
                     RELD4(7, "%[s7]") RELD_END
                     : "+v"(g[0]), "+v"(g[1]), "+v"(g[2]), "+v"(g[3]), "+v"(g[4]), "+v"(g[5]), "+v"(g[6]), "+v"(g[7])
                     : [off] "v"(go), [rs] "s"(rws), [m0] "s"(m[0]), [m1] "s"(m[1]), [m2] "s"(m[2]), [m3] "s"(m[3]), [m4] "s"(m[4]), [m5] "s"(m[5]),
                       [m6] "s"(m[6]), [m7] "s"(m[7]), [s1] "s"(1024), [s2] "s"(2048), [s3] "s"(3072), [s4] "s"(4096), [s5] "s"(5120),
                       [s6] "s"(6144), [s7] "s"(7168)
                     : "memory");
        u64 any = 0;
#pragma unroll
        for (int k = 0; k < 8; ++k) {
          m[k] = __builtin_amdgcn_ballot_w64(((g[k][1] ^ th) | (g[k][3] ^ th) | (((g[k][0] ^ tl) | (g[k][2] ^ tl)) & 0xffu)) != 0);
          any |= m[k];
        }
        if (any == 0) break;
        __builtin_amdgcn_s_sleep(2);
      }
#pragma unroll
      for (int c = 0; c < 2; ++c) {
        const float a0 = (asf(g[c][0] & ~0xffu) + asf(g[2 + c][0] & ~0xffu)) + (asf(g[4 + c][0] & ~0xffu) + asf(g[6 + c][0] & ~0xffu));
        const float a1 = (asf(g[c][2] & ~0xffu) + asf(g[2 + c][2] & ~0xffu)) + (asf(g[4 + c][2] & ~0xffu) + asf(g[6 + c][2] & ~0xffu));
        *(LAS u32*)(L + L_Q + c * 256 + lane * 4) = cvtpk(a0, a1);
      }
      lds_add<F_QRDY>(FL);
    }
    yt = w;
  }
  int w1_ = w;
  OPAQUE(w1_);
  v4u t[16];
  const rsrc_t rk = mkrs(w < 8 ? kc : vc, 2 * 16 * SK * 256);
  const int kvbase = ((bb * 16 + kvh) * SK + (SK - WIN) + (jj & 7) * 256 + kw * 4) * 256;
  if (w1_ >= 4 && w1_ < 8) {
    const int voff = (r16 >> 2) * 256 + (g4 * 4 + (lane & 3)) * 16 + kvbase;
    const u32 tk = tstart + kw * (DK >> 5);
    u32 tl = tstart;
#pragma unroll
    for (int i = 0; i < 16; ++i) {
      if (i >= KVWN) waitvm<KVWN - 1>();
      tl = pace_min(tk + TK0 + (i * DK >> 3), tl, MINSP);
      t[i] = __builtin_amdgcn_raw_buffer_load_b128(rk, voff, ((i + rot) & 15) * 4096, AUX_NT);
      SCHED();
      if (i == KREP) lds_add<F_KISS>(FL);
    }
    lds_wait<F_QRDY, 1>(FL, 1);
    v4u qf = *(LAS v4u*)(L + L_Q + ((r16 >> 2) & 1) * 256 + (g4 * 4 + (lane & 3)) * 16);
    const v4u z4 = {0, 0, 0, 0};
    const v4u q0 = r16 < 8 ? qf : z4, q1 = r16 < 8 ? z4 : qf;
    float sc[8];
#pragma unroll
    for (int m = 0; m < 8; ++m) {
      f4 d = mfma(t[m], q0, f4{0, 0, 0, 0});
      d = mfma(t[m + 8], q1, d);
      sc[m] = dpp<0xFF>(d[3]) + (dpp<0xAA>(d[2]) + (dpp<0x55>(d[1]) + dpp<0x00>(d[0])));
    }
    const bool b0 = lane & 1, b1 = lane & 2;
    float s0 = b1 ? (b0 ? sc[6] : sc[4]) : (b0 ? sc[2] : sc[0]);
    float s1 = b1 ? (b0 ? sc[7] : sc[5]) : (b0 ? sc[3] : sc[1]);
    u32 pk = cvtpk(s0, s1);
    pk = cvtpk(blo(pk) * 0.08838834764831845f, bhi(pk) * 0.08838834764831845f);
    s0 = blo(pk) * 1.4426950408889634f; s1 = bhi(pk) * 1.4426950408889634f;
    float mx = fmaxf(s0, s1);
    mx = fmaxf(mx, dpp<XOR1>(mx)); mx = fmaxf(mx, dpp<XOR2>(mx)); mx = fmaxf(mx, dpp<ROR8>(mx));
    mx = x32_max(x16_max(mx));
    const int hd = (r16 >> 2) & 1;
    LAS float* MX = (LAS float*)(L + L_MX);
    const float p0 = ex2(s0 - mx), p1 = ex2(s1 - mx);
    *(LAS u32*)(L + L_P + kw * 256 + hd * 128 + (r16 >> 3) * 64 + g4 * 16 + (lane & 3) * 4) = cvtpk(p0, p1);
    float ls = p0 + p1;
    ls += dpp<XOR1>(ls); ls += dpp<XOR2>(ls); ls += dpp<ROR8>(ls);
    ls = x32_sum(x16_sum(ls));
    MX[hd * 4 + kw] = mx; MX[8 + hd * 4 + kw] = ls;
    lds_add<F_PCNT>(FL);
    if (isX && kw == 0) {
      const int z = xcd * 9 + ii - 18, zl = z >> 1, zb = zl / 18, zz = zl % 18;
      const int col = (zz >> 1) * 64 + (z & 1) * 32 + (lane & 3) * 8 + (zz & 1) * 4;
      const int jz = (g4 >> 1) + 2 * (g4 & 1);
      const u32 xv = __builtin_amdgcn_raw_buffer_load_b16(rx, (zb * D + col + jz) * 2, 0, 0);
      const int ho = sel(lane < 32, WS_AP + ((zz >> 1) * 8 + (lane >> 2)) * 512 + (lane & 3) * 128 + zb * 64 + (zz & 1) * 32 + (z & 1) * 16, OOB);
      int oo = sel(r16 < 4, (zb * D + col + jz) * 2, OOB);
      const int mo = WS_MP + zl * 18432 + (lane >> 2) * 128 + (z & 1) * 64 + (lane & 3) * 16;
      lds_wait<F_XCNT, 2>(FL, 5);
      v4u hv;
      NOUNROLL for (int n = 0; n < SPIN_GLB; ++n) {
        hv = LD128(ho, 0);
        CBAR();
        if (none_sent(mx4(hv, 0))) break;
        __builtin_amdgcn_s_sleep(4);
      }
      float h = t16_sum(t32_sum(asf(hv[0]), asf(hv[1])), t32_sum(asf(hv[2]), asf(hv[3])));
      h += dpp<ROR4>(h); h += dpp<ROR8>(h);
      h = rbf(blo(xv) + rbf(h));
      asm volatile("" : "+v"(oo), "+v"(h));
      const rsrc_t rout = mkrs(out, 2 * D * 2);
      __builtin_amdgcn_raw_buffer_store_b16((u16)(asu(h) >> 16), rout, oo, 0, AUX_SC1);
      v4u p[9];
      u64 m[9];
      f4 sum;
#pragma unroll
      for (int k = 0; k < 9; ++k) { m[k] = ~0ull; asm volatile("" : "=v"(p[k])); }
      NOUNROLL for (int n = 0; n < SPIN_GLB; ++n) {
        asm volatile(RELD4(0, "0") RELD4(1, "%[s1]") RELD4(2, "%[s2]") RELD4(3, "%[s3]") RELD4(4, "%[s4]") RELD4(5, "%[s5]") RELD4(6, "%[s6]")
                     RELD4(7, "%[s7]") RELD4(8, "%[s8]") RELD_END
                     : "+v"(p[0]), "+v"(p[1]), "+v"(p[2]), "+v"(p[3]), "+v"(p[4]), "+v"(p[5]), "+v"(p[6]), "+v"(p[7]), "+v"(p[8])
                     : [off] "v"(mo), [rs] "s"(rws), [m0] "s"(m[0]), [m1] "s"(m[1]), [m2] "s"(m[2]), [m3] "s"(m[3]), [m4] "s"(m[4]), [m5] "s"(m[5]),
                       [m6] "s"(m[6]), [m7] "s"(m[7]), [m8] "s"(m[8]), [s1] "s"(2048), [s2] "s"(4096), [s3] "s"(6144), [s4] "s"(8192),
                       [s5] "s"(10240), [s6] "s"(12288), [s7] "s"(14336), [s8] "s"(16384)
                     : "memory");
        sum = (((asf4(p[0]) + asf4(p[1])) + (asf4(p[2]) + asf4(p[3]))) + ((asf4(p[4]) + asf4(p[5])) + (asf4(p[6]) + asf4(p[7])))) + asf4(p[8]);
        if (!any_nan(sum)) break;
        u64 any = 0, all = ~0ull;
#pragma unroll
        for (int k = 0; k < 9; ++k) { m[k] = sent_mask(mx4(p[k], 0)); any |= m[k]; all &= m[k]; }
        if (any == 0) break;
        if (all == ~0ull) __builtin_amdgcn_s_sleep(4);
      }
      float r = t16_sum(t32_sum(sum[0], sum[1]), t32_sum(sum[2], sum[3]));
      r += dpp<ROR4>(r); r += dpp<ROR8>(r);
      r = h + rbf(r);
      __builtin_amdgcn_raw_buffer_store_b16((u16)cvtpk(r, r), rout, oo, 0, AUX_SC1);
      if (z == 0) __builtin_amdgcn_raw_buffer_store_b64(LDS_TAG(), rws, lane == 0 ? WS_EPOCH : OOB, 0, 0);
    }
    if (kw == 0) yt = 4;
    if (kw == 2) yt = 5;
    if (kw == 1) v2 = 4;
  }
  int w2_ = w;
  OPAQUE(w2_);
  if (w2_ >= 8 && w2_ < 12) {
    const int voff = lane * 16 + kvbase;
    u32 tk = tstart + kw * (DV >> 5);
    pace<2>(tk, TK0 + (15 * DK >> 3));
    lds_wait<F_KISS, 1>(FL, 4);
    int dt = 0;
    u32 tl = tstart;
#pragma unroll
    for (int i = 0; i < 16; ++i) {
      if (i == VSYNC) dt = (int)(TREF() - tstart);
      if (i >= KVWN) waitvm<KVWN - 1>();
      tl = pace_min(tk + TK0 + 2 * DK + (i * DV >> 3) + (i >= VSYNC ? dt * (i - VSYNC + 1) / (16 - VSYNC) : 0), tl, MINSP);
      t[i] = __builtin_amdgcn_raw_buffer_load_b128(rk, voff, ((i + rot) & 15) * 4096, AUX_NT);
      SCHED();
      if (i == VREP) lds_add<F_VNEAR>(FL);
    }
    lds_add<F_VISS>(FL);
    v4u f[8];
    tr8(t, f);
    PIN8(f);
    lds_wait<F_PCNT, 1>(FL, 4);
    const v4u pa = *(LAS v4u*)(L + L_P + kw * 256 + ((r16 >> 2) & 1) * 128 + g4 * 16);
    const v4u pb = *(LAS v4u*)(L + L_P + kw * 256 + ((r16 >> 2) & 1) * 128 + 64 + g4 * 16);
    const f4 m4 = *(LAS f4*)(L + L_MX + (g4 & 1) * 16), l4 = *(LAS f4*)(L + L_MX + 32 + (g4 & 1) * 16);
    const float mw = *(LAS float*)(L + L_MX + (g4 & 1) * 16 + kw * 4);
    f4 acc[8];
#pragma unroll
    for (int j = 0; j < 8; ++j) acc[j] = mfma(pa, f[j], f4{0, 0, 0, 0});
    const float M = fmaxf(fmaxf(m4[0], m4[1]), fmaxf(m4[2], m4[3]));
    float fs = ex2(mw - M);
    v2u ml = {nosent(asu(M)), nosent(asu((l4[0] * ex2(m4[0] - M) + l4[1] * ex2(m4[1] - M)) + (l4[2] * ex2(m4[2] - M) + l4[3] * ex2(m4[3] - M))))};
    int mlo = sel((lane & 0x2F) == 0, WS_OP + (xcd * 32 + ii) * 640 + 512 + g4 * 8, OOB);
    int opo = WS_OP + (xcd * 32 + ii) * 640 + lane * 8;
    int lo = L_O + kw * 1024 + (g4 & 1) * 512 + r16 * 32 + (g4 >> 1) * 16;
    asm volatile("" : "+v"(fs), "+v"(ml), "+v"(mlo), "+v"(opo), "+v"(lo));
    lds_wait<F_CLR, 1>(FL, 1);
    SCHED();
    tr8(t + 8, f);
#pragma unroll
    for (int j = 0; j < 8; ++j) acc[j] = mfma(pb, f[j], acc[j]);
    {
      const bool hi = g4 >= 2;
      f4 o = {hi ? acc[4][0] : acc[0][0], hi ? acc[5][0] : acc[1][0], hi ? acc[6][0] : acc[2][0], hi ? acc[7][0] : acc[3][0]};
      *(LAS f4*)(L + lo) = o * fs;
    }
    if (lds_add_rtn<F_OCNT>(FL) == 3) {
      ST64(ml, mlo, 0);
      f4 o = (*(LAS f4*)(L + L_O + lane * 16) + *(LAS f4*)(L + L_O + 1024 + lane * 16)) +
             (*(LAS f4*)(L + L_O + 2048 + lane * 16) + *(LAS f4*)(L + L_O + 3072 + lane * 16));
      ST64((v2u{nosent(cvtpk(o[0], o[1])), nosent(cvtpk(o[2], o[3]))}), opo, 0);
      lds_add<F_OCNT>(FL);
    }
    v2 = kw;
  }
  int w3_ = w;
  OPAQUE(w3_);
  if (w3_ == 3) {
    const u64 tag = LOAD_TAG();
    asm volatile("s_waitcnt vmcnt(0)" ::"v"(cpf) : "memory");
    const v2u tg = {(u32)tag, (u32)(tag >> 32)};
    ST128((v4u{tg[0], tg[1], tstart, 0}), lane == 0 ? WS_FLAGS + bid * 128 : OOB, 0);
    *(LAS v2u*)(FL + F_TAG) = tg;
    lds_add<F_CLR>(FL);
    {
      const bool act = lane < 32 ? isX : lane < 40;
      const int fo = sel(act, WS_FLAGS + (lane < 32 ? lane * 8 + xcd : 248 + lane - 32) * 128, OOB);
      pace<4>(tstart, TFL);
      v4u g;
      NOUNROLL for (int n = 0; n < SPIN_GLB; ++n) {
        g = LD128(fo, 0);
        CBAR();
        const bool done = __builtin_amdgcn_ballot_w64(act && ((g[0] ^ tg[0]) | (g[1] ^ tg[1])) != 0) == 0;
        if (done) break;
        __builtin_amdgcn_s_sleep(12);
      }
      int d = (int)(g[2] - tstart);
      d = d > 100 ? 100 : d < -100 ? -100 : d;
      d += (int)dppu<XOR1>(d); d += (int)dppu<XOR2>(d); d += (int)dppu<HMIR>(d);
      FL[F_DT] = tstart + (__builtin_amdgcn_readlane(d, 32) >> 3);
      lds_add<F_INOK>(FL);
    }
    if (!isY) {
      constexpr int NPP = (81 - 9 * NPX + 4) / 5;
      const rsrc_t rp = mkrs(w2, xcd * (576 * D * 2) + (isX ? 81 : 81 - 9 * NPX) * 8192);
      const int po = lane * 128 + xcd * (576 * D * 2) + (isX ? 81 - 9 * NPX + xc : ii - 27) * 8192;
      const int step = isX ? 9 * 8192 : 5 * 8192;
      if (isX) lds_wait<F_XCNT, 1>(FL, 5); else lds_wait<F_BI, 2>(FL, 1);
      pace<1>(TREF(), TW2);
      u32 pf[NPP];
#pragma unroll
      for (int k = 0; k < NPP; ++k) {
        if (k >= PWIN) waitvm<PWIN - 1>();
        pf[k] = __builtin_amdgcn_raw_buffer_load_b32(rp, po, k * step, 0);
        SCHED();
      }
#pragma unroll
      for (int k = 0; k < NPP; ++k) asm volatile("" ::"v"(pf[k]));
    }
    yt = 3;
  } else if (w3_ >= 12) {
    if (isX) {
      const int hh = 4 * xcd + kw;
      const rsrc_t ro = mkrs(wo, QD * D * 2);
      const int vo = (hh * 128 + g4 * 32) * (D * 2) + (xc * 64 + r16 * 4) * 2;
      int xo = L_X + kw * 512 + ((r16 >> 1) & 3) * 128 + (g4 >> 1) * 64 + (r16 & 1) * 32 + (r16 >> 3) * 16;
      int po = sel(lane < 32, WS_AP + (xc * 8 + xcd) * 512 + lane * 16, OOB);
      const int ao = WS_OP + ((hh >> 1) * 16 + (r16 & 12)) * 640 + (hh & 1) * 256 + (g4 * 4 + (lane & 3)) * 16;
      const int mo = sel(r16 < 8, WS_OP + ((hh >> 1) * 16 + (g4 >> 1) * 8 + ((r16 + 4 * (g4 & 1)) & 7)) * 640 + 512 + (hh & 1) * 8, OOB);
      asm volatile("" : "+v"(xo), "+v"(po));
      lds_wait<F_VISS, 1>(FL, 4);
      const u32 tr = TREF();
      pace<1>(tr, TXB + kw * DXB);
      v2u u[32];
#pragma unroll
      for (int i = 0; i < 32; ++i) u[i] = __builtin_amdgcn_raw_buffer_load_b64(ro, vo, i * (D * 2), AUX_NT);
      SCHED();
      pace<1>(tr, TXO);
      lds_wait<F_INOK, 1>(FL, 1);
      v4u a[4] = {LD128(ao, 0), LD128(ao + 640, 0), LD128(ao + 1280, 0), LD128(ao + 1920, 0)};
      v2u ml = LD64(mo, 0);
      SCHED();
      v4u f[16];
#pragma unroll
      for (int k = 0; k < 4; ++k) { tr4(u + 8 * k, f + 4 * k); PIN4(f + 4 * k); }
      NOUNROLL for (int n = 0; n < SPIN_GLB; ++n) {
        if (none_sent(mx4(a[3], mx4(a[2], mx4(a[1], mx4(a[0], umax(ml[0], ml[1]))))))) break;
        u64 m[4];
#pragma unroll
        for (int k = 0; k < 4; ++k) m[k] = sent_mask(mx4(a[k], 0));
        const u64 mm = sent_mask(umax(ml[0], ml[1]));
        asm volatile("s_mov_b64 exec, %[mm]\n\tbuffer_load_dwordx2 %4, %[mo], %[rs], 0 offen sc1\n\t"
                     "s_mov_b64 exec, %[m0]\n\tbuffer_load_dwordx4 %0, %[off], %[rs], 0 offen sc1\n\t"
                     "s_mov_b64 exec, %[m1]\n\tbuffer_load_dwordx4 %1, %[off], %[rs], 0 offen offset:640 sc1\n\t"
                     "s_mov_b64 exec, %[m2]\n\tbuffer_load_dwordx4 %2, %[off], %[rs], 0 offen offset:1280 sc1\n\t"
                     "s_mov_b64 exec, %[m3]\n\tbuffer_load_dwordx4 %3, %[off], %[rs], 0 offen offset:1920 sc1\n\t" RELD_END
                     : "+v"(a[0]), "+v"(a[1]), "+v"(a[2]), "+v"(a[3]), "+v"(ml)
                     : [off] "v"(ao), [mo] "v"(mo), [rs] "s"(rws), [m0] "s"(m[0]), [m1] "s"(m[1]), [m2] "s"(m[2]), [m3] "s"(m[3]), [mm] "s"(mm)
                     : "memory");
      }
      quad_tr(a);
      f4 d[4];
#pragma unroll
      for (int j = 0; j < 4; ++j) d[j] = mfma(a[0], f[j], f4{0, 0, 0, 0});
#pragma unroll
      for (int k = 1; k < 4; ++k)
#pragma unroll
        for (int j = 0; j < 4; ++j) d[j] = mfma(a[k], f[4 * k + j], d[j]);
      float M = asf(ml[0]);
      M = fmaxf(M, dpp<XOR1>(M)); M = fmaxf(M, dpp<XOR2>(M)); M = fmaxf(M, dpp<HMIR>(M));
      const float ee = ex2(asf(ml[0]) - M);
      float Ls = ee * asf(ml[1]);
      Ls += dpp<XOR1>(Ls); Ls += dpp<XOR2>(Ls); Ls += dpp<HMIR>(Ls);
      const float wr = ee * __builtin_amdgcn_rcpf(Ls);
      const float e[4] = {dpp<0x150>(wr), dpp<0x151>(wr), dpp<0x152>(wr), dpp<0x153>(wr)};
      f4 o;
#pragma unroll
      for (int j = 0; j < 4; ++j) o[j] = x16_sum((e[0] * d[j][0] + e[1] * d[j][1]) + (e[2] * d[j][2] + e[3] * d[j][3]));
      *(LAS f4*)(L + xo) = o;
      if (lds_add_rtn<F_XCNT>(FL) == 3) {
        LAS char* rp = L + L_X + l5 * 16;
        const f4 s = (*(LAS f4*)(rp) + *(LAS f4*)(rp + 512)) + (*(LAS f4*)(rp + 1024) + *(LAS f4*)(rp + 1536));
        ST128(nosent4(s), po, 0);
        lds_add<F_XCNT>(FL);
      }
    } else if (w == 15) {
      const int pi = isY ? ii : ii - 9;
      const int br = lane / 9;
      const int band = sel(lane < 63, br * (FF * 2) + (lane - br * 9) * 128, OOB) + xcd * 1152;
      const rsrc_t ro = mkrs(wo, (xcd + 1) * (512 * D * 2));
      const int oo = lane * 128 + xcd * (512 * D * 2) + (22 - pi) * 8192;
      u32 pf[12];
      pace<4>(tstart, TWO - 150);
      lds_wait<F_VNEAR, 1>(FL, 4);
      const u32 tp = TREF() + (pi % 6 - 3) * DITH;
      u32 tl = tstart;
#pragma unroll
      for (int k = 0; k < 4; ++k) {
        if (k < 3) tl = pace_min(tp + TWO + k * DWO, tl, PMIN);
        pf[k] = __builtin_amdgcn_raw_buffer_load_b32(ro, oo, k * 23 * 8192, 0);
        SCHED();
      }
      lds_wait<F_VISS, 1>(FL, 4);
#pragma unroll
      for (int k = 0; k < 8; ++k) {
        const int m = 22 - pi + 23 * k;
        if (k < 7 || !isY) {
          waitvm<PWIN - 1>();
          if (k < 7) tl = pace_min(tp + TPF + k * DPF, tl, PMIN);
          const bool second = k > 3 || (k == 3 && m >= 83);
          pf[4 + k] = __builtin_amdgcn_raw_buffer_load_b32(mkrs(second ? w3 : w1, D * FF * 2), band, (second ? m - 83 : m) * (7 * FF * 2), 0);
        } else pf[4 + k] = 0;
        SCHED();
      }
      if (!isY) lds_add<F_BI>(FL);
      lds_wait<F_OCNT, 1>(FL, 5);
      lds_add<F_PF>(FL);
#pragma unroll
      for (int k = 0; k < 12; ++k) asm volatile("" ::"v"(pf[k]));
    }
    yt = w < 15 ? w - 6 : -1;
  }
  if (isY) {
    if (yt >= 0) {
      const GAS char* wp = (const GAS char*)((lane & 4) ? w3 : w1) + (size_t)((yt * 64 + (r16 >> 3) * 32 + g4 * 8) * (FF * 2) + (yi * 32 + (lane & 3) * 8) * 2);
      const int ao = WS_AP + (yt * 8 + (r16 >> 3)) * 512 + g4 * 128 + (lane & 7) * 16;
      int ypo = sel(r16 < 8, L_R + yt * 1024 + ((lane & 3) * 16 + g4 * 4 + (r16 >> 2) * 2) * 16, L_DUMMY + lane * 32);
      int sto = sel(lane == 0 || lane == 8 || lane == 20 || lane == 24, L_ST + ((r16 == 8 ? 0 : 2) + g4) * 64 + yt * 4, L_DUMMY + lane * 32);
      asm volatile("" : "+v"(ypo), "+v"(sto), "+v"(wp));
      v2u xv = __builtin_amdgcn_raw_buffer_load_b64(rx, (((lane >> 2) & 1) * D + yt * 64 + (lane & 1) * 32 + g4 * 8 + ((lane >> 1) & 1) * 4) * 2, 0, 0);
      SCHED();
      lds_wait<F_INOK, 2>(FL, 1);
      const u32 tr = TREF();
      lds_wait<F_PF, 1>(FL, 1);
#pragma unroll
      for (int i = 0; i < 8; ++i) t[i] = __builtin_nontemporal_load((const GAS v4u*)(wp + i * (FF * 2)));
      SCHED();
      lds_add<F_BI>(FL);
      const bool ride = (u32)__builtin_amdgcn_s_memrealtime() - tr >= (u32)TAP;
      const int ar = ride ? ao : OOB;
      v4u p[4] = {LD128(ar, 0), LD128(ar + 1024, 0), LD128(ar + 2048, 0), LD128(ar + 3072, 0)};
      SCHED();
      v4u f[8];
      tr8(t, f);
      PIN8(f);
      if (!ride) p[0] = p[1] = p[2] = p[3] = v4u{SENT, SENT, SENT, SENT};
      f4 hs;
      NOUNROLL for (int n = 0; n < SPIN_GLB; ++n) {
        hs = (asf4(p[0]) + asf4(p[1])) + (asf4(p[2]) + asf4(p[3]));
        if (!any_nan(hs)) break;
        u64 m[4];
#pragma unroll
        for (int k = 0; k < 4; ++k) m[k] = sent_mask(mx4(p[k], 0));
        if ((m[0] | m[1] | m[2] | m[3]) == 0) break;
        if (n == 0) { pace<1>(tr, TAP); if (ride) lds_wait<F_BI, 1>(FL, 9); }
        else __builtin_amdgcn_s_sleep(3);
        asm volatile("s_mov_b64 exec, %[m0]\n\tbuffer_load_dwordx4 %0, %[off], %[rs], 0 offen sc1\n\t"
                     "s_mov_b64 exec, %[m1]\n\tbuffer_load_dwordx4 %1, %[off], %[rs], 0 offen offset:1024 sc1\n\t"
                     "s_mov_b64 exec, %[m2]\n\tbuffer_load_dwordx4 %2, %[off], %[rs], 0 offen offset:2048 sc1\n\t"
                     "s_mov_b64 exec, %[m3]\n\tbuffer_load_dwordx4 %3, %[off], %[rs], 0 offen offset:3072 sc1\n\t" RELD_END
                     : "+v"(p[0]), "+v"(p[1]), "+v"(p[2]), "+v"(p[3])
                     : [off] "v"(ao), [rs] "s"(rws), [m0] "s"(m[0]), [m1] "s"(m[1]), [m2] "s"(m[2]), [m3] "s"(m[3])
                     : "memory");
      }
      float h[4];
#pragma unroll
      for (int j = 0; j < 4; ++j) h[j] = hs[j] + dpp<ROR8>(hs[j]);
      const u32 c0 = cvtpk(h[0], h[1]), c1 = cvtpk(h[2], h[3]);
      const u32 h0 = cvtpk(blo(xv[0]) + blo(c0), bhi(xv[0]) + bhi(c0)), h1 = cvtpk(blo(xv[1]) + blo(c1), bhi(xv[1]) + bhi(c1));
      const u32 h2 = dppu<XOR2>(h0), h3 = dppu<XOR2>(h1);
      const bool one = lane & 8;
      const v4u a = {one ? 0x3F803F80u : h0, one ? 0x3F803F80u : h1, one ? 0x3F803F80u : h2, one ? 0x3F803F80u : h3};
      f4 acc[8];
#pragma unroll
      for (int j = 0; j < 8; ++j) acc[j] = mfma(a, f[j], f4{0, 0, 0, 0});
      const f4 gm = mfma(a, a, f4{0, 0, 0, 0});
      float o[8];
#pragma unroll
      for (int j = 0; j < 8; ++j) o[j] = acc[j][0] + dpp<ROR8>(acc[j][1]);
      *(LAS f4*)(L + ypo) = f4{o[0], o[1], o[2], o[3]};
      *(LAS f4*)(L + ypo + 16) = f4{o[4], o[5], o[6], o[7]};
      *(LAS float*)(L + sto) = gm[0] + dpp<SHL1>(gm[1]);
      lds_add<F_RCNT>(FL);
    } else if (v2 >= 0) {
      const rsrc_t r2 = mkrs(w2, FF * D * 2);
      const int vo = sel(v2 < 4 || r16 < 8, (yi * 32 + g4 * 8) * (D * 2) + (v2 * 128 + r16 * 8) * 2, OOB);
      int mo = sel(g4 < 2 && (v2 < 4 || r16 < 8), WS_MP + (g4 * 18 + v2 * 4 + (r16 >> 3) * 2) * 18432 + yi * 128 + (lane & 7) * 16, OOB);
      int sto = sel(r16 < 9, L_ST + lane * 4, L_FLAG + 4 * F_ZERO);
      asm volatile("" : "+v"(mo), "+v"(sto));
      lds_wait<F_BI, 1>(FL, 9);
#pragma unroll
      for (int i = 0; i < 8; ++i) t[i] = __builtin_amdgcn_raw_buffer_load_b128(r2, vo, i * (D * 2), AUX_NT);
      SCHED();
      v4u f[8];
      tr8(t, f);
      PIN8(f);
      lds_wait<F_RCNT, 2>(FL, 1);
      lds_wait<F_RCNT, 0>(FL, 9);
      float st = *(LAS float*)(L + sto);
      f4 y[9];
#pragma unroll
      for (int k = 0; k < 9; ++k) y[k] = *(LAS f4*)(L + L_R + k * 1024 + lane * 16);
      SCHED();
      const f4 s = (((y[0] + y[1]) + (y[2] + y[3])) + ((y[4] + y[5]) + (y[6] + y[7]))) + y[8];
      st += dpp<XOR1>(st); st += dpp<XOR2>(st); st += dpp<HMIR>(st); st += dpp<ROR8>(st);
      const float a0 = asf(__builtin_amdgcn_readlane(asu(st), 0)), a1 = asf(__builtin_amdgcn_readlane(asu(st), 16));
      const float q0 = asf(__builtin_amdgcn_readlane(asu(st), 32)), q1 = asf(__builtin_amdgcn_readlane(asu(st), 48));
      const float mean = ((lane & 4) ? a1 : a0) * (1.f / D);
      const float var = ((lane & 4) ? q1 : q0) * (1.f / D) - mean * mean;
      const float rs = __builtin_amdgcn_rsqf(fmaxf(var, 0.f) + 1e-5f);
      float hid[4];
#pragma unroll
      for (int j = 0; j < 4; ++j) {
        const float zv = rs * (s[j] - mean * dpp<ROR8>(s[j]));
        const float up = dpp<XOR2>(zv);
        hid[j] = zv * __builtin_amdgcn_rcpf(1.f + ex2(-1.4426950408889634f * zv)) * up;
      }
      const u32 h0 = cvtpk(hid[0], hid[1]), h1 = cvtpk(hid[2], hid[3]);
      const v4u a = {h0, h1, dppu<XOR1>(h0), dppu<XOR1>(h1)};
      f4 acc[8];
#pragma unroll
      for (int j = 0; j < 8; ++j) acc[j] = mfma(a, f[j], f4{0, 0, 0, 0});
      ST128(nosent4(f4{acc[0][0], acc[1][0], acc[2][0], acc[3][0]}), mo, 0);
      ST128(nosent4(f4{acc[4][0], acc[5][0], acc[6][0], acc[7][0]}), mo, 18432);
    }
  }
}

extern "C" void kernel_launch(void* const* d_in, const int* in_sizes, int n_in, void* d_out, int out_size, void* d_ws, size_t ws_size,
                              hipStream_t stream) {
  fused_decode<<<256, 1024, 0, stream>>>((const u16*)d_in[0], (const u16*)d_in[1], (const u16*)d_in[2], (const u16*)d_in[3],
                                         (const u16*)d_in[4], (const u16*)d_in[6], (const u16*)d_in[7], (const u16*)d_in[8], (u16*)d_out,
                                         (char*)d_ws);
}
```

```cpp
#include <hip/hip_runtime.h>
#include <hip/hip_bf16.h>
#include <stdint.h>

#define GAS __attribute__((address_space(1)))
#define LAS __attribute__((address_space(3)))
#define CAS __attribute__((address_space(4)))
typedef unsigned u32;
typedef unsigned long long u64;
typedef unsigned short u16;
typedef u32 v4u __attribute__((ext_vector_type(4)));
typedef u32 v2u __attribute__((ext_vector_type(2)));
typedef float f4 __attribute__((ext_vector_type(4)));
typedef float f2 __attribute__((ext_vector_type(2)));
typedef __bf16 bf8 __attribute__((ext_vector_type(8)));
typedef __bf16 bf2 __attribute__((ext_vector_type(2)));
typedef __amdgpu_buffer_rsrc_t rsrc_t;

constexpr int TOFF = -10;
constexpr int TK0 = 60, DK = 120, DV = 120;
constexpr int TVE = TK0 + 2 * DK + 2 * DV;
constexpr int DWO = 22, DPF = 22;
constexpr int MINSP = 11, PMIN = 16;
constexpr int KREP = 13, VREP = 14;
constexpr int TWO = TVE + 7, TPF = TWO + 3 + 3 * DWO, TW2 = TPF + 7 * DPF + (-5);
constexpr int TXB = TVE + 67, DXB = 21, TXO = TVE + 147;
constexpr int TAP = TVE + 360, OW2L = 40;
constexpr int OZH = 40, OZ = 262;
constexpr int TQ = 250, QS = 50, TFL = 250;
constexpr int VSYNC = 6, DITH = 4;
constexpr int KVWN = 8, PWIN = 4, NPX = 5;
constexpr int CODE_LINES = 150;

constexpr int D = 576, SK = 8192, WIN = 2048, FF = 4608, QD = 4096;
constexpr int WS_EPOCH = 0, WS_FLAGS = 4096, WS_OP = WS_FLAGS + 256 * 128, WS_AP = WS_OP + 256 * 640,
              WS_MP = WS_AP + 36864, WS_QG = WS_MP + 663552, WS_END = WS_QG + 262144;
constexpr u32 SENT = 0xFFFFFFFFu;
constexpr int AUX_NT = 2, AUX_SC1 = 16, OOB = 1 << 30;
constexpr int SPIN_LDS = 1 << 20, SPIN_GLB = 1 << 17;
constexpr int L_FLAG = 0;
constexpr int L_Q = 256;
constexpr int L_P = 768;
constexpr int L_MX = 1792;
constexpr int L_ST = 1920;
constexpr int L_QP = 2304;
constexpr int L_O = 4096;
constexpr int L_X = 8192;
constexpr int L_R = 10240;
constexpr int L_DUMMY = L_R + 9216;
constexpr int L_END = L_DUMMY + 2048;
enum { F_QCNT = 0, F_QRDY, F_PCNT, F_OCNT, F_VISS, F_CLR, F_INOK, F_PF, F_RCNT, F_XCNT, F_ZERO, F_BI, F_DT, F_KISS, F_VNEAR, F_TAG = 16 };

__device__ __forceinline__ rsrc_t mkrs(const void* p, int bytes) { return __builtin_amdgcn_make_buffer_rsrc((void*)p, 0, bytes, 0x00020000); }
__device__ __forceinline__ float asf(u32 u) { return __uint_as_float(u); }
__device__ __forceinline__ u32 asu(float f) { return __float_as_uint(f); }
__device__ __forceinline__ u32 cvtpk(float lo, float hi) { return __builtin_bit_cast(u32, __builtin_convertvector(f2{lo, hi}, bf2)); }
__device__ __forceinline__ float rbf(float f) { return asf(cvtpk(f, f) & 0xffff0000u); }
__device__ __forceinline__ float blo(u32 u) { return asf(u << 16); }
__device__ __forceinline__ float bhi(u32 u) { return asf(u & 0xffff0000u); }
__device__ __forceinline__ float ex2(float x) { return __builtin_amdgcn_exp2f(x); }
__device__ __forceinline__ float dot2(u32 a, u32 b, float c) { return __builtin_amdgcn_fdot2_f32_bf16(__builtin_bit_cast(bf2, a), __builtin_bit_cast(bf2, b), c, false); }
template <int C> __device__ __forceinline__ u32 dppu(u32 x) { return (u32)__builtin_amdgcn_mov_dpp((int)x, C, 0xf, 0xf, true); }
template <int C> __device__ __forceinline__ float dpp(float x) { return asf(dppu<C>(asu(x))); }
constexpr int XOR1 = 0xB1, XOR2 = 0x4E, HMIR = 0x141, ROR4 = 0x124, ROR8 = 0x128, SHL1 = 0x101;
__device__ __forceinline__ float x16_sum(float x) { auto s = __builtin_amdgcn_permlane16_swap(asu(x), asu(x), false, false); return asf(s[0]) + asf(s[1]); }
__device__ __forceinline__ float x32_sum(float x) { auto s = __builtin_amdgcn_permlane32_swap(asu(x), asu(x), false, false); return asf(s[0]) + asf(s[1]); }
__device__ __forceinline__ float x16_max(float x) { auto s = __builtin_amdgcn_permlane16_swap(asu(x), asu(x), false, false); return fmaxf(asf(s[0]), asf(s[1])); }
__device__ __forceinline__ float x32_max(float x) { auto s = __builtin_amdgcn_permlane32_swap(asu(x), asu(x), false, false); return fmaxf(asf(s[0]), asf(s[1])); }
__device__ __forceinline__ float t32_sum(float a, float b) { auto s = __builtin_amdgcn_permlane32_swap(asu(a), asu(b), false, false); return asf(s[0]) + asf(s[1]); }
__device__ __forceinline__ float t16_sum(float a, float b) { auto s = __builtin_amdgcn_permlane16_swap(asu(a), asu(b), false, false); return asf(s[0]) + asf(s[1]); }
__device__ __forceinline__ f4 asf4(v4u v) { return __builtin_bit_cast(f4, v); }
__device__ __forceinline__ bool any_nan(f4 v) { return __builtin_amdgcn_ballot_w64(__builtin_isunordered(v[0], v[1]) || __builtin_isunordered(v[2], v[3])) != 0; }
__device__ __forceinline__ f4 mfma(v4u a, v4u b, f4 c) { return __builtin_amdgcn_mfma_f32_16x16x32_bf16(__builtin_bit_cast(bf8, a), __builtin_bit_cast(bf8, b), c, 0, 0, 0); }
__device__ __forceinline__ u32 plo(u32 a, u32 b) { return __builtin_amdgcn_perm(b, a, 0x05040100u); }
__device__ __forceinline__ u32 phi(u32 a, u32 b) { return __builtin_amdgcn_perm(b, a, 0x07060302u); }
__device__ __forceinline__ void tr8(const v4u* r, v4u* f) {
#pragma unroll
  for (int d = 0; d < 4; ++d)
#pragma unroll
    for (int e = 0; e < 4; ++e) { f[2 * e][d] = plo(r[2 * d][e], r[2 * d + 1][e]); f[2 * e + 1][d] = phi(r[2 * d][e], r[2 * d + 1][e]); }
}
__device__ __forceinline__ void tr4(const v2u* r, v4u* f) {
#pragma unroll
  for (int d = 0; d < 4; ++d)
#pragma unroll
    for (int e = 0; e < 2; ++e) { f[2 * e][d] = plo(r[2 * d][e], r[2 * d + 1][e]); f[2 * e + 1][d] = phi(r[2 * d][e], r[2 * d + 1][e]); }
}

#define CND(d, o, k, P) "v_cndmask_b32_dpp %" #d ", %" #o ", %" #k ", vcc quad_perm:" P " row_mask:0xf bank_mask:0xf\n\t"
template <int X> __device__ __forceinline__ void quad_stage(const v4u& lo, const v4u& hi, v4u& nlo, v4u& nhi) {
  u32 l0, l1, l2, l3, h0, h1, h2, h3;
  if (X == 1)
    asm volatile("s_nop 1\n\ts_mov_b64 vcc, %16\n\t" CND(0, 12, 8, "[1,0,3,2]") CND(1, 13, 9, "[1,0,3,2]") CND(2, 14, 10, "[1,0,3,2]") CND(3, 15, 11, "[1,0,3,2]")
                 "s_not_b64 vcc, vcc\n\t" CND(4, 8, 12, "[1,0,3,2]") CND(5, 9, 13, "[1,0,3,2]") CND(6, 10, 14, "[1,0,3,2]") CND(7, 11, 15, "[1,0,3,2]") "s_nop 1"
                 : "=&v"(l0), "=&v"(l1), "=&v"(l2), "=&v"(l3), "=&v"(h0), "=&v"(h1), "=&v"(h2), "=&v"(h3)
                 : "v"(lo[0]), "v"(lo[1]), "v"(lo[2]), "v"(lo[3]), "v"(hi[0]), "v"(hi[1]), "v"(hi[2]), "v"(hi[3]), "s"(0x5555555555555555ull) : "vcc", "scc");
  else
    asm volatile("s_nop 1\n\ts_mov_b64 vcc, %16\n\t" CND(0, 12, 8, "[2,3,0,1]") CND(1, 13, 9, "[2,3,0,1]") CND(2, 14, 10, "[2,3,0,1]") CND(3, 15, 11, "[2,3,0,1]")
                 "s_not_b64 vcc, vcc\n\t" CND(4, 8, 12, "[2,3,0,1]") CND(5, 9, 13, "[2,3,0,1]") CND(6, 10, 14, "[2,3,0,1]") CND(7, 11, 15, "[2,3,0,1]") "s_nop 1"
                 : "=&v"(l0), "=&v"(l1), "=&v"(l2), "=&v"(l3), "=&v"(h0), "=&v"(h1), "=&v"(h2), "=&v"(h3)
                 : "v"(lo[0]), "v"(lo[1]), "v"(lo[2]), "v"(lo[3]), "v"(hi[0]), "v"(hi[1]), "v"(hi[2]), "v"(hi[3]), "s"(0x3333333333333333ull) : "vcc", "scc");
  nlo = v4u{l0, l1, l2, l3}; nhi = v4u{h0, h1, h2, h3};
}
__device__ __forceinline__ void quad_tr(v4u* a) {
  v4u b[4];
  quad_stage<1>(a[0], a[1], b[0], b[1]); quad_stage<1>(a[2], a[3], b[2], b[3]);
  quad_stage<2>(b[0], b[2], a[0], a[2]); quad_stage<2>(b[1], b[3], a[1], a[3]);
}
__device__ __forceinline__ u32 umax(u32 a, u32 b) { return a > b ? a : b; }
__device__ __forceinline__ u32 nosent(u32 x) { return x < 0xFFFFFFFEu ? x : 0xFFFFFFFEu; }
__device__ __forceinline__ v4u nosent4(f4 v) { return v4u{nosent(asu(v[0])), nosent(asu(v[1])), nosent(asu(v[2])), nosent(asu(v[3]))}; }
__device__ __forceinline__ u32 mx4(v4u v, u32 m) { return umax(umax(umax(v[0], v[1]), m), umax(v[2], v[3])); }
__device__ __forceinline__ bool none_sent(u32 m) { return __builtin_amdgcn_ballot_w64(m == SENT) == 0; }
#define PIN4(f) asm volatile("" : "+v"((f)[0]), "+v"((f)[1]), "+v"((f)[2]), "+v"((f)[3]))
#define PIN8(f) asm volatile("" : "+v"(f[0]), "+v"(f[1]), "+v"(f[2]), "+v"(f[3]), "+v"(f[4]), "+v"(f[5]), "+v"(f[6]), "+v"(f[7]))
#define CBAR() asm volatile("" ::: "memory")
#define SCHED() __builtin_amdgcn_sched_barrier(0)
#define OPAQUE(x) asm volatile("" : "+s"(x))
template <int N> __device__ __forceinline__ void waitvm() { asm volatile("s_waitcnt vmcnt(%0)" ::"i"(N) : "memory"); }
#define NOUNROLL _Pragma("nounroll")
#define LD128(off, soff) __builtin_amdgcn_raw_buffer_load_b128(rws, off, soff, AUX_SC1)
#define LD64(off, soff) __builtin_amdgcn_raw_buffer_load_b64(rws, off, soff, AUX_SC1)
#define ST128(v, off, soff) __builtin_amdgcn_raw_buffer_store_b128(v, rws, off, soff, AUX_SC1)
#define ST64(v, off, soff) __builtin_amdgcn_raw_buffer_store_b64(v, rws, off, soff, AUX_SC1)
#define BARRIER() do { SCHED(); asm volatile("s_barrier" ::: "memory"); SCHED(); } while (0)

__device__ __forceinline__ u64 sent_mask(u32 m) { return __builtin_amdgcn_ballot_w64(m == SENT); }
#define RELD4(k, so) "s_mov_b64 exec, %[m" #k "]\n\tbuffer_load_dwordx4 %" #k ", %[off], %[rs], " so " offen sc1\n\t"
#define RELD_END "s_mov_b64 exec, -1\n\ts_waitcnt vmcnt(0)"

template <int F, int SLEEP> __device__ __forceinline__ void lds_wait(LAS u32* fl, u32 want) {
  CBAR();
  NOUNROLL for (int n = 0; n < SPIN_LDS; ++n) {
    if ((u32)__builtin_amdgcn_readfirstlane((int)__hip_atomic_load(fl + F, __ATOMIC_RELAXED, __HIP_MEMORY_SCOPE_WORKGROUP)) >= want) break;
    if (SLEEP) __builtin_amdgcn_s_sleep(SLEEP);
  }
  CBAR();
}
template <int F> __device__ __forceinline__ void lds_add(LAS u32* fl) {
  asm volatile("s_mov_b64 exec, 1\n\tds_add_u32 %0, %1 offset:%2\n\ts_mov_b64 exec, -1" ::"v"((u32)(size_t)fl), "v"(1u), "i"(4 * F) : "memory");
}
template <int F> __device__ __forceinline__ u32 lds_add_rtn(LAS u32* fl) {
  u32 old;
  asm volatile("s_mov_b64 exec, 1\n\tds_add_rtn_u32 %0, %1, %2 offset:%3\n\ts_mov_b64 exec, -1\n\ts_waitcnt lgkmcnt(0)"
               : "=v"(old) : "v"((u32)(size_t)fl), "v"(1u), "i"(4 * F) : "memory");
  return (u32)__builtin_amdgcn_readfirstlane((int)old);
}
template <int SLEEP> __device__ __forceinline__ void pace(u32 t0, u32 ticks) {
  NOUNROLL while ((int)((u32)__builtin_amdgcn_s_memrealtime() - t0) < (int)ticks) { if (SLEEP) __builtin_amdgcn_s_sleep(SLEEP); }
}
__device__ __forceinline__ u32 pace_min(u32 due, u32 prev, int gap) {
  if ((int)(prev + gap - due) > 0) due = prev + gap;
  u32 now;
  NOUNROLL do { now = (u32)__builtin_amdgcn_s_memrealtime(); } while ((int)(now - due) < 0);
  return now;
}
__device__ __forceinline__ u32 tref_or(LAS u32* fl, u32 own) {
  const u32 t = (u32)__builtin_amdgcn_readfirstlane((int)__hip_atomic_load(fl + F_DT, __ATOMIC_RELAXED, __HIP_MEMORY_SCOPE_WORKGROUP));
  return t ? t : own;
}
__device__ __forceinline__ u64 next_tag(v2u e) { asm volatile("" : "+v"(e)); return ((u64)e[0] | ((u64)e[1] << 32)) + 1; }
__device__ __forceinline__ int sel(bool c, int a, int b) { asm volatile("" : "+v"(a)); return c ? a : b; }

__global__ void __launch_bounds__(1024, 4) fused_decode(const u16* x_a, const u16* kc_a, const u16* vc_a, const u16* wq_a, const u16* wo_a,
                                                     const u16* w1_a, const u16* w3_a, const u16* w2_a, u16* out_a, char* ws_a) {
  __shared__ __attribute__((aligned(16))) char smem[L_END];
  LAS char* L = (LAS char*)smem;
  LAS u32* FL = (LAS u32*)(L + L_FLAG);
  const int bid = blockIdx.x, lane = threadIdx.x & 63, w = __builtin_amdgcn_readfirstlane(threadIdx.x >> 6);
  typedef u32 v16u __attribute__((ext_vector_type(16)));
  const CAS u64* kargs = (const CAS u64*)__builtin_amdgcn_kernarg_segment_ptr();
  v16u ka;
  v4u kb;
  asm volatile("s_load_dwordx16 %0, %2, 0x0\n\ts_load_dwordx4 %1, %2, 0x40" : "=&s"(ka), "=&s"(kb) : "s"(kargs));
  u32 cpf = 0;
  if (w == 3) {
    u64 pc0; asm volatile("s_getpc_b64 %0" : "=s"(pc0));
    int ln = (bid >> 3) + 32 * (lane & 7);
    ln = ln < CODE_LINES ? ln : 0;
    cpf = *(const GAS u32*)((const GAS char*)(pc0 & ~(u64)127) + ln * 128);
    FL[lane] = 0;
  }
  const int xcd = bid & 7, ii = bid >> 3;
  const int kvh = 2 * xcd + (ii >> 4), jj = ii & 15, bb = jj >> 3;
  const bool isY = ii < 18, isX = ii >= 18 && ii < 27;
  const int yi = xcd * 18 + ii, xc = ii - 18;
  const int g4 = lane >> 4, r16 = lane & 15, hb = lane >> 5, l5 = lane & 31;
  const int kw = w & 3;
  const int rot = (bb * 8 + kvh + jj) * 5;
  const int k0 = w * 64 + (r16 >> 3) * 32 + g4 * 8, ka_ = w * 64 + (lane & 1) * 32 + g4 * 8;
  int xo = l5 < 24 ? hb * (D * 2) + l5 * 16 : OOB;
  int xf = sel(ka_ < 144, ((r16 >> 2) & 1) * (D * 2) + ka_ * 2, OOB) + (jj & 3) * 288;
  int qo = sel(k0 < 144, k0 * (QD * 2) + (lane & 7) * 16, OOB) + (jj & 3) * (144 * QD * 2) + kvh * 512 + (jj >> 2) * 128;
  asm volatile("" : "+v"(xo), "+v"(xf), "+v"(qo));
  SCHED();
  asm volatile("s_waitcnt lgkmcnt(0)" : "+s"(ka), "+s"(kb)::"memory");
  SCHED();
#define KPTR(v, i) ((u64)v[2 * (i)] | ((u64)v[2 * (i) + 1] << 32))
  const u16 *x = (const u16*)KPTR(ka, 0), *kc = (const u16*)KPTR(ka, 1), *vc = (const u16*)KPTR(ka, 2), *wq = (const u16*)KPTR(ka, 3),
            *wo = (const u16*)KPTR(ka, 4), *w1 = (const u16*)KPTR(ka, 5), *w3 = (const u16*)KPTR(ka, 6), *w2 = (const u16*)KPTR(ka, 7);
  u16* out = (u16*)KPTR(kb, 0);
  char* ws = (char*)KPTR(kb, 1);
  const u32 tstart = (u32)__builtin_amdgcn_s_memrealtime() + TOFF;
  const rsrc_t rws = mkrs(ws, WS_END);
  const rsrc_t rx = mkrs(x, 2 * D * 2);
#define LDS_TAG() (*(LAS v2u*)(FL + F_TAG))
#define TREF() tref_or(FL, tstart)

  const v4u s4 = {SENT, SENT, SENT, SENT};
  int yt = -1;
  int v2 = -1;
  v4u xs[4], tq[8];
  v2u ep;
  if (w < 3) {
#pragma unroll
    for (int i = 0; i < 3; ++i) xs[i] = __builtin_amdgcn_raw_buffer_load_b128(rx, xo + i * 384, 0, 0);
    xs[3] = __builtin_amdgcn_raw_buffer_load_b128(rx, xf, 0, 0);
    const rsrc_t rq = mkrs(wq, D * QD * 2);
#pragma unroll
    for (int i = 0; i < 8; ++i) tq[i] = __builtin_amdgcn_raw_buffer_load_b128(rq, qo, i * (QD * 2), AUX_NT);
    ep = LD64(0, WS_EPOCH);
  }
  BARRIER();
  if (w == 3) {
    ep = LD64(0, WS_EPOCH);
    ST128(s4, lane < 40 ? lane * 16 : OOB, WS_OP + (xcd * 32 + ii) * 640);
    if (isY || ii == 31) {
      const rsrc_t rc = mkrs(ws, isY ? WS_QG : WS_MP);
      const int co = isY ? WS_MP + (lane >> 3) * 18432 + yi * 128 + (lane & 7) * 16 : WS_AP + (hb * 8 + xcd) * 512 + l5 * 16;
      const int cs = isY ? 8 * 18432 : 8192;
      NOUNROLL for (int n = 0; n < 5; ++n) __builtin_amdgcn_raw_buffer_store_b128(s4, rc, co, n * cs, AUX_SC1);
    }
    SCHED();
  }
  int w0_ = w;
  OPAQUE(w0_);
  if (w0_ < 3) {
    int qpo = sel((lane & 0x28) == 0, L_QP + w * 512 + g4 * 256 + r16 * 32, L_DUMMY + lane * 32);
    int qso = WS_QG + ((hb * 16 + kvh) * 4 + (jj & 3)) * 2048 + (jj >> 2) * 512 + l5 * 16;
    asm volatile("" : "+v"(qpo), "+v"(qso));
    SCHED();
    float s = 0, v = 0;
#pragma unroll
    for (int i = 0; i < 3; ++i)
#pragma unroll
      for (int e = 0; e < 4; ++e) { s = dot2(xs[i][e], 0x3F803F80u, s); v = dot2(xs[i][e], xs[i][e], v); }
    s += dpp<XOR1>(s); v += dpp<XOR1>(v); s += dpp<XOR2>(s); v += dpp<XOR2>(v);
    s += dpp<HMIR>(s); v += dpp<HMIR>(v); s += dpp<ROR8>(s); v += dpp<ROR8>(v);
    s = x16_sum(s); v = x16_sum(v);
    const float mean = s * (1.f / D);
    const float mb = rbf(mean);
    const float rd = __builtin_amdgcn_rcpf(rbf(__builtin_amdgcn_sqrtf(rbf(rbf(fmaxf(v * (1.f / D) - mean * mean, 0.f)) + 1e-5f))));
    const auto ms = __builtin_amdgcn_permlane32_swap(asu(mb), asu(mb), false, false);
    const auto rs = __builtin_amdgcn_permlane32_swap(asu(rd), asu(rd), false, false);
    const float mm = asf((lane & 4) ? ms[1] : ms[0]), rr = asf((lane & 4) ? rs[1] : rs[0]);
    v4u a;
#pragma unroll
    for (int e = 0; e < 4; ++e) { u32 c = cvtpk(blo(xs[3][e]) - mm, bhi(xs[3][e]) - mm); a[e] = cvtpk(blo(c) * rr, bhi(c) * rr); }
    asm volatile("" : "+v"(a));
    SCHED();
    v4u f[8];
    tr8(tq, f);
    f4 acc[8];
#pragma unroll
    for (int j = 0; j < 8; ++j) acc[j] = mfma(a, f[j], f4{0, 0, 0, 0});
    float o[8];
#pragma unroll
    for (int j = 0; j < 8; ++j) o[j] = acc[j][0] + dpp<ROR8>(acc[j][1]);
    *(LAS f4*)(L + qpo) = f4{o[0], o[1], o[2], o[3]};
    *(LAS f4*)(L + qpo + 16) = f4{o[4], o[5], o[6], o[7]};
    if (lds_add_rtn<F_QCNT>(FL) == 2) {
      const f2 q = (*(LAS f2*)(L + L_QP + lane * 8) + *(LAS f2*)(L + L_QP + 512 + lane * 8)) + *(LAS f2*)(L + L_QP + 1024 + lane * 8);
      const u64 tag = next_tag(ep);
      const u32 th = (u32)(tag >> 8), tl = (u32)tag & 0xffu;
      ST128((v4u{((asu(q[0]) + 0x80u) & ~0xffu) | tl, th, ((asu(q[1]) + 0x80u) & ~0xffu) | tl, th}), qso, 0);
      const int go = WS_QG + (bb * 16 + kvh) * 8192 + lane * 16;
      v4u g[8];
      u64 m[8];
#pragma unroll
      for (int k = 0; k < 8; ++k) { m[k] = ~0ull; asm volatile("" : "=v"(g[k])); }
      pace<1>((u32)__builtin_amdgcn_s_memrealtime(), QS);
      pace<1>(tstart, TQ);
      NOUNROLL for (int n = 0; n < SPIN_GLB; ++n) {
        asm volatile(RELD4(0, "0") RELD4(1, "%[s1]") RELD4(2, "%[s2]") RELD4(3, "%[s3]") RELD4(4, "%[s4]") RELD4(5, "%[s5]") RELD4(6, "%[s6]")
                     RELD4(7, "%[s7]") RELD_END
                     : "+v"(g[0]), "+v"(g[1]), "+v"(g[2]), "+v"(g[3]), "+v"(g[4]), "+v"(g[5]), "+v"(g[6]), "+v"(g[7])
                     : [off] "v"(go), [rs] "s"(rws), [m0] "s"(m[0]), [m1] "s"(m[1]), [m2] "s"(m[2]), [m3] "s"(m[3]), [m4] "s"(m[4]), [m5] "s"(m[5]),
                       [m6] "s"(m[6]), [m7] "s"(m[7]), [s1] "s"(1024), [s2] "s"(2048), [s3] "s"(3072), [s4] "s"(4096), [s5] "s"(5120),
                       [s6] "s"(6144), [s7] "s"(7168)
                     : "memory");
        u64 any = 0;
#pragma unroll
        for (int k = 0; k < 8; ++k) {
          m[k] = __builtin_amdgcn_ballot_w64(((g[k][1] ^ th) | (g[k][3] ^ th) | (((g[k][0] ^ tl) | (g[k][2] ^ tl)) & 0xffu)) != 0);
          any |= m[k];
        }
        if (any == 0) break;
        __builtin_amdgcn_s_sleep(2);
      }
#pragma unroll
      for (int c = 0; c < 2; ++c) {
        const float a0 = (asf(g[c][0] & ~0xffu) + asf(g[2 + c][0] & ~0xffu)) + (asf(g[4 + c][0] & ~0xffu) + asf(g[6 + c][0] & ~0xffu));
        const float a1 = (asf(g[c][2] & ~0xffu) + asf(g[2 + c][2] & ~0xffu)) + (asf(g[4 + c][2] & ~0xffu) + asf(g[6 + c][2] & ~0xffu));
        *(LAS u32*)(L + L_Q + c * 256 + lane * 4) = cvtpk(a0, a1);
      }
      lds_add<F_QRDY>(FL);
    }
    yt = w;
  }
  int w1_ = w;
  OPAQUE(w1_);
  v4u t[16];
  const rsrc_t rk = mkrs(w < 8 ? kc : vc, 2 * 16 * SK * 256);
  const int kvbase = ((bb * 16 + kvh) * SK + (SK - WIN) + (jj & 7) * 256 + kw * 4) * 256;
  if (w1_ >= 4 && w1_ < 8) {
    const int voff = (r16 >> 2) * 256 + (g4 * 4 + (lane & 3)) * 16 + kvbase;
    const u32 tk = tstart + kw * (DK >> 5);
    u32 tl = tstart;
#pragma unroll
    for (int i = 0; i < 16; ++i) {
      if (i >= KVWN) waitvm<KVWN - 1>();
      tl = pace_min(tk + TK0 + (i * DK >> 3), tl, MINSP);
      t[i] = __builtin_amdgcn_raw_buffer_load_b128(rk, voff, ((i + rot) & 15) * 4096, AUX_NT);
      SCHED();
      if (i == KREP) lds_add<F_KISS>(FL);
    }
    lds_wait<F_QRDY, 1>(FL, 1);
    v4u qf = *(LAS v4u*)(L + L_Q + ((r16 >> 2) & 1) * 256 + (g4 * 4 + (lane & 3)) * 16);
    const v4u z4 = {0, 0, 0, 0};
    const v4u q0 = r16 < 8 ? qf : z4, q1 = r16 < 8 ? z4 : qf;
    float sc[8];
#pragma unroll
    for (int m = 0; m < 8; ++m) {
      f4 d = mfma(t[m], q0, f4{0, 0, 0, 0});
      d = mfma(t[m + 8], q1, d);
      sc[m] = dpp<0xFF>(d[3]) + (dpp<0xAA>(d[2]) + (dpp<0x55>(d[1]) + dpp<0x00>(d[0])));
    }
    const bool b0 = lane & 1, b1 = lane & 2;
    float s0 = b1 ? (b0 ? sc[6] : sc[4]) : (b0 ? sc[2] : sc[0]);
    float s1 = b1 ? (b0 ? sc[7] : sc[5]) : (b0 ? sc[3] : sc[1]);
    u32 pk = cvtpk(s0, s1);
    pk = cvtpk(blo(pk) * 0.08838834764831845f, bhi(pk) * 0.08838834764831845f);
    s0 = blo(pk) * 1.4426950408889634f; s1 = bhi(pk) * 1.4426950408889634f;
    float mx = fmaxf(s0, s1);
    mx = fmaxf(mx, dpp<XOR1>(mx)); mx = fmaxf(mx, dpp<XOR2>(mx)); mx = fmaxf(mx, dpp<ROR8>(mx));
    mx = x32_max(x16_max(mx));
    const int hd = (r16 >> 2) & 1;
    LAS float* MX = (LAS float*)(L + L_MX);
    const float p0 = ex2(s0 - mx), p1 = ex2(s1 - mx);
    *(LAS u32*)(L + L_P + kw * 256 + hd * 128 + (r16 >> 3) * 64 + g4 * 16 + (lane & 3) * 4) = cvtpk(p0, p1);
    float ls = p0 + p1;
    ls += dpp<XOR1>(ls); ls += dpp<XOR2>(ls); ls += dpp<ROR8>(ls);
    ls = x32_sum(x16_sum(ls));
    MX[hd * 4 + kw] = mx; MX[8 + hd * 4 + kw] = ls;
    lds_add<F_PCNT>(FL);
    if (isX && kw == 0) {
      const int z = xcd * 9 + ii - 18, zl = z >> 1, zb = zl / 18, zz = zl % 18;
      const int col = (zz >> 1) * 64 + (z & 1) * 32 + (lane & 3) * 8 + (zz & 1) * 4;
      const int jz = (g4 >> 1) + 2 * (g4 & 1);
      const u32 xv = __builtin_amdgcn_raw_buffer_load_b16(rx, (zb * D + col + jz) * 2, 0, 0);
      const int ho = sel(lane < 32, WS_AP + ((zz >> 1) * 8 + (lane >> 2)) * 512 + (lane & 3) * 128 + zb * 64 + (zz & 1) * 32 + (z & 1) * 16, OOB);
      int oo = sel(r16 < 4, (zb * D + col + jz) * 2, OOB);
      const int mo = WS_MP + zl * 18432 + (lane >> 2) * 128 + (z & 1) * 64 + (lane & 3) * 16;
      lds_wait<F_XCNT, 2>(FL, 5);
      const u32 tr = TREF();
      pace<2>(tr, TAP + OZH);
      v4u hv;
      NOUNROLL for (int n = 0; n < SPIN_GLB; ++n) {
        hv = LD128(ho, 0);
        CBAR();
        if (none_sent(mx4(hv, 0))) break;
        __builtin_amdgcn_s_sleep(4);
      }
      float h = t16_sum(t32_sum(asf(hv[0]), asf(hv[1])), t32_sum(asf(hv[2]), asf(hv[3])));
      h += dpp<ROR4>(h); h += dpp<ROR8>(h);
      h = rbf(blo(xv) + rbf(h));
      asm volatile("" : "+v"(oo), "+v"(h));
      const rsrc_t rout = mkrs(out, 2 * D * 2);
      __builtin_amdgcn_raw_buffer_store_b16((u16)(asu(h) >> 16), rout, oo, 0, AUX_SC1);
      pace<1>(tr, TAP + OZ);
      v4u p[9];
      u64 m[9];
      f4 sum;
#pragma unroll
      for (int k = 0; k < 9; ++k) { m[k] = ~0ull; asm volatile("" : "=v"(p[k])); }
      NOUNROLL for (int n = 0; n < SPIN_GLB; ++n) {
        asm volatile(RELD4(0, "0") RELD4(1, "%[s1]") RELD4(2, "%[s2]") RELD4(3, "%[s3]") RELD4(4, "%[s4]") RELD4(5, "%[s5]") RELD4(6, "%[s6]")
                     RELD4(7, "%[s7]") RELD4(8, "%[s8]") RELD_END
                     : "+v"(p[0]), "+v"(p[1]), "+v"(p[2]), "+v"(p[3]), "+v"(p[4]), "+v"(p[5]), "+v"(p[6]), "+v"(p[7]), "+v"(p[8])
                     : [off] "v"(mo), [rs] "s"(rws), [m0] "s"(m[0]), [m1] "s"(m[1]), [m2] "s"(m[2]), [m3] "s"(m[3]), [m4] "s"(m[4]), [m5] "s"(m[5]),
                       [m6] "s"(m[6]), [m7] "s"(m[7]), [m8] "s"(m[8]), [s1] "s"(2048), [s2] "s"(4096), [s3] "s"(6144), [s4] "s"(8192),
                       [s5] "s"(10240), [s6] "s"(12288), [s7] "s"(14336), [s8] "s"(16384)
                     : "memory");
        sum = (((asf4(p[0]) + asf4(p[1])) + (asf4(p[2]) + asf4(p[3]))) + ((asf4(p[4]) + asf4(p[5])) + (asf4(p[6]) + asf4(p[7])))) + asf4(p[8]);
        if (!any_nan(sum)) break;
        u64 any = 0;
#pragma unroll
        for (int k = 0; k < 9; ++k) { m[k] = sent_mask(mx4(p[k], 0)); any |= m[k]; }
        if (any == 0) break;
        __builtin_amdgcn_s_sleep(2);
      }
      float r = t16_sum(t32_sum(sum[0], sum[1]), t32_sum(sum[2], sum[3]));
      r += dpp<ROR4>(r); r += dpp<ROR8>(r);
      r = h + rbf(r);
      __builtin_amdgcn_raw_buffer_store_b16((u16)cvtpk(r, r), rout, oo, 0, AUX_SC1);
      if (z == 0) ST64(LDS_TAG(), lane == 0 ? WS_EPOCH : OOB, 0);
    }
    if (kw == 0) yt = 4;
    if (kw == 3) yt = 5;
    if (kw == 1) v2 = 4;
  }
  int w2_ = w;
  OPAQUE(w2_);
  if (w2_ >= 8 && w2_ < 12) {
    const int voff = lane * 16 + kvbase;
    u32 tk = tstart + kw * (DV >> 5);
    pace<2>(tk, TK0 + (15 * DK >> 3));
    lds_wait<F_KISS, 1>(FL, 4);
    int dt = 0;
    u32 tl = tstart;
#pragma unroll
    for (int i = 0; i < 16; ++i) {
      if (i == VSYNC) dt = (int)(TREF() - tstart);
      if (i >= KVWN) waitvm<KVWN - 1>();
      tl = pace_min(tk + TK0 + 2 * DK + (i * DV >> 3) + (i >= VSYNC ? dt * (i - VSYNC + 1) / (16 - VSYNC) : 0), tl, MINSP);
      t[i] = __builtin_amdgcn_raw_buffer_load_b128(rk, voff, ((i + rot) & 15) * 4096, AUX_NT);
      SCHED();
      if (i == VREP) lds_add<F_VNEAR>(FL);
    }
    lds_add<F_VISS>(FL);
    v4u f[8];
    tr8(t, f);
    PIN8(f);
    lds_wait<F_PCNT, 1>(FL, 4);
    const v4u pa = *(LAS v4u*)(L + L_P + kw * 256 + ((r16 >> 2) & 1) * 128 + g4 * 16);
    const v4u pb = *(LAS v4u*)(L + L_P + kw * 256 + ((r16 >> 2) & 1) * 128 + 64 + g4 * 16);
    const f4 m4 = *(LAS f4*)(L + L_MX + (g4 & 1) * 16), l4 = *(LAS f4*)(L + L_MX + 32 + (g4 & 1) * 16);
    const float mw = *(LAS float*)(L + L_MX + (g4 & 1) * 16 + kw * 4);
    f4 acc[8];
#pragma unroll
    for (int j = 0; j < 8; ++j) acc[j] = mfma(pa, f[j], f4{0, 0, 0, 0});
    const float M = fmaxf(fmaxf(m4[0], m4[1]), fmaxf(m4[2], m4[3]));
    float fs = ex2(mw - M);
    v2u ml = {nosent(asu(M)), nosent(asu((l4[0] * ex2(m4[0] - M) + l4[1] * ex2(m4[1] - M)) + (l4[2] * ex2(m4[2] - M) + l4[3] * ex2(m4[3] - M))))};
    int mlo = sel((lane & 0x2F) == 0, WS_OP + (xcd * 32 + ii) * 640 + 512 + g4 * 8, OOB);
    int opo = WS_OP + (xcd * 32 + ii) * 640 + lane * 8;
    int lo = L_O + kw * 1024 + (g4 & 1) * 512 + r16 * 32 + (g4 >> 1) * 16;
    asm volatile("" : "+v"(fs), "+v"(ml), "+v"(mlo), "+v"(opo), "+v"(lo));
    lds_wait<F_CLR, 1>(FL, 1);
    SCHED();
    tr8(t + 8, f);
#pragma unroll
    for (int j = 0; j < 8; ++j) acc[j] = mfma(pb, f[j], acc[j]);
    {
      const bool hi = g4 >= 2;
      f4 o = {hi ? acc[4][0] : acc[0][0], hi ? acc[5][0] : acc[1][0], hi ? acc[6][0] : acc[2][0], hi ? acc[7][0] : acc[3][0]};
      *(LAS f4*)(L + lo) = o * fs;
    }
    if (lds_add_rtn<F_OCNT>(FL) == 3) {
      ST64(ml, mlo, 0);
      f4 o = (*(LAS f4*)(L + L_O + lane * 16) + *(LAS f4*)(L + L_O + 1024 + lane * 16)) +
             (*(LAS f4*)(L + L_O + 2048 + lane * 16) + *(LAS f4*)(L + L_O + 3072 + lane * 16));
      ST64((v2u{nosent(cvtpk(o[0], o[1])), nosent(cvtpk(o[2], o[3]))}), opo, 0);
      lds_add<F_OCNT>(FL);
    }
    v2 = kw;
  }
  int w3_ = w;
  OPAQUE(w3_);
  if (w3_ == 3) {
    asm volatile("s_waitcnt vmcnt(0)" ::"v"(cpf) : "memory");
    const u64 tag = next_tag(ep);
    const v2u tg = {(u32)tag, (u32)(tag >> 32)};
    ST128((v4u{tg[0], tg[1], tstart, 0}), lane == 0 ? WS_FLAGS + bid * 128 : OOB, 0);
    *(LAS v2u*)(FL + F_TAG) = tg;
    lds_add<F_CLR>(FL);
    {
      const bool act = lane < 32 ? isX : lane < 40;
      const int fo = sel(act, WS_FLAGS + (lane < 32 ? lane * 8 + xcd : 248 + lane - 32) * 128, OOB);
      pace<4>(tstart, TFL);
      v4u g;
      NOUNROLL for (int n = 0; n < SPIN_GLB; ++n) {
        g = LD128(fo, 0);
        CBAR();
        const bool done = __builtin_amdgcn_ballot_w64(act && ((g[0] ^ tg[0]) | (g[1] ^ tg[1])) != 0) == 0;
        if (done) break;
        __builtin_amdgcn_s_sleep(12);
      }
      int d = (int)(g[2] - tstart);
      d = d > 100 ? 100 : d < -100 ? -100 : d;
      d += (int)dppu<XOR1>(d); d += (int)dppu<XOR2>(d); d += (int)dppu<HMIR>(d);
      FL[F_DT] = tstart + (__builtin_amdgcn_readlane(d, 32) >> 3);
      lds_add<F_INOK>(FL);
    }
    if (!isY) {
      constexpr int NPP = (81 - 9 * NPX + 4) / 5;
      const rsrc_t rp = mkrs(w2, xcd * (576 * D * 2) + (isX ? 81 : 81 - 9 * NPX) * 8192);
      const int po = lane * 128 + xcd * (576 * D * 2) + (isX ? 81 - 9 * NPX + xc : ii - 27) * 8192;
      const int step = isX ? 9 * 8192 : 5 * 8192;
      if (isX) lds_wait<F_XCNT, 1>(FL, 5); else lds_wait<F_BI, 2>(FL, 1);
      pace<1>(TREF(), TW2);
      u32 pf[NPP];
#pragma unroll
      for (int k = 0; k < NPP; ++k) {
        if (k >= PWIN) waitvm<PWIN - 1>();
        pf[k] = __builtin_amdgcn_raw_buffer_load_b32(rp, po, k * step, 0);
        SCHED();
      }
#pragma unroll
      for (int k = 0; k < NPP; ++k) asm volatile("" ::"v"(pf[k]));
    }
    yt = 3;
  } else if (w3_ >= 12) {
    if (isX) {
      const int hh = 4 * xcd + kw;
      const rsrc_t ro = mkrs(wo, QD * D * 2);
      const int vo = (hh * 128 + g4 * 32) * (D * 2) + (xc * 64 + r16 * 4) * 2;
      int xo = L_X + kw * 512 + ((r16 >> 1) & 3) * 128 + (g4 >> 1) * 64 + (r16 & 1) * 32 + (r16 >> 3) * 16;
      int po = sel(lane < 32, WS_AP + (xc * 8 + xcd) * 512 + lane * 16, OOB);
      const int ao = WS_OP + ((hh >> 1) * 16 + (r16 & 12)) * 640 + (hh & 1) * 256 + (g4 * 4 + (lane & 3)) * 16;
      const int mo = sel(r16 < 8, WS_OP + ((hh >> 1) * 16 + (g4 >> 1) * 8 + ((r16 + 4 * (g4 & 1)) & 7)) * 640 + 512 + (hh & 1) * 8, OOB);
      asm volatile("" : "+v"(xo), "+v"(po));
      lds_wait<F_VISS, 1>(FL, 4);
      const u32 tr = TREF();
      pace<1>(tr, TXB + kw * DXB);
      v2u u[32];
#pragma unroll
      for (int i = 0; i < 32; ++i) u[i] = __builtin_amdgcn_raw_buffer_load_b64(ro, vo, i * (D * 2), AUX_NT);
      SCHED();
      pace<1>(tr, TXO);
      lds_wait<F_INOK, 1>(FL, 1);
      v4u a[4] = {LD128(ao, 0), LD128(ao + 640, 0), LD128(ao + 1280, 0), LD128(ao + 1920, 0)};
      v2u ml = LD64(mo, 0);
      SCHED();
      v4u f[16];
#pragma unroll
      for (int k = 0; k < 4; ++k) { tr4(u + 8 * k, f + 4 * k); PIN4(f + 4 * k); }
      NOUNROLL for (int n = 0; n < SPIN_GLB; ++n) {
        if (none_sent(mx4(a[3], mx4(a[2], mx4(a[1], mx4(a[0], umax(ml[0], ml[1]))))))) break;
        u64 m[4];
#pragma unroll
        for (int k = 0; k < 4; ++k) m[k] = sent_mask(mx4(a[k], 0));
        const u64 mm = sent_mask(umax(ml[0], ml[1]));
        asm volatile("s_mov_b64 exec, %[mm]\n\tbuffer_load_dwordx2 %4, %[mo], %[rs], 0 offen sc1\n\t"
                     "s_mov_b64 exec, %[m0]\n\tbuffer_load_dwordx4 %0, %[off], %[rs], 0 offen sc1\n\t"
                     "s_mov_b64 exec, %[m1]\n\tbuffer_load_dwordx4 %1, %[off], %[rs], 0 offen offset:640 sc1\n\t"
                     "s_mov_b64 exec, %[m2]\n\tbuffer_load_dwordx4 %2, %[off], %[rs], 0 offen offset:1280 sc1\n\t"
                     "s_mov_b64 exec, %[m3]\n\tbuffer_load_dwordx4 %3, %[off], %[rs], 0 offen offset:1920 sc1\n\t" RELD_END
                     : "+v"(a[0]), "+v"(a[1]), "+v"(a[2]), "+v"(a[3]), "+v"(ml)
                     : [off] "v"(ao), [mo] "v"(mo), [rs] "s"(rws), [m0] "s"(m[0]), [m1] "s"(m[1]), [m2] "s"(m[2]), [m3] "s"(m[3]), [mm] "s"(mm)
                     : "memory");
      }
      quad_tr(a);
      f4 d[4];
#pragma unroll
      for (int j = 0; j < 4; ++j) d[j] = mfma(a[0], f[j], f4{0, 0, 0, 0});
#pragma unroll
      for (int k = 1; k < 4; ++k)
#pragma unroll
        for (int j = 0; j < 4; ++j) d[j] = mfma(a[k], f[4 * k + j], d[j]);
      float M = asf(ml[0]);
      M = fmaxf(M, dpp<XOR1>(M)); M = fmaxf(M, dpp<XOR2>(M)); M = fmaxf(M, dpp<HMIR>(M));
      const float ee = ex2(asf(ml[0]) - M);
      float Ls = ee * asf(ml[1]);
      Ls += dpp<XOR1>(Ls); Ls += dpp<XOR2>(Ls); Ls += dpp<HMIR>(Ls);
      const float wr = ee * __builtin_amdgcn_rcpf(Ls);
      const float e[4] = {dpp<0x150>(wr), dpp<0x151>(wr), dpp<0x152>(wr), dpp<0x153>(wr)};
      f4 o;
#pragma unroll
      for (int j = 0; j < 4; ++j) o[j] = x16_sum((e[0] * d[j][0] + e[1] * d[j][1]) + (e[2] * d[j][2] + e[3] * d[j][3]));
      *(LAS f4*)(L + xo) = o;
      if (lds_add_rtn<F_XCNT>(FL) == 3) {
        LAS char* rp = L + L_X + l5 * 16;
        const f4 s = (*(LAS f4*)(rp) + *(LAS f4*)(rp + 512)) + (*(LAS f4*)(rp + 1024) + *(LAS f4*)(rp + 1536));
        ST128(nosent4(s), po, 0);
        lds_add<F_XCNT>(FL);
      }
    } else if (w == 15) {
      const int pi = isY ? ii : ii - 9;
      const int br = lane / 9;
      const int band = sel(lane < 63, br * (FF * 2) + (lane - br * 9) * 128, OOB) + xcd * 1152;
      const rsrc_t ro = mkrs(wo, (xcd + 1) * (512 * D * 2));
      const int oo = lane * 128 + xcd * (512 * D * 2) + (22 - pi) * 8192;
      u32 pf[12];
      pace<4>(tstart, TWO - 150);
      lds_wait<F_VNEAR, 1>(FL, 4);
      const u32 tp = TREF() + (pi % 6 - 3) * DITH;
      u32 tl = tstart;
#pragma unroll
      for (int k = 0; k < 4; ++k) {
        if (k < 3) tl = pace_min(tp + TWO + k * DWO, tl, PMIN);
        pf[k] = __builtin_amdgcn_raw_buffer_load_b32(ro, oo, k * 23 * 8192, 0);
        SCHED();
      }
      lds_wait<F_VISS, 1>(FL, 4);
#pragma unroll
      for (int k = 0; k < 8; ++k) {
        const int m = 22 - pi + 23 * k;
        if (k < 7 || !isY) {
          waitvm<PWIN - 1>();
          if (k < 7) tl = pace_min(tp + TPF + k * DPF, tl, PMIN);
          const bool second = k > 3 || (k == 3 && m >= 83);
          pf[4 + k] = __builtin_amdgcn_raw_buffer_load_b32(mkrs(second ? w3 : w1, D * FF * 2), band, (second ? m - 83 : m) * (7 * FF * 2), 0);
        } else pf[4 + k] = 0;
        SCHED();
      }
      if (!isY) lds_add<F_BI>(FL);
      lds_wait<F_OCNT, 1>(FL, 5);
      lds_add<F_PF>(FL);
#pragma unroll
      for (int k = 0; k < 12; ++k) asm volatile("" ::"v"(pf[k]));
    }
    yt = w < 15 ? w - 6 : -1;
  }
  if (isY) {
    if (yt >= 0) {
      const GAS char* wp = (const GAS char*)((lane & 4) ? w3 : w1) + (size_t)((yt * 64 + (r16 >> 3) * 32 + g4 * 8) * (FF * 2) + (yi * 32 + (lane & 3) * 8) * 2);
      const int ao = WS_AP + (yt * 8 + (r16 >> 3)) * 512 + g4 * 128 + (lane & 7) * 16;
      int ypo = sel(r16 < 8, L_R + yt * 1024 + ((lane & 3) * 16 + g4 * 4 + (r16 >> 2) * 2) * 16, L_DUMMY + lane * 32);
      int sto = sel(lane == 0 || lane == 8 || lane == 20 || lane == 24, L_ST + ((r16 == 8 ? 0 : 2) + g4) * 64 + yt * 4, L_DUMMY + lane * 32);
      asm volatile("" : "+v"(ypo), "+v"(sto), "+v"(wp));
      v2u xv = __builtin_amdgcn_raw_buffer_load_b64(rx, (((lane >> 2) & 1) * D + yt * 64 + (lane & 1) * 32 + g4 * 8 + ((lane >> 1) & 1) * 4) * 2, 0, 0);
      SCHED();
      lds_wait<F_INOK, 2>(FL, 1);
      const u32 tr = TREF();
      lds_wait<F_PF, 1>(FL, 1);
#pragma unroll
      for (int i = 0; i < 8; ++i) t[i] = __builtin_nontemporal_load((const GAS v4u*)(wp + i * (FF * 2)));
      SCHED();
      lds_add<F_BI>(FL);
      const bool ride = (u32)__builtin_amdgcn_s_memrealtime() - tr >= (u32)TAP;
      const int ar = ride ? ao : OOB;
      v4u p[4] = {LD128(ar, 0), LD128(ar + 1024, 0), LD128(ar + 2048, 0), LD128(ar + 3072, 0)};
      SCHED();
      v4u f[8];
      tr8(t, f);
      PIN8(f);
      if (!ride) p[0] = p[1] = p[2] = p[3] = v4u{SENT, SENT, SENT, SENT};
      f4 hs;
      NOUNROLL for (int n = 0; n < SPIN_GLB; ++n) {
        hs = (asf4(p[0]) + asf4(p[1])) + (asf4(p[2]) + asf4(p[3]));
        if (!any_nan(hs)) break;
        u64 m[4];
#pragma unroll
        for (int k = 0; k < 4; ++k) m[k] = sent_mask(mx4(p[k], 0));
        if ((m[0] | m[1] | m[2] | m[3]) == 0) break;
        if (n == 0) { pace<1>(tr, TAP); if (ride) lds_wait<F_BI, 1>(FL, 9); }
        else __builtin_amdgcn_s_sleep(3);
        asm volatile("s_mov_b64 exec, %[m0]\n\tbuffer_load_dwordx4 %0, %[off], %[rs], 0 offen sc1\n\t"
                     "s_mov_b64 exec, %[m1]\n\tbuffer_load_dwordx4 %1, %[off], %[rs], 0 offen offset:1024 sc1\n\t"
                     "s_mov_b64 exec, %[m2]\n\tbuffer_load_dwordx4 %2, %[off], %[rs], 0 offen offset:2048 sc1\n\t"
                     "s_mov_b64 exec, %[m3]\n\tbuffer_load_dwordx4 %3, %[off], %[rs], 0 offen offset:3072 sc1\n\t" RELD_END
                     : "+v"(p[0]), "+v"(p[1]), "+v"(p[2]), "+v"(p[3])
                     : [off] "v"(ao), [rs] "s"(rws), [m0] "s"(m[0]), [m1] "s"(m[1]), [m2] "s"(m[2]), [m3] "s"(m[3])
                     : "memory");
      }
      float h[4];
#pragma unroll
      for (int j = 0; j < 4; ++j) h[j] = hs[j] + dpp<ROR8>(hs[j]);
      const u32 c0 = cvtpk(h[0], h[1]), c1 = cvtpk(h[2], h[3]);
      const u32 h0 = cvtpk(blo(xv[0]) + blo(c0), bhi(xv[0]) + bhi(c0)), h1 = cvtpk(blo(xv[1]) + blo(c1), bhi(xv[1]) + bhi(c1));
      const u32 h2 = dppu<XOR2>(h0), h3 = dppu<XOR2>(h1);
      const bool one = lane & 8;
      const v4u a = {one ? 0x3F803F80u : h0, one ? 0x3F803F80u : h1, one ? 0x3F803F80u : h2, one ? 0x3F803F80u : h3};
      f4 acc[8];
#pragma unroll
      for (int j = 0; j < 8; ++j) acc[j] = mfma(a, f[j], f4{0, 0, 0, 0});
      const f4 gm = mfma(a, a, f4{0, 0, 0, 0});
      float o[8];
#pragma unroll
      for (int j = 0; j < 8; ++j) o[j] = acc[j][0] + dpp<ROR8>(acc[j][1]);
      *(LAS f4*)(L + ypo) = f4{o[0], o[1], o[2], o[3]};
      *(LAS f4*)(L + ypo + 16) = f4{o[4], o[5], o[6], o[7]};
      *(LAS float*)(L + sto) = gm[0] + dpp<SHL1>(gm[1]);
      lds_add<F_RCNT>(FL);
    } else if (v2 >= 0) {
      const rsrc_t r2 = mkrs(w2, FF * D * 2);
      const int vo = sel(v2 < 4 || r16 < 8, (yi * 32 + g4 * 8) * (D * 2) + (v2 * 128 + r16 * 8) * 2, OOB);
      int mo = sel(g4 < 2 && (v2 < 4 || r16 < 8), WS_MP + (g4 * 18 + v2 * 4 + (r16 >> 3) * 2) * 18432 + yi * 128 + (lane & 7) * 16, OOB);
      int sto = sel(r16 < 9, L_ST + lane * 4, L_FLAG + 4 * F_ZERO);
      asm volatile("" : "+v"(mo), "+v"(sto));
      lds_wait<F_BI, 1>(FL, 9);
      pace<1>(TREF(), TAP + OW2L);
#pragma unroll
      for (int i = 0; i < 8; ++i) t[i] = __builtin_amdgcn_raw_buffer_load_b128(r2, vo, i * (D * 2), AUX_NT);
      SCHED();
      v4u f[8];
      tr8(t, f);
      PIN8(f);
      lds_wait<F_RCNT, 1>(FL, 6);
      lds_wait<F_RCNT, 0>(FL, 9);
      float st = *(LAS float*)(L + sto);
      f4 y[9];
#pragma unroll
      for (int k = 0; k < 9; ++k) y[k] = *(LAS f4*)(L + L_R + k * 1024 + lane * 16);
      SCHED();
      const f4 s = (((y[0] + y[1]) + (y[2] + y[3])) + ((y[4] + y[5]) + (y[6] + y[7]))) + y[8];
      st += dpp<XOR1>(st); st += dpp<XOR2>(st); st += dpp<HMIR>(st); st += dpp<ROR8>(st);
      const float a0 = asf(__builtin_amdgcn_readlane(asu(st), 0)), a1 = asf(__builtin_amdgcn_readlane(asu(st), 16));
      const float q0 = asf(__builtin_amdgcn_readlane(asu(st), 32)), q1 = asf(__builtin_amdgcn_readlane(asu(st), 48));
      const float mean = ((lane & 4) ? a1 : a0) * (1.f / D);
      const float var = ((lane & 4) ? q1 : q0) * (1.f / D) - mean * mean;
      const float rs = __builtin_amdgcn_rsqf(fmaxf(var, 0.f) + 1e-5f);
      float hid[4];
#pragma unroll
      for (int j = 0; j < 4; ++j) {
        const float zv = rs * (s[j] - mean * dpp<ROR8>(s[j]));
        const float up = dpp<XOR2>(zv);
        hid[j] = zv * __builtin_amdgcn_rcpf(1.f + ex2(-1.4426950408889634f * zv)) * up;
      }
      const u32 h0 = cvtpk(hid[0], hid[1]), h1 = cvtpk(hid[2], hid[3]);
      const v4u a = {h0, h1, dppu<XOR1>(h0), dppu<XOR1>(h1)};
      f4 acc[8];
#pragma unroll
      for (int j = 0; j < 8; ++j) acc[j] = mfma(a, f[j], f4{0, 0, 0, 0});
      ST128(nosent4(f4{acc[0][0], acc[1][0], acc[2][0], acc[3][0]}), mo, 0);
      ST128(nosent4(f4{acc[4][0], acc[5][0], acc[6][0], acc[7][0]}), mo, 18432);
    }
  }
}

extern "C" void kernel_launch(void* const* d_in, const int* in_sizes, int n_in, void* d_out, int out_size, void* d_ws, size_t ws_size,
                              hipStream_t stream) {
  fused_decode<<<256, 1024, 0, stream>>>((const u16*)d_in[0], (const u16*)d_in[1], (const u16*)d_in[2], (const u16*)d_in[3],
                                         (const u16*)d_in[4], (const u16*)d_in[6], (const u16*)d_in[7], (const u16*)d_in[8], (u16*)d_out,
                                         (char*)d_ws);
}
```
